```python
import torch

def _softmax(x, axis=-1):
    return torch.softmax(x, dim=axis)

def _gelu(x):
    return torch.nn.functional.gelu(x, approximate='tanh')

def _silu(x):
    return torch.nn.functional.silu(x)

def _sigmoid(x):
    return torch.sigmoid(x)

def _layernorm(x, axis=-1, eps=1e-05):
    m = x.mean(dim=axis, keepdim=True)
    v = x.var(dim=axis, keepdim=True, unbiased=False)
    return (x - m) / torch.sqrt(v + eps)

def _scatter_add(tgt, idx, src, axis=0):
    return tgt.clone().index_add_(axis, idx, src)

def _segment_sum(src, seg, n):
    out = torch.zeros((n,) + src.shape[1:], dtype=src.dtype)
    return out.index_add_(0, seg, src)

def _pad(x, pw):
    flat = tuple((v for pair in reversed(pw) for v in pair))
    return torch.nn.functional.pad(x, flat)
kernel_name = 'recurrent_scan__memory_aug_recurrent_mem_tr__mixer_channel_mlp__prompt'

def setup_inputs():

    _w = lambda a: (lambda t: t.to(torch.bfloat16) if t.is_floating_point() else t)(torch.as_tensor(a))

    def _A_setup():
        B, S, Dm = (1, 2048, 512)
        H, Dh = (32, 128)
        M, seg = (8, 32)
        seg = min(seg, S)
        S = S // seg * seg
        return {'x': _w((lambda *s: torch.randn(*s, dtype=torch.bfloat16) * 0.05)(B, S, Dm)), 'mem0': _w((lambda *s: torch.randn(*s, dtype=torch.bfloat16) * 0.8)(B, M, Dm)), 'Wq': _w((lambda *s: torch.randn(*s, dtype=torch.bfloat16) * 0.8)(Dm, H, Dh) * Dm ** (-0.5)), 'Wk': _w((lambda *s: torch.randn(*s, dtype=torch.bfloat16) * 0.8)(Dm, H, Dh) * Dm ** (-0.5)), 'Wv': _w((lambda *s: torch.randn(*s, dtype=torch.bfloat16) * 0.8)(Dm, H, Dh) * Dm ** (-0.5)), 'Wo': _w((lambda *s: torch.randn(*s, dtype=torch.bfloat16) * 0.8)(H, Dh, Dm) * (H * Dh) ** (-0.5)), 'seg_len': seg}
    _A_in = _A_setup()

    def _B_setup():
        B, S, D, F = (1, 2048, 512, 1376)
        return {'x': _w((lambda *s: torch.randn(*s, dtype=torch.bfloat16) * 0.05)(B, S, D)), 'w1': _w((lambda *s: torch.randn(*s, dtype=torch.bfloat16) * 0.8)(D, F) * D ** (-0.5)), 'b1': _w((lambda *s: torch.randn(*s, dtype=torch.bfloat16) * 0.4)(F)), 'w2': _w((lambda *s: torch.randn(*s, dtype=torch.bfloat16) * 0.2)(F, D) * F ** (-0.5)), 'b2': _w((lambda *s: torch.randn(*s, dtype=torch.bfloat16) * 0.2)(D))}
    _B_in = _B_setup()
    return {'A_x': _A_in['x'], 'A_mem0': _A_in['mem0'], 'A_Wq': _A_in['Wq'], 'A_Wk': _A_in['Wk'], 'A_Wv': _A_in['Wv'], 'A_Wo': _A_in['Wo'], 'A_seg_len': _A_in['seg_len'], 'B_x': _B_in['x'], 'B_w1': _B_in['w1'], 'B_b1': _B_in['b1'], 'B_w2': _B_in['w2'], 'B_b2': _B_in['b2']}

def reference(A_x, A_mem0, A_Wq, A_Wk, A_Wv, A_Wo, A_seg_len, B_x, B_w1, B_b1, B_w2, B_b2):

    def _A_ref(x, mem0, Wq, Wk, Wv, Wo, seg_len):
        B, S, Dm = x.shape
        H, Dh = (Wq.shape[1], Wq.shape[2])
        M = mem0.shape[1]
        scale = Dh ** (-0.5)
        n_seg = S // seg_len
        L = M + seg_len
        idx = torch.arange(L)
        mask = (idx[:, None] < M) | (idx[:, None] >= idx[None, :])
        mem = mem0
        outs = []
        for s in range(n_seg):
            xs = x[:, s * seg_len:(s + 1) * seg_len, :].clone()
            h = torch.concatenate([mem, xs], axis=1)
            q = torch.einsum('btd,dhe->bhte', h, Wq)
            k = torch.einsum('btd,dhe->bhte', h, Wk)
            v = torch.einsum('btd,dhe->bhte', h, Wv)
            scores = torch.einsum('bhqe,bhke->bhqk', q, k) * scale
            scores = torch.where(mask[None, None], scores, -1000000000.0)
            p = _softmax(scores, axis=-1)
            o = torch.einsum('bhqk,bhke->bhqe', p, v)
            o = torch.einsum('bhte,hed->btd', o, Wo)
            mem = o[:, :M, :].clone()
            outs.append(o[:, M:, :])
        return torch.concatenate(outs, axis=1)

    def _B_ref(x, w1, b1, w2, b2):
        y = _layernorm(x, axis=-1)
        h = _gelu(torch.einsum('bsd,df->bsf', y, w1) + b1)
        return torch.einsum('bsf,fd->bsd', h, w2) + b2
    x = A_x
    xn = _layernorm(x, axis=-1)
    s = _A_ref(xn, A_mem0, A_Wq, A_Wk, A_Wv, A_Wo, A_seg_len)
    g = _silu(_B_ref(xn, B_w1, B_b1, B_w2, B_b2))
    return x + s * g
```

```cpp
#include <hip/hip_runtime.h>
#include <hip/hip_bf16.h>
#include <stdint.h>

typedef __attribute__((ext_vector_type(8))) short bf16x8;
typedef __attribute__((ext_vector_type(4))) short bf16x4;
typedef __attribute__((ext_vector_type(4))) float f32x4;
typedef __attribute__((ext_vector_type(4))) unsigned u32x4;
typedef __attribute__((ext_vector_type(2))) unsigned u32x2;
typedef unsigned short u16;

#define S_TOK 2048
#define DM 512
#define NH 32
#define DH 128
#define HD 4096
#define NMEM 8
#define SEG 32
#define NSEG 64
#define NSTEPS 64
#define FF 1376
#define FFP 1408

__device__ __forceinline__ u16 f2bf(float f) { return __builtin_bit_cast(u16, (__bf16)f); }
__device__ __forceinline__ float bf2f(u16 h) { return __builtin_bit_cast(float, (unsigned)h << 16); }
__device__ __forceinline__ float rbf(float f) { return bf2f(f2bf(f)); }
__device__ __forceinline__ unsigned pack2(float a, float b) { return (unsigned)f2bf(a) | ((unsigned)f2bf(b) << 16); }

struct WS {
    u16 *xn, *y, *WqkvT, *WoT, *W1T, *W2T, *Q, *K, *VT, *hmid, *g, *Ox, *km, *vmT;
    unsigned *exA, *exB;
};

__device__ __forceinline__ float wave_sum(float v) {
#pragma unroll
    for (int m = 32; m >= 1; m >>= 1) v += __shfl_xor(v, m);
    return v;
}
__device__ __forceinline__ void ln8(float (&v)[8]) {
    float s = 0.f;
#pragma unroll
    for (int i = 0; i < 8; ++i) s += v[i];
    s = wave_sum(s);
    float mean = s * (1.f / 512.f);
    float q = 0.f;
#pragma unroll
    for (int i = 0; i < 8; ++i) { float d = v[i] - mean; q += d * d; }
    q = wave_sum(q);
    float var = rbf(q * (1.f / 512.f));
    float mb = rbf(mean);
    float sd = rbf(sqrtf(rbf(var + 1e-5f)));
#pragma unroll
    for (int i = 0; i < 8; ++i) v[i] = rbf(rbf(v[i] - mb) / sd);
}

__global__ void __launch_bounds__(256) prep_ln(const u16* __restrict__ x, u16* __restrict__ xn, u16* __restrict__ y) {
    int row = blockIdx.x * 4 + (threadIdx.x >> 6);
    int lane = threadIdx.x & 63;
    u32x4 r = *(const u32x4*)(x + (size_t)row * DM + lane * 8);
    float v[8];
#pragma unroll
    for (int i = 0; i < 4; ++i) { v[2 * i] = bf2f(r[i] & 0xffff); v[2 * i + 1] = bf2f(r[i] >> 16); }
    ln8(v);
    u32x4 o;
#pragma unroll
    for (int i = 0; i < 4; ++i) o[i] = pack2(v[2 * i], v[2 * i + 1]);
    *(u32x4*)(xn + (size_t)row * DM + lane * 8) = o;
    ln8(v);
#pragma unroll
    for (int i = 0; i < 4; ++i) o[i] = pack2(v[2 * i], v[2 * i + 1]);
    *(u32x4*)(y + (size_t)row * DM + lane * 8) = o;
}

__global__ void __launch_bounds__(256) transpose_k(const u16* __restrict__ src, u16* __restrict__ dst, int R, int C, int ldd, int Cpad) {
    __shared__ u16 t[64][66];
    int c0 = blockIdx.x * 64, r0 = blockIdx.y * 64;
    int tx = threadIdx.x & 63, ty = threadIdx.x >> 6;
    for (int i = ty; i < 64; i += 4) {
        int r = r0 + i, c = c0 + tx;
        t[i][tx] = (r < R && c < C) ? src[(size_t)r * C + c] : (u16)0;
    }
    __syncthreads();
    for (int i = ty; i < 64; i += 4) {
        int c = c0 + i, r = r0 + tx;
        if (c < Cpad && r < ldd) dst[(size_t)c * ldd + r] = t[tx][i];
    }
}

__global__ void zero_k(unsigned* p, int n) {
    int i = blockIdx.x * blockDim.x + threadIdx.x;
    if (i < n) p[i] = 0;
}

enum { EPI_QKV = 0, EPI_H = 1, EPI_G = 2, EPI_OUT = 3 };
struct EpiArgs {
    u16* o0; u16* o1; u16* o2;
    const u16* bias; const u16* x; const u16* g;
};

__device__ __forceinline__ float gelu_tanh(float v) {
    float u = 0.7978845608028654f * (v + 0.044715f * v * v * v);
    return 0.5f * v * (1.f + tanhf(u));
}

template <int EPI>
__global__ void __launch_bounds__(256) gemm_k(const u16* __restrict__ A, const u16* __restrict__ Bt, int lda, int ldb, int K, EpiArgs ea) {
    __shared__ __attribute__((aligned(16))) char lds[2][2][128 * 128];
    const int tid = threadIdx.x, lane = tid & 63, w = tid >> 6;
    const int wr = w >> 1, wc = w & 1;
    const int brow = blockIdx.y * 128, bcol = blockIdx.x * 128;
    const int fr = lane & 15, fg = lane >> 4;
    f32x4 acc[4][4];
#pragma unroll
    for (int i = 0; i < 4; ++i)
#pragma unroll
        for (int j = 0; j < 4; ++j) acc[i][j] = (f32x4){0.f, 0.f, 0.f, 0.f};
    const int srow = tid >> 3, sslot = tid & 7;
    const u16* ga = A + (size_t)(brow + srow) * lda + sslot * 8;
    const u16* gb = Bt + (size_t)(bcol + srow) * ldb + sslot * 8;
    u32x4 ra[4], rb[4];
    auto gload = [&](int kt) {
#pragma unroll
        for (int i = 0; i < 4; ++i) {
            ra[i] = *(const u32x4*)(ga + (size_t)(32 * i) * lda + kt * 64);
            rb[i] = *(const u32x4*)(gb + (size_t)(32 * i) * ldb + kt * 64);
        }
    };
    auto swrite = [&](int buf) {
#pragma unroll
        for (int i = 0; i < 4; ++i) {
            int r = srow + 32 * i;
            int off = r * 128 + ((sslot ^ (r & 7)) << 4);
            *(u32x4*)(&lds[buf][0][off]) = ra[i];
            *(u32x4*)(&lds[buf][1][off]) = rb[i];
        }
    };
    const int nt = K / 64;
    gload(0);
    swrite(0);
    __syncthreads();
    const bool vtile = (EPI == EPI_QKV) && (bcol >= 2 * HD);
    for (int kt = 0; kt < nt; ++kt) {
        int buf = kt & 1;
        if (kt + 1 < nt) gload(kt + 1);
#pragma unroll
        for (int kk = 0; kk < 2; ++kk) {
            bf16x8 af[4], bfr[4];
#pragma unroll
            for (int i = 0; i < 4; ++i) {
                int r = wr * 64 + i * 16 + fr;
                af[i] = *(const bf16x8*)(&lds[buf][0][r * 128 + (((kk * 4 + fg) ^ (r & 7)) << 4)]);
                int c = wc * 64 + i * 16 + fr;
                bfr[i] = *(const bf16x8*)(&lds[buf][1][c * 128 + (((kk * 4 + fg) ^ (c & 7)) << 4)]);
            }
            if (vtile) {
#pragma unroll
                for (int i = 0; i < 4; ++i)
#pragma unroll
                    for (int j = 0; j < 4; ++j) acc[i][j] = __builtin_amdgcn_mfma_f32_16x16x32_bf16(af[i], bfr[j], acc[i][j], 0, 0, 0);
            } else {
#pragma unroll
                for (int i = 0; i < 4; ++i)
#pragma unroll
                    for (int j = 0; j < 4; ++j) acc[i][j] = __builtin_amdgcn_mfma_f32_16x16x32_bf16(bfr[j], af[i], acc[i][j], 0, 0, 0);
            }
        }
        if (kt + 1 < nt) swrite(buf ^ 1);
        __syncthreads();
    }
#pragma unroll
    for (int i = 0; i < 4; ++i)
#pragma unroll
        for (int j = 0; j < 4; ++j) {
            f32x4 a = acc[i][j];
            if (vtile) {
                int col = bcol + wc * 64 + j * 16 + fr - 2 * HD;
                int tok = brow + wr * 64 + i * 16 + fg * 4;
                u32x2 o = {pack2(a[0], a[1]), pack2(a[2], a[3])};
                *(u32x2*)(ea.o2 + (size_t)col * S_TOK + tok) = o;
            } else {
                int tok = brow + wr * 64 + i * 16 + fr;
                int col = bcol + wc * 64 + j * 16 + fg * 4;
                if (EPI == EPI_QKV) {
                    u16* dst = (col >= HD) ? (ea.o1 + (size_t)tok * HD + (col - HD)) : (ea.o0 + (size_t)tok * HD + col);
                    u32x2 o = {pack2(a[0], a[1]), pack2(a[2], a[3])};
                    *(u32x2*)dst = o;
                } else if (EPI == EPI_H) {
                    float r[4];
#pragma unroll
                    for (int q = 0; q < 4; ++q) {
                        float b = (col + q < FF) ? bf2f(ea.bias[col + q]) : 0.f;
                        float v = rbf(rbf(a[q]) + b);
                        r[q] = (col + q < FF) ? gelu_tanh(v) : 0.f;
                    }
                    u32x2 o = {pack2(r[0], r[1]), pack2(r[2], r[3])};
                    *(u32x2*)(ea.o0 + (size_t)tok * FFP + col) = o;
                } else if (EPI == EPI_G) {
                    float r[4];
#pragma unroll
                    for (int q = 0; q < 4; ++q) {
                        float v = rbf(rbf(a[q]) + bf2f(ea.bias[col + q]));
                        r[q] = v / (1.f + __expf(-v));
                    }
                    u32x2 o = {pack2(r[0], r[1]), pack2(r[2], r[3])};
                    *(u32x2*)(ea.o0 + (size_t)tok * DM + col) = o;
                } else {
                    u32x2 xv = *(const u32x2*)(ea.x + (size_t)tok * DM + col);
                    u32x2 gv = *(const u32x2*)(ea.g + (size_t)tok * DM + col);
                    float xs[4] = {bf2f(xv[0] & 0xffff), bf2f(xv[0] >> 16), bf2f(xv[1] & 0xffff), bf2f(xv[1] >> 16)};
                    float gs[4] = {bf2f(gv[0] & 0xffff), bf2f(gv[0] >> 16), bf2f(gv[1] & 0xffff), bf2f(gv[1] >> 16)};
                    float r[4];
#pragma unroll
                    for (int q = 0; q < 4; ++q) r[q] = xs[q] + rbf(rbf(a[q]) * gs[q]);
                    u32x2 o = {pack2(r[0], r[1]), pack2(r[2], r[3])};
                    *(u32x2*)(ea.o0 + (size_t)tok * DM + col) = o;
                }
            }
        }
}

#define SCALE 0.08838834764831845f
#define MEM_STRIDE 1056
#define QK_STRIDE 288

typedef __amdgpu_buffer_rsrc_t rsrc_t;
__device__ __forceinline__ rsrc_t mk_rsrc(const void* p) { return __builtin_amdgcn_make_buffer_rsrc((void*)p, 0, 0x7fffffff, 0x00020000); }
__device__ __forceinline__ u32x4 bld16(rsrc_t r, unsigned vo, unsigned so) { return __builtin_amdgcn_raw_buffer_load_b128(r, vo, so, 0); }
__device__ __forceinline__ u32x4 bld16_sc1(rsrc_t r, unsigned vo, unsigned so) { return __builtin_amdgcn_raw_buffer_load_b128(r, vo, so, 16); }
__device__ __forceinline__ u32x2 bld8(rsrc_t r, unsigned vo, unsigned so) { return __builtin_amdgcn_raw_buffer_load_b64(r, vo, so, 0); }
__device__ __forceinline__ void bst16_sc1(rsrc_t r, unsigned vo, unsigned so, u32x4 v) { __builtin_amdgcn_raw_buffer_store_b128(v, r, vo, so, 16); }
__device__ __forceinline__ void bst8(rsrc_t r, unsigned vo, unsigned so, u32x2 v) { __builtin_amdgcn_raw_buffer_store_b64(v, r, vo, so, 0); }

__global__ void __launch_bounds__(256, 1) chain_k(const u16* __restrict__ mem0, WS ws) {
    extern __shared__ __attribute__((aligned(16))) char smem[];
    char* sWo = smem;
    char* sMem = smem + 131072;
    char* sQK = sMem + 8 * MEM_STRIDE;
    char* sO = sQK + 16 * QK_STRIDE;
    const int h = blockIdx.x;
    const int tid = threadIdx.x, lane = tid & 63;
    const int w = __builtin_amdgcn_readfirstlane(tid >> 6);
    const int fr = lane & 15, fg = lane >> 4, fr8 = lane & 7;

    bf16x8 wq[6][16];
#pragma unroll
    for (int t = 0; t < 6; ++t) {
        int col = (t >> 1) * HD + h * DH + w * 32 + (t & 1) * 16 + fr;
        const u16* p = ws.WqkvT + (size_t)col * DM + fg * 8;
#pragma unroll
        for (int ks = 0; ks < 16; ++ks) wq[t][ks] = *(const bf16x8*)(p + ks * 32);
    }
    for (int i = w; i < 128; i += 4) {
        int t = i >> 2, ks = i & 3;
        bf16x8 v = *(const bf16x8*)(ws.WoT + (size_t)(t * 16 + fr) * HD + h * DH + ks * 32 + fg * 8);
        *(bf16x8*)(sWo + (size_t)i * 1024 + lane * 16) = v;
    }
    const rsrc_t rA = mk_rsrc(ws.exA), rB = mk_rsrc(ws.exB), rK = mk_rsrc(ws.K), rV = mk_rsrc(ws.VT), rKm = mk_rsrc(ws.km), rVm = mk_rsrc(ws.vmT);
    const unsigned l16 = lane * 16;
    const unsigned kx_vo = (unsigned)fr * (HD * 2) + fg * 16;
    const unsigned vx_vo = (unsigned)fr * (S_TOK * 2) + fg * 8;
    const unsigned stA_vo = (unsigned)fr * 64 + fg * 16;
    const unsigned rdA_vo = (unsigned)(lane >> 3) * 512 + (lane & 7) * 16;

    for (int s = 0; s < NSTEPS; ++s) {
        if (s == 0) {
            for (int i = tid; i < 8 * 64; i += 256) {
                int row = i >> 6, c = (i & 63) * 8;
                *(u32x4*)(sMem + row * MEM_STRIDE + c * 2) = *(const u32x4*)(mem0 + row * DM + c);
            }
        } else {
            const unsigned tag = (unsigned)s;
            u32x4 vv[4];
            for (unsigned spin = 0;; ++spin) {
                if (spin > (1u << 20)) break;
                asm volatile("" ::: "memory");
#pragma unroll
                for (int i = 0; i < 4; ++i) vv[i] = bld16_sc1(rB, l16, (unsigned)(i * 4096 + w * 1024));
                bool ok = true;
#pragma unroll
                for (int i = 0; i < 4; ++i)
#pragma unroll
                    for (int q = 0; q < 4; ++q) ok &= ((vv[i][q] & 0xffff) == tag);
                if (__all(ok)) break;
            }
#pragma unroll
            for (int i = 0; i < 4; ++i) {
                int row = 2 * i + (w >> 1), c = ((w & 1) * 64 + lane) * 4;
                u32x2 o = {(vv[i][0] >> 16) | (vv[i][1] & 0xffff0000u), (vv[i][2] >> 16) | (vv[i][3] & 0xffff0000u)};
                *(u32x2*)(sMem + row * MEM_STRIDE + c * 2) = o;
            }
        }
        __syncthreads();
        f32x4 acc[6];
#pragma unroll
        for (int t = 0; t < 6; ++t) acc[t] = (f32x4){0.f, 0.f, 0.f, 0.f};
#pragma unroll
        for (int ks = 0; ks < 16; ++ks) {
            bf16x8 mb = *(const bf16x8*)(sMem + fr8 * MEM_STRIDE + (ks * 32 + fg * 8) * 2);
#pragma unroll
            for (int t = 0; t < 4; ++t) acc[t] = __builtin_amdgcn_mfma_f32_16x16x32_bf16(wq[t][ks], mb, acc[t], 0, 0, 0);
        }
        if (fr < 8) {
#pragma unroll
            for (int t = 0; t < 4; ++t) {
                int row = (t >> 1) * 8 + fr;
                int c = w * 32 + (t & 1) * 16 + fg * 4;
                u32x2 o = {pack2(acc[t][0], acc[t][1]), pack2(acc[t][2], acc[t][3])};
                *(u32x2*)(sQK + row * QK_STRIDE + c * 2) = o;
                if (t >= 2) bst8(rKm, (unsigned)fr * 256 + fg * 8, (unsigned)((s * NH + h) * 2048 + (w * 32 + (t & 1) * 16) * 2), o);
            }
        }
        u32x4 kx[2][4];
#pragma unroll
        for (int t = 0; t < 2; ++t)
#pragma unroll
            for (int ks = 0; ks < 4; ++ks) kx[t][ks] = bld16(rK, kx_vo, (unsigned)((s * SEG + t * 16) * (HD * 2) + h * 256 + ks * 64));
        u32x2 vx[2][2];
#pragma unroll
        for (int t = 0; t < 2; ++t) {
            unsigned so = (unsigned)((h * DH + w * 32 + t * 16) * (S_TOK * 2) + s * 64);
            vx[t][0] = bld8(rV, vx_vo, so);
            vx[t][1] = bld8(rV, vx_vo, so + 32);
        }
#pragma unroll
        for (int ks = 0; ks < 16; ++ks) {
            bf16x8 mb = *(const bf16x8*)(sMem + fr8 * MEM_STRIDE + (ks * 32 + fg * 8) * 2);
#pragma unroll
            for (int t = 4; t < 6; ++t) acc[t] = __builtin_amdgcn_mfma_f32_16x16x32_bf16(mb, wq[t][ks], acc[t], 0, 0, 0);
        }
        u32x2 vm[2];
#pragma unroll
        for (int t = 0; t < 2; ++t) {
            vm[t] = (u32x2){pack2(acc[4 + t][0], acc[4 + t][1]), pack2(acc[4 + t][2], acc[4 + t][3])};
            if (fg < 2) bst8(rVm, (unsigned)fr * 16 + fg * 8, (unsigned)((s * NH + h) * 2048 + (w * 32 + t * 16) * 16), vm[t]);
            else vm[t] = (u32x2){0u, 0u};
        }
        __syncthreads();
        f32x4 sm = {0.f, 0.f, 0.f, 0.f}, sx0 = sm, sx1 = sm;
#pragma unroll
        for (int ks = 0; ks < 4; ++ks) {
            bf16x8 qf = *(const bf16x8*)(sQK + fr8 * QK_STRIDE + (ks * 32 + fg * 8) * 2);
            bf16x8 kf = *(const bf16x8*)(sQK + (8 + fr8) * QK_STRIDE + (ks * 32 + fg * 8) * 2);
            sm = __builtin_amdgcn_mfma_f32_16x16x32_bf16(kf, qf, sm, 0, 0, 0);
            sx0 = __builtin_amdgcn_mfma_f32_16x16x32_bf16(__builtin_bit_cast(bf16x8, kx[0][ks]), qf, sx0, 0, 0, 0);
            sx1 = __builtin_amdgcn_mfma_f32_16x16x32_bf16(__builtin_bit_cast(bf16x8, kx[1][ks]), qf, sx1, 0, 0, 0);
        }
        float sv[12];
#pragma unroll
        for (int q = 0; q < 4; ++q) {
            sv[q] = (fg < 2) ? rbf(rbf(sm[q]) * SCALE) : -3.0e38f;
            sv[4 + q] = rbf(rbf(sx0[q]) * SCALE);
            sv[8 + q] = rbf(rbf(sx1[q]) * SCALE);
        }
        float mx = sv[0];
#pragma unroll
        for (int q = 1; q < 12; ++q) mx = fmaxf(mx, sv[q]);
        mx = fmaxf(mx, __shfl_xor(mx, 16));
        mx = fmaxf(mx, __shfl_xor(mx, 32));
        float sum = 0.f;
#pragma unroll
        for (int q = 0; q < 12; ++q) { sv[q] = __expf(sv[q] - mx); sum += sv[q]; }
        sum += __shfl_xor(sum, 16);
        sum += __shfl_xor(sum, 32);
        float inv = 1.f / sum;
        u32x4 px = {pack2(sv[4] * inv, sv[5] * inv), pack2(sv[6] * inv, sv[7] * inv), pack2(sv[8] * inv, sv[9] * inv), pack2(sv[10] * inv, sv[11] * inv)};
        u32x4 pm = {pack2(sv[0] * inv, sv[1] * inv), pack2(sv[2] * inv, sv[3] * inv), 0u, 0u};
#pragma unroll
        for (int t = 0; t < 2; ++t) {
            u32x4 ax = {vx[t][0][0], vx[t][0][1], vx[t][1][0], vx[t][1][1]};
            u32x4 am = {vm[t][0], vm[t][1], 0u, 0u};
            f32x4 o = {0.f, 0.f, 0.f, 0.f};
            o = __builtin_amdgcn_mfma_f32_16x16x32_bf16(__builtin_bit_cast(bf16x8, ax), __builtin_bit_cast(bf16x8, px), o, 0, 0, 0);
            o = __builtin_amdgcn_mfma_f32_16x16x32_bf16(__builtin_bit_cast(bf16x8, am), __builtin_bit_cast(bf16x8, pm), o, 0, 0, 0);
            if (fr < 8) {
                u32x2 ov = {pack2(o[0], o[1]), pack2(o[2], o[3])};
                *(u32x2*)(sO + fr * QK_STRIDE + (w * 32 + t * 16 + fg * 4) * 2) = ov;
            }
        }
        __syncthreads();
        bf16x8 of[4];
#pragma unroll
        for (int ks = 0; ks < 4; ++ks) of[ks] = *(const bf16x8*)(sO + fr8 * QK_STRIDE + (ks * 32 + fg * 8) * 2);
        const unsigned tagA = (unsigned)((s + 1) & 1);
#pragma unroll
        for (int t = 0; t < 8; ++t) {
            int tile = w * 8 + t;
            f32x4 a = {0.f, 0.f, 0.f, 0.f};
#pragma unroll
            for (int ks = 0; ks < 4; ++ks) {
                bf16x8 wf = *(const bf16x8*)(sWo + (size_t)(tile * 4 + ks) * 1024 + l16);
                a = __builtin_amdgcn_mfma_f32_16x16x32_bf16(wf, of[ks], a, 0, 0, 0);
            }
            if (fr < 8) {
                u32x4 o;
#pragma unroll
                for (int q = 0; q < 4; ++q) o[q] = (__float_as_uint(a[q]) & ~1u) | tagA;
                bst16_sc1(rA, stA_vo, (unsigned)((tile * NH + h) * 512), o);
            }
        }
        {
            float tot[4];
            for (unsigned spin = 0;; ++spin) {
                if (spin > (1u << 20)) break;
                asm volatile("" ::: "memory");
                u32x4 v[4];
#pragma unroll
                for (int i = 0; i < 4; ++i) v[i] = bld16_sc1(rA, rdA_vo, (unsigned)(h * NH * 512 + i * 4096 + w * 128));
                bool ok = true;
#pragma unroll
                for (int q = 0; q < 4; ++q) {
                    ok &= ((v[0][q] & 1u) == tagA) & ((v[1][q] & 1u) == tagA) & ((v[2][q] & 1u) == tagA) & ((v[3][q] & 1u) == tagA);
                    tot[q] = (__uint_as_float(v[0][q] & ~1u) + __uint_as_float(v[1][q] & ~1u)) +
                             (__uint_as_float(v[2][q] & ~1u) + __uint_as_float(v[3][q] & ~1u));
                }
                if (__all(ok)) break;
            }
#pragma unroll
            for (int q = 0; q < 4; ++q) {
                tot[q] += __shfl_xor(tot[q], 8);
                tot[q] += __shfl_xor(tot[q], 16);
                tot[q] += __shfl_xor(tot[q], 32);
            }
            if (lane < 8) {
                u32x4 o;
#pragma unroll
                for (int q = 0; q < 4; ++q) o[q] = ((unsigned)f2bf(tot[q]) << 16) | (unsigned)(s + 1);
                bst16_sc1(rB, (unsigned)(lane >> 2) * 2048 + (lane & 3) * 16, (unsigned)(w * 4096 + h * 64), o);
            }
        }
    }
}

__global__ void __launch_bounds__(256) attn_k(WS ws) {
    const int lane = threadIdx.x & 63, w = threadIdx.x >> 6;
    const int s = blockIdx.x >> 3, h = (blockIdx.x & 7) * 4 + w;
    const int fr = lane & 15, fg = lane >> 4, fr8 = lane & 7;
    bf16x8 kx[2][4], kmf[4];
#pragma unroll
    for (int ks = 0; ks < 4; ++ks) {
        kmf[ks] = *(const bf16x8*)(ws.km + ((size_t)(s * NH + h) * 8 + fr8) * DH + ks * 32 + fg * 8);
#pragma unroll
        for (int t = 0; t < 2; ++t) kx[t][ks] = *(const bf16x8*)(ws.K + (size_t)(s * SEG + t * 16 + fr) * HD + h * DH + ks * 32 + fg * 8);
    }
    u32x4 px[2], pm[2];
#pragma unroll
    for (int qt = 0; qt < 2; ++qt) {
        f32x4 sm = {0.f, 0.f, 0.f, 0.f}, sx0 = sm, sx1 = sm;
#pragma unroll
        for (int ks = 0; ks < 4; ++ks) {
            bf16x8 qf = *(const bf16x8*)(ws.Q + (size_t)(s * SEG + qt * 16 + fr) * HD + h * DH + ks * 32 + fg * 8);
            sm = __builtin_amdgcn_mfma_f32_16x16x32_bf16(kmf[ks], qf, sm, 0, 0, 0);
            sx0 = __builtin_amdgcn_mfma_f32_16x16x32_bf16(kx[0][ks], qf, sx0, 0, 0, 0);
            sx1 = __builtin_amdgcn_mfma_f32_16x16x32_bf16(kx[1][ks], qf, sx1, 0, 0, 0);
        }
        const int qi = qt * 16 + fr;
        float sv[12];
#pragma unroll
        for (int q = 0; q < 4; ++q) {
            sv[q] = (fg < 2) ? rbf(rbf(sm[q]) * SCALE) : -3.0e38f;
            int k0 = fg * 4 + q, k1 = 16 + fg * 4 + q;
            sv[4 + q] = (k0 <= qi) ? rbf(rbf(sx0[q]) * SCALE) : -3.0e38f;
            sv[8 + q] = (k1 <= qi) ? rbf(rbf(sx1[q]) * SCALE) : -3.0e38f;
        }
        float mx = sv[0];
#pragma unroll
        for (int q = 1; q < 12; ++q) mx = fmaxf(mx, sv[q]);
        mx = fmaxf(mx, __shfl_xor(mx, 16));
        mx = fmaxf(mx, __shfl_xor(mx, 32));
        float sum = 0.f;
#pragma unroll
        for (int q = 0; q < 12; ++q) { sv[q] = __expf(sv[q] - mx); sum += sv[q]; }
        sum += __shfl_xor(sum, 16);
        sum += __shfl_xor(sum, 32);
        float inv = 1.f / sum;
        px[qt] = (u32x4){pack2(sv[4] * inv, sv[5] * inv), pack2(sv[6] * inv, sv[7] * inv), pack2(sv[8] * inv, sv[9] * inv), pack2(sv[10] * inv, sv[11] * inv)};
        pm[qt] = (u32x4){pack2(sv[0] * inv, sv[1] * inv), pack2(sv[2] * inv, sv[3] * inv), 0u, 0u};
    }
#pragma unroll
    for (int et = 0; et < 8; ++et) {
        const u16* p = ws.VT + (size_t)(h * DH + et * 16 + fr) * S_TOK + s * SEG + fg * 4;
        u32x2 a0 = *(const u32x2*)p, a1 = *(const u32x2*)(p + 16);
        u32x2 m0 = {0u, 0u};
        if (fg < 2) m0 = *(const u32x2*)(ws.vmT + ((size_t)(s * NH + h) * DH + et * 16 + fr) * 8 + fg * 4);
        u32x4 ax = {a0[0], a0[1], a1[0], a1[1]};
        u32x4 am = {m0[0], m0[1], 0u, 0u};
#pragma unroll
        for (int qt = 0; qt < 2; ++qt) {
            f32x4 o = {0.f, 0.f, 0.f, 0.f};
            o = __builtin_amdgcn_mfma_f32_16x16x32_bf16(__builtin_bit_cast(bf16x8, ax), __builtin_bit_cast(bf16x8, px[qt]), o, 0, 0, 0);
            o = __builtin_amdgcn_mfma_f32_16x16x32_bf16(__builtin_bit_cast(bf16x8, am), __builtin_bit_cast(bf16x8, pm[qt]), o, 0, 0, 0);
            u32x2 ov = {pack2(o[0], o[1]), pack2(o[2], o[3])};
            *(u32x2*)(ws.Ox + (size_t)(s * SEG + qt * 16 + fr) * HD + h * DH + et * 16 + fg * 4) = ov;
        }
    }
}

extern "C" void kernel_launch(void* const* d_in, const int* in_sizes, int n_in, void* d_out, int out_size, void* d_ws, size_t ws_size,
                              hipStream_t stream) {
    const u16* x = (const u16*)d_in[0];
    const u16* mem0 = (const u16*)d_in[1];
    const u16* Wq = (const u16*)d_in[2];
    const u16* Wk = (const u16*)d_in[3];
    const u16* Wv = (const u16*)d_in[4];
    const u16* Wo = (const u16*)d_in[5];
    const u16* w1 = (const u16*)d_in[8];
    const u16* b1 = (const u16*)d_in[9];
    const u16* w2 = (const u16*)d_in[10];
    const u16* b2 = (const u16*)d_in[11];
    u16* out = (u16*)d_out;

    char* p = (char*)d_ws;
    auto carve = [&](size_t bytes) { char* r = p; p += (bytes + 255) & ~(size_t)255; return r; };
    WS ws;
    ws.exA = (unsigned*)carve((size_t)32 * 32 * 128 * 4);
    ws.exB = (unsigned*)carve((size_t)8 * 512 * 4);
    ws.xn = (u16*)carve((size_t)S_TOK * DM * 2);
    ws.y = (u16*)carve((size_t)S_TOK * DM * 2);
    ws.WqkvT = (u16*)carve((size_t)3 * HD * DM * 2);
    ws.WoT = (u16*)carve((size_t)DM * HD * 2);
    ws.W1T = (u16*)carve((size_t)FFP * DM * 2);
    ws.W2T = (u16*)carve((size_t)DM * FFP * 2);
    ws.Q = (u16*)carve((size_t)S_TOK * HD * 2);
    ws.K = (u16*)carve((size_t)S_TOK * HD * 2);
    ws.VT = (u16*)carve((size_t)S_TOK * HD * 2);
    ws.hmid = (u16*)carve((size_t)S_TOK * FFP * 2);
    ws.g = (u16*)carve((size_t)S_TOK * DM * 2);
    ws.Ox = (u16*)carve((size_t)S_TOK * HD * 2);
    ws.km = (u16*)carve((size_t)NSEG * NH * 8 * DH * 2);
    ws.vmT = (u16*)carve((size_t)NSEG * NH * 8 * DH * 2);

    zero_k<<<(32 * 32 * 128 + 8 * 512 + 64 + 255) / 256, 256, 0, stream>>>(ws.exA, 32 * 32 * 128 + 8 * 512);
    prep_ln<<<S_TOK / 4, 256, 0, stream>>>(x, ws.xn, ws.y);
    transpose_k<<<dim3(HD / 64, DM / 64), 256, 0, stream>>>(Wq, ws.WqkvT, DM, HD, DM, HD);
    transpose_k<<<dim3(HD / 64, DM / 64), 256, 0, stream>>>(Wk, ws.WqkvT + (size_t)HD * DM, DM, HD, DM, HD);
    transpose_k<<<dim3(HD / 64, DM / 64), 256, 0, stream>>>(Wv, ws.WqkvT + (size_t)2 * HD * DM, DM, HD, DM, HD);
    transpose_k<<<dim3(DM / 64, HD / 64), 256, 0, stream>>>(Wo, ws.WoT, HD, DM, HD, DM);
    transpose_k<<<dim3(FFP / 64, DM / 64), 256, 0, stream>>>(w1, ws.W1T, DM, FF, DM, FFP);
    transpose_k<<<dim3(DM / 64, FFP / 64), 256, 0, stream>>>(w2, ws.W2T, FF, DM, FFP, DM);

    EpiArgs ea;
    ea = EpiArgs{ws.Q, ws.K, ws.VT, nullptr, nullptr, nullptr};
    gemm_k<EPI_QKV><<<dim3(3 * HD / 128, S_TOK / 128), 256, 0, stream>>>(ws.xn, ws.WqkvT, DM, DM, DM, ea);
    hipFuncSetAttribute((const void*)chain_k, hipFuncAttributeMaxDynamicSharedMemorySize, 147456);
    chain_k<<<NH, 256, 147456, stream>>>(mem0, ws);
    ea = EpiArgs{ws.hmid, nullptr, nullptr, b1, nullptr, nullptr};
    gemm_k<EPI_H><<<dim3(FFP / 128, S_TOK / 128), 256, 0, stream>>>(ws.y, ws.W1T, DM, DM, DM, ea);
    ea = EpiArgs{ws.g, nullptr, nullptr, b2, nullptr, nullptr};
    gemm_k<EPI_G><<<dim3(DM / 128, S_TOK / 128), 256, 0, stream>>>(ws.hmid, ws.W2T, FFP, FFP, FFP, ea);
    attn_k<<<NSEG * 8, 256, 0, stream>>>(ws);
    ea = EpiArgs{out, nullptr, nullptr, nullptr, x, ws.g};
    gemm_k<EPI_OUT><<<dim3(DM / 128, S_TOK / 128), 256, 0, stream>>>(ws.Ox, ws.WoT, HD, HD, HD, ea);
}
```

```cpp
#include <hip/hip_runtime.h>
#include <hip/hip_bf16.h>
#include <stdint.h>

typedef __attribute__((ext_vector_type(8))) short bf16x8;
typedef __attribute__((ext_vector_type(4))) short bf16x4;
typedef __attribute__((ext_vector_type(4))) float f32x4;
typedef __attribute__((ext_vector_type(4))) unsigned u32x4;
typedef __attribute__((ext_vector_type(2))) unsigned u32x2;
typedef unsigned short u16;

#define S_TOK 2048
#define DM 512
#define NH 32
#define DH 128
#define HD 4096
#define NMEM 8
#define SEG 32
#define NSEG 64
#define NSTEPS 64
#define FF 1376
#define FFP 1408

__device__ __forceinline__ u16 f2bf(float f) { return __builtin_bit_cast(u16, (__bf16)f); }
__device__ __forceinline__ float bf2f(u16 h) { return __builtin_bit_cast(float, (unsigned)h << 16); }
__device__ __forceinline__ float rbf(float f) { return bf2f(f2bf(f)); }
__device__ __forceinline__ unsigned pack2(float a, float b) { return (unsigned)f2bf(a) | ((unsigned)f2bf(b) << 16); }

struct WS {
    u16 *xn, *y, *WqkvT, *WoT, *W1T, *W2T, *Q, *K, *VT, *hmid, *g, *Ox, *km, *vmT;
    unsigned *exA, *exB;
    unsigned long long* dbg;
};

__device__ __forceinline__ float wave_sum(float v) {
#pragma unroll
    for (int m = 32; m >= 1; m >>= 1) v += __shfl_xor(v, m);
    return v;
}
__device__ __forceinline__ void ln8(float (&v)[8]) {
    float s = 0.f;
#pragma unroll
    for (int i = 0; i < 8; ++i) s += v[i];
    s = wave_sum(s);
    float mean = s * (1.f / 512.f);
    float q = 0.f;
#pragma unroll
    for (int i = 0; i < 8; ++i) { float d = v[i] - mean; q += d * d; }
    q = wave_sum(q);
    float var = rbf(q * (1.f / 512.f));
    float mb = rbf(mean);
    float sd = rbf(sqrtf(rbf(var + 1e-5f)));
#pragma unroll
    for (int i = 0; i < 8; ++i) v[i] = rbf(rbf(v[i] - mb) / sd);
}

__global__ void __launch_bounds__(256) prep_ln(const u16* __restrict__ x, u16* __restrict__ xn, u16* __restrict__ y) {
    int row = blockIdx.x * 4 + (threadIdx.x >> 6);
    int lane = threadIdx.x & 63;
    u32x4 r = *(const u32x4*)(x + (size_t)row * DM + lane * 8);
    float v[8];
#pragma unroll
    for (int i = 0; i < 4; ++i) { v[2 * i] = bf2f(r[i] & 0xffff); v[2 * i + 1] = bf2f(r[i] >> 16); }
    ln8(v);
    u32x4 o;
#pragma unroll
    for (int i = 0; i < 4; ++i) o[i] = pack2(v[2 * i], v[2 * i + 1]);
    *(u32x4*)(xn + (size_t)row * DM + lane * 8) = o;
    ln8(v);
#pragma unroll
    for (int i = 0; i < 4; ++i) o[i] = pack2(v[2 * i], v[2 * i + 1]);
    *(u32x4*)(y + (size_t)row * DM + lane * 8) = o;
}

__global__ void __launch_bounds__(256) transpose_k(const u16* __restrict__ src, u16* __restrict__ dst, int R, int C, int ldd, int Cpad) {
    __shared__ u16 t[64][66];
    int c0 = blockIdx.x * 64, r0 = blockIdx.y * 64;
    int tx = threadIdx.x & 63, ty = threadIdx.x >> 6;
    for (int i = ty; i < 64; i += 4) {
        int r = r0 + i, c = c0 + tx;
        t[i][tx] = (r < R && c < C) ? src[(size_t)r * C + c] : (u16)0;
    }
    __syncthreads();
    for (int i = ty; i < 64; i += 4) {
        int c = c0 + i, r = r0 + tx;
        if (c < Cpad && r < ldd) dst[(size_t)c * ldd + r] = t[tx][i];
    }
}

__global__ void zero_k(unsigned* p, int n) {
    int i = blockIdx.x * blockDim.x + threadIdx.x;
    if (i < n) p[i] = 0;
}

enum { EPI_QKV = 0, EPI_H = 1, EPI_G = 2, EPI_OUT = 3 };
struct EpiArgs {
    u16* o0; u16* o1; u16* o2;
    const u16* bias; const u16* x; const u16* g;
};

__device__ __forceinline__ float gelu_tanh(float v) {
    float u = 0.7978845608028654f * (v + 0.044715f * v * v * v);
    return 0.5f * v * (1.f + tanhf(u));
}

template <int EPI>
__global__ void __launch_bounds__(256) gemm_k(const u16* __restrict__ A, const u16* __restrict__ Bt, int lda, int ldb, int K, EpiArgs ea) {
    __shared__ __attribute__((aligned(16))) char lds[2][2][128 * 128];
    const int tid = threadIdx.x, lane = tid & 63, w = tid >> 6;
    const int wr = w >> 1, wc = w & 1;
    const int brow = blockIdx.y * 128, bcol = blockIdx.x * 128;
    const int fr = lane & 15, fg = lane >> 4;
    f32x4 acc[4][4];
#pragma unroll
    for (int i = 0; i < 4; ++i)
#pragma unroll
        for (int j = 0; j < 4; ++j) acc[i][j] = (f32x4){0.f, 0.f, 0.f, 0.f};
    const int srow = tid >> 3, sslot = tid & 7;
    const u16* ga = A + (size_t)(brow + srow) * lda + sslot * 8;
    const u16* gb = Bt + (size_t)(bcol + srow) * ldb + sslot * 8;
    u32x4 ra[4], rb[4];
    auto gload = [&](int kt) {
#pragma unroll
        for (int i = 0; i < 4; ++i) {
            ra[i] = *(const u32x4*)(ga + (size_t)(32 * i) * lda + kt * 64);
            rb[i] = *(const u32x4*)(gb + (size_t)(32 * i) * ldb + kt * 64);
        }
    };
    auto swrite = [&](int buf) {
#pragma unroll
        for (int i = 0; i < 4; ++i) {
            int r = srow + 32 * i;
            int off = r * 128 + ((sslot ^ (r & 7)) << 4);
            *(u32x4*)(&lds[buf][0][off]) = ra[i];
            *(u32x4*)(&lds[buf][1][off]) = rb[i];
        }
    };
    const int nt = K / 64;
    gload(0);
    swrite(0);
    __syncthreads();
    const bool vtile = (EPI == EPI_QKV) && (bcol >= 2 * HD);
    for (int kt = 0; kt < nt; ++kt) {
        int buf = kt & 1;
        if (kt + 1 < nt) gload(kt + 1);
#pragma unroll
        for (int kk = 0; kk < 2; ++kk) {
            bf16x8 af[4], bfr[4];
#pragma unroll
            for (int i = 0; i < 4; ++i) {
                int r = wr * 64 + i * 16 + fr;
                af[i] = *(const bf16x8*)(&lds[buf][0][r * 128 + (((kk * 4 + fg) ^ (r & 7)) << 4)]);
                int c = wc * 64 + i * 16 + fr;
                bfr[i] = *(const bf16x8*)(&lds[buf][1][c * 128 + (((kk * 4 + fg) ^ (c & 7)) << 4)]);
            }
            if (vtile) {
#pragma unroll
                for (int i = 0; i < 4; ++i)
#pragma unroll
                    for (int j = 0; j < 4; ++j) acc[i][j] = __builtin_amdgcn_mfma_f32_16x16x32_bf16(af[i], bfr[j], acc[i][j], 0, 0, 0);
            } else {
#pragma unroll
                for (int i = 0; i < 4; ++i)
#pragma unroll
                    for (int j = 0; j < 4; ++j) acc[i][j] = __builtin_amdgcn_mfma_f32_16x16x32_bf16(bfr[j], af[i], acc[i][j], 0, 0, 0);
            }
        }
        if (kt + 1 < nt) swrite(buf ^ 1);
        __syncthreads();
    }
#pragma unroll
    for (int i = 0; i < 4; ++i)
#pragma unroll
        for (int j = 0; j < 4; ++j) {
            f32x4 a = acc[i][j];
            if (vtile) {
                int col = bcol + wc * 64 + j * 16 + fr - 2 * HD;
                int tok = brow + wr * 64 + i * 16 + fg * 4;
                u32x2 o = {pack2(a[0], a[1]), pack2(a[2], a[3])};
                *(u32x2*)(ea.o2 + (size_t)col * S_TOK + tok) = o;
            } else {
                int tok = brow + wr * 64 + i * 16 + fr;
                int col = bcol + wc * 64 + j * 16 + fg * 4;
                if (EPI == EPI_QKV) {
                    u16* dst = (col >= HD) ? (ea.o1 + (size_t)tok * HD + (col - HD)) : (ea.o0 + (size_t)tok * HD + col);
                    u32x2 o = {pack2(a[0], a[1]), pack2(a[2], a[3])};
                    *(u32x2*)dst = o;
                } else if (EPI == EPI_H) {
                    float r[4];
#pragma unroll
                    for (int q = 0; q < 4; ++q) {
                        float b = (col + q < FF) ? bf2f(ea.bias[col + q]) : 0.f;
                        float v = rbf(rbf(a[q]) + b);
                        r[q] = (col + q < FF) ? gelu_tanh(v) : 0.f;
                    }
                    u32x2 o = {pack2(r[0], r[1]), pack2(r[2], r[3])};
                    *(u32x2*)(ea.o0 + (size_t)tok * FFP + col) = o;
                } else if (EPI == EPI_G) {
                    float r[4];
#pragma unroll
                    for (int q = 0; q < 4; ++q) {
                        float v = rbf(rbf(a[q]) + bf2f(ea.bias[col + q]));
                        r[q] = v / (1.f + __expf(-v));
                    }
                    u32x2 o = {pack2(r[0], r[1]), pack2(r[2], r[3])};
                    *(u32x2*)(ea.o0 + (size_t)tok * DM + col) = o;
                } else {
                    u32x2 xv = *(const u32x2*)(ea.x + (size_t)tok * DM + col);
                    u32x2 gv = *(const u32x2*)(ea.g + (size_t)tok * DM + col);
                    float xs[4] = {bf2f(xv[0] & 0xffff), bf2f(xv[0] >> 16), bf2f(xv[1] & 0xffff), bf2f(xv[1] >> 16)};
                    float gs[4] = {bf2f(gv[0] & 0xffff), bf2f(gv[0] >> 16), bf2f(gv[1] & 0xffff), bf2f(gv[1] >> 16)};
                    float r[4];
#pragma unroll
                    for (int q = 0; q < 4; ++q) r[q] = xs[q] + rbf(rbf(a[q]) * gs[q]);
                    u32x2 o = {pack2(r[0], r[1]), pack2(r[2], r[3])};
                    *(u32x2*)(ea.o0 + (size_t)tok * DM + col) = o;
                }
            }
        }
}

#define SCALE 0.08838834764831845f
#define MEM_STRIDE 1056
#define QK_STRIDE 288

typedef __amdgpu_buffer_rsrc_t rsrc_t;
__device__ __forceinline__ rsrc_t mk_rsrc(const void* p) { return __builtin_amdgcn_make_buffer_rsrc((void*)p, 0, 0x7fffffff, 0x00020000); }
__device__ __forceinline__ u32x4 bld16(rsrc_t r, unsigned vo, unsigned so) { return __builtin_amdgcn_raw_buffer_load_b128(r, vo, so, 0); }
__device__ __forceinline__ u32x4 bld16_sc1(rsrc_t r, unsigned vo, unsigned so) { return __builtin_amdgcn_raw_buffer_load_b128(r, vo, so, 16); }
__device__ __forceinline__ u32x2 bld8(rsrc_t r, unsigned vo, unsigned so) { return __builtin_amdgcn_raw_buffer_load_b64(r, vo, so, 0); }
__device__ __forceinline__ void bst16_sc1(rsrc_t r, unsigned vo, unsigned so, u32x4 v) { __builtin_amdgcn_raw_buffer_store_b128(v, r, vo, so, 16); }
__device__ __forceinline__ void bst8(rsrc_t r, unsigned vo, unsigned so, u32x2 v) { __builtin_amdgcn_raw_buffer_store_b64(v, r, vo, so, 0); }

#define STAMP(i)
#define MFMA0_A(acc, a, b) asm volatile("v_mfma_f32_16x16x32_bf16 %0, %1, %2, 0" : "=&v"(acc) : "a"(a), "v"(b))
#define MFMA_A(acc, a, b) asm volatile("v_mfma_f32_16x16x32_bf16 %0, %1, %2, %0" : "+v"(acc) : "a"(a), "v"(b))
#define MFMA0_V(acc, a, b) asm volatile("v_mfma_f32_16x16x32_bf16 %0, %1, %2, 0" : "=&v"(acc) : "v"(a), "v"(b))
#define MFMA_V(acc, a, b) asm volatile("v_mfma_f32_16x16x32_bf16 %0, %1, %2, %0" : "+v"(acc) : "v"(a), "v"(b))
#define BARRIER() asm volatile("s_waitcnt lgkmcnt(0)\n\ts_barrier" ::: "memory")

__device__ __forceinline__ float xmax16(float x) {
    unsigned u = __float_as_uint(x);
    auto r = __builtin_amdgcn_permlane16_swap(u, u, false, false);
    return fmaxf(__uint_as_float(r[0]), __uint_as_float(r[1]));
}
__device__ __forceinline__ float xmax32(float x) {
    unsigned u = __float_as_uint(x);
    auto r = __builtin_amdgcn_permlane32_swap(u, u, false, false);
    return fmaxf(__uint_as_float(r[0]), __uint_as_float(r[1]));
}
__device__ __forceinline__ float xsum16(float x) {
    unsigned u = __float_as_uint(x);
    auto r = __builtin_amdgcn_permlane16_swap(u, u, false, false);
    return __uint_as_float(r[0]) + __uint_as_float(r[1]);
}
__device__ __forceinline__ float xsum32(float x) {
    unsigned u = __float_as_uint(x);
    auto r = __builtin_amdgcn_permlane32_swap(u, u, false, false);
    return __uint_as_float(r[0]) + __uint_as_float(r[1]);
}
__device__ __forceinline__ float xsum8(float x) {
    return x + __uint_as_float(__builtin_amdgcn_update_dpp(0u, __float_as_uint(x), 0x128, 0xf, 0xf, false));
}

__global__ void __launch_bounds__(256, 1) chain_k(const u16* __restrict__ mem0, WS ws) {
    extern __shared__ __attribute__((aligned(16))) char smem[];
    char* sWo = smem;
    char* sMem = smem + 131072;
    char* sQK = sMem + 8 * MEM_STRIDE;
    char* sO = sQK + 16 * QK_STRIDE;
    const int h = blockIdx.x;
    const int tid = threadIdx.x, lane = tid & 63;
    const int w = __builtin_amdgcn_readfirstlane(tid >> 6);
    const int fr = lane & 15, fg = lane >> 4, fr8 = lane & 7;

    bf16x8 wq[6][16];
#pragma unroll
    for (int t = 0; t < 6; ++t) {
        int col = (t >> 1) * HD + h * DH + w * 32 + (t & 1) * 16 + fr;
        const u16* p = ws.WqkvT + (size_t)col * DM + fg * 8;
#pragma unroll
        for (int ks = 0; ks < 16; ++ks) wq[t][ks] = *(const bf16x8*)(p + ks * 32);
    }
    for (int i = w; i < 128; i += 4) {
        int t = i >> 2, ks = i & 3;
        bf16x8 v = *(const bf16x8*)(ws.WoT + (size_t)(t * 16 + fr) * HD + h * DH + ks * 32 + fg * 8);
        *(bf16x8*)(sWo + (size_t)i * 1024 + lane * 16) = v;
    }
    const rsrc_t rA = mk_rsrc(ws.exA), rB = mk_rsrc(ws.exB), rK = mk_rsrc(ws.K), rV = mk_rsrc(ws.VT), rKm = mk_rsrc(ws.km), rVm = mk_rsrc(ws.vmT);
    const unsigned l16 = lane * 16;
    const unsigned kx_vo = (unsigned)fr * (HD * 2) + fg * 16;
    const unsigned vx_vo = (unsigned)fr * (S_TOK * 2) + fg * 8;
    const unsigned stA_vo = (unsigned)fr * 64 + fg * 16;
    const unsigned rdA_vo = (unsigned)(lane >> 3) * 512 + (lane & 7) * 16;
    const char* memrd = sMem + fr8 * MEM_STRIDE + fg * 16;
    const char* qrd = sQK + fr8 * QK_STRIDE + fg * 16;
    const char* ord_ = sO + fr8 * QK_STRIDE + fg * 16;
    const char* word = sWo + w * 32768 + l16;

    for (int s = 0; s < NSTEPS; ++s) {
        STAMP(0);
        u32x4 kx[2][4];
#pragma unroll
        for (int t = 0; t < 2; ++t)
#pragma unroll
            for (int ks = 0; ks < 4; ++ks) kx[t][ks] = bld16(rK, kx_vo, (unsigned)((s * SEG + t * 16) * (HD * 2) + h * 256 + ks * 64));
        u32x2 vx[2][2];
#pragma unroll
        for (int t = 0; t < 2; ++t) {
            unsigned so = (unsigned)((h * DH + w * 32 + t * 16) * (S_TOK * 2) + s * 64);
            vx[t][0] = bld8(rV, vx_vo, so);
            vx[t][1] = bld8(rV, vx_vo, so + 32);
        }
        if (s == 0) {
            for (int i = tid; i < 8 * 64; i += 256) {
                int row = i >> 6, c = (i & 63) * 8;
                *(u32x4*)(sMem + row * MEM_STRIDE + c * 2) = *(const u32x4*)(mem0 + row * DM + c);
            }
        } else {
            const unsigned tag = (unsigned)s;
            u32x4 vv[4];
            for (unsigned spin = 0;; ++spin) {
                if (spin > (1u << 20)) break;
                asm volatile("" ::: "memory");
#pragma unroll
                for (int i = 0; i < 4; ++i) vv[i] = bld16_sc1(rB, l16, (unsigned)(i * 4096 + w * 1024));
                bool ok = true;
#pragma unroll
                for (int i = 0; i < 4; ++i)
#pragma unroll
                    for (int q = 0; q < 4; ++q) ok &= ((vv[i][q] & 0xffff) == tag);
                if (__all(ok)) break;
            }
#pragma unroll
            for (int i = 0; i < 4; ++i) {
                int row = 2 * i + (w >> 1), c = ((w & 1) * 64 + lane) * 4;
                u32x2 o = {(vv[i][0] >> 16) | (vv[i][1] & 0xffff0000u), (vv[i][2] >> 16) | (vv[i][3] & 0xffff0000u)};
                *(u32x2*)(sMem + row * MEM_STRIDE + c * 2) = o;
            }
        }
        STAMP(1);
        BARRIER();
        STAMP(2);
        f32x4 acc[4];
        {
            bf16x8 mb[4];
#pragma unroll
            for (int i = 0; i < 4; ++i) mb[i] = *(const bf16x8*)(memrd + i * 64);
#pragma unroll
            for (int ks = 0; ks < 16; ++ks) {
                bf16x8 m = mb[ks & 3];
                if (ks == 0) {
                    MFMA0_A(acc[0], wq[0][ks], m); MFMA0_A(acc[1], wq[1][ks], m); MFMA0_A(acc[2], wq[2][ks], m); MFMA0_A(acc[3], wq[3][ks], m);
                } else {
                    MFMA_A(acc[0], wq[0][ks], m); MFMA_A(acc[1], wq[1][ks], m); MFMA_A(acc[2], wq[2][ks], m); MFMA_A(acc[3], wq[3][ks], m);
                }
                if (ks + 4 < 16) mb[ks & 3] = *(const bf16x8*)(memrd + (ks + 4) * 64);
            }
        }
        STAMP(3);
        f32x4 av[2];
        {
            bf16x8 mb[4];
#pragma unroll
            for (int i = 0; i < 4; ++i) mb[i] = *(const bf16x8*)(memrd + i * 64);
            asm volatile("s_nop 7" : "+v"(acc[0]), "+v"(acc[1]), "+v"(acc[2]), "+v"(acc[3]));
            if (fr < 8) {
#pragma unroll
                for (int t = 0; t < 4; ++t) {
                    int row = (t >> 1) * 8 + fr;
                    int c = w * 32 + (t & 1) * 16 + fg * 4;
                    u32x2 o = {pack2(acc[t][0], acc[t][1]), pack2(acc[t][2], acc[t][3])};
                    *(u32x2*)(sQK + row * QK_STRIDE + c * 2) = o;
                    if (t >= 2) bst8(rKm, (unsigned)fr * 256 + fg * 8, (unsigned)((s * NH + h) * 2048 + (w * 32 + (t & 1) * 16) * 2), o);
                }
            }
#pragma unroll
            for (int ks = 0; ks < 16; ++ks) {
                bf16x8 m = mb[ks & 3];
                if (ks == 0) { MFMA0_V(av[0], m, wq[4][ks]); MFMA0_V(av[1], m, wq[5][ks]); }
                else { MFMA_V(av[0], m, wq[4][ks]); MFMA_V(av[1], m, wq[5][ks]); }
                if (ks + 4 < 16) mb[ks & 3] = *(const bf16x8*)(memrd + (ks + 4) * 64);
            }
        }
        STAMP(4);
        BARRIER();
        STAMP(5);
        f32x4 sm = {0.f, 0.f, 0.f, 0.f}, sx0 = sm, sx1 = sm;
#pragma unroll
        for (int ks = 0; ks < 4; ++ks) {
            bf16x8 qf = *(const bf16x8*)(qrd + ks * 64);
            bf16x8 kf = *(const bf16x8*)(qrd + 8 * QK_STRIDE + ks * 64);
            sx0 = __builtin_amdgcn_mfma_f32_16x16x32_bf16(__builtin_bit_cast(bf16x8, kx[0][ks]), qf, sx0, 0, 0, 0);
            sx1 = __builtin_amdgcn_mfma_f32_16x16x32_bf16(__builtin_bit_cast(bf16x8, kx[1][ks]), qf, sx1, 0, 0, 0);
            sm = __builtin_amdgcn_mfma_f32_16x16x32_bf16(kf, qf, sm, 0, 0, 0);
        }
        asm volatile("s_nop 7" : "+v"(av[0]), "+v"(av[1]));
        u32x2 vm[2];
#pragma unroll
        for (int t = 0; t < 2; ++t) {
            vm[t] = (u32x2){pack2(av[t][0], av[t][1]), pack2(av[t][2], av[t][3])};
            if (fg < 2) bst8(rVm, (unsigned)fr * 16 + fg * 8, (unsigned)((s * NH + h) * 2048 + (w * 32 + t * 16) * 16), vm[t]);
            else vm[t] = (u32x2){0u, 0u};
        }
        float sv[12];
#pragma unroll
        for (int q = 0; q < 4; ++q) {
            sv[q] = (fg < 2) ? rbf(rbf(sm[q]) * SCALE) : -3.0e38f;
            sv[4 + q] = rbf(rbf(sx0[q]) * SCALE);
            sv[8 + q] = rbf(rbf(sx1[q]) * SCALE);
        }
        float mx = fmaxf(fmaxf(fmaxf(sv[0], sv[1]), fmaxf(sv[2], sv[3])), fmaxf(fmaxf(fmaxf(sv[4], sv[5]), fmaxf(sv[6], sv[7])), fmaxf(fmaxf(sv[8], sv[9]), fmaxf(sv[10], sv[11]))));
        mx = xmax16(mx);
        mx = xmax32(mx);
        float sum = 0.f;
#pragma unroll
        for (int q = 0; q < 12; ++q) { sv[q] = __expf(sv[q] - mx); sum += sv[q]; }
        sum = xsum16(sum);
        sum = xsum32(sum);
        float inv = 1.f / sum;
        u32x4 px = {pack2(sv[4] * inv, sv[5] * inv), pack2(sv[6] * inv, sv[7] * inv), pack2(sv[8] * inv, sv[9] * inv), pack2(sv[10] * inv, sv[11] * inv)};
        u32x4 pm = {pack2(sv[0] * inv, sv[1] * inv), pack2(sv[2] * inv, sv[3] * inv), 0u, 0u};
#pragma unroll
        for (int t = 0; t < 2; ++t) {
            u32x4 ax = {vx[t][0][0], vx[t][0][1], vx[t][1][0], vx[t][1][1]};
            u32x4 am = {vm[t][0], vm[t][1], 0u, 0u};
            f32x4 o = {0.f, 0.f, 0.f, 0.f};
            o = __builtin_amdgcn_mfma_f32_16x16x32_bf16(__builtin_bit_cast(bf16x8, ax), __builtin_bit_cast(bf16x8, px), o, 0, 0, 0);
            o = __builtin_amdgcn_mfma_f32_16x16x32_bf16(__builtin_bit_cast(bf16x8, am), __builtin_bit_cast(bf16x8, pm), o, 0, 0, 0);
            if (fr < 8) {
                u32x2 ov = {pack2(o[0], o[1]), pack2(o[2], o[3])};
                *(u32x2*)(sO + fr * QK_STRIDE + (w * 32 + t * 16 + fg * 4) * 2) = ov;
            }
        }
        bf16x8 wf[8];
#pragma unroll
        for (int i = 0; i < 8; ++i) wf[i] = *(const bf16x8*)(word + i * 1024);
        STAMP(6);
        BARRIER();
        STAMP(7);
        bf16x8 of[4];
#pragma unroll
        for (int ks = 0; ks < 4; ++ks) of[ks] = *(const bf16x8*)(ord_ + ks * 64);
        const unsigned tagA = (unsigned)((s + 1) & 1);
#pragma unroll
        for (int t = 0; t < 8; ++t) {
            f32x4 a = {0.f, 0.f, 0.f, 0.f};
#pragma unroll
            for (int ks = 0; ks < 4; ++ks) {
                bf16x8 f = wf[(t & 1) * 4 + ks];
                if (t + 2 < 8) wf[(t & 1) * 4 + ks] = *(const bf16x8*)(word + ((t + 2) * 4 + ks) * 1024);
                a = __builtin_amdgcn_mfma_f32_16x16x32_bf16(f, of[ks], a, 0, 0, 0);
            }
            if (fr < 8) {
                u32x4 o;
#pragma unroll
                for (int q = 0; q < 4; ++q) o[q] = (__float_as_uint(a[q]) & ~1u) | tagA;
                bst16_sc1(rA, stA_vo, (unsigned)(((w * 8 + t) * NH + h) * 512), o);
            }
        }
        STAMP(8);
        {
            float tot[4];
            for (unsigned spin = 0;; ++spin) {
                if (spin > (1u << 20)) break;
                asm volatile("" ::: "memory");
                u32x4 v[4];
#pragma unroll
                for (int i = 0; i < 4; ++i) v[i] = bld16_sc1(rA, rdA_vo, (unsigned)(h * NH * 512 + i * 4096 + w * 128));
                bool ok = true;
#pragma unroll
                for (int q = 0; q < 4; ++q) {
                    ok &= ((v[0][q] & 1u) == tagA) & ((v[1][q] & 1u) == tagA) & ((v[2][q] & 1u) == tagA) & ((v[3][q] & 1u) == tagA);
                    tot[q] = (__uint_as_float(v[0][q] & ~1u) + __uint_as_float(v[1][q] & ~1u)) +
                             (__uint_as_float(v[2][q] & ~1u) + __uint_as_float(v[3][q] & ~1u));
                }
                if (__all(ok)) break;
            }
#pragma unroll
            for (int q = 0; q < 4; ++q) {
                tot[q] = xsum8(tot[q]);
                tot[q] = xsum16(tot[q]);
                tot[q] = xsum32(tot[q]);
            }
            STAMP(9);
            if (lane < 8) {
                u32x4 o;
#pragma unroll
                for (int q = 0; q < 4; ++q) o[q] = ((unsigned)f2bf(tot[q]) << 16) | (unsigned)(s + 1);
                bst16_sc1(rB, (unsigned)(lane >> 2) * 2048 + (lane & 3) * 16, (unsigned)(w * 4096 + h * 64), o);
            }
        }
    }
}

__global__ void __launch_bounds__(256) attn_k(WS ws) {
    const int lane = threadIdx.x & 63, w = threadIdx.x >> 6;
    const int s = blockIdx.x >> 3, h = (blockIdx.x & 7) * 4 + w;
    const int fr = lane & 15, fg = lane >> 4, fr8 = lane & 7;
    bf16x8 kx[2][4], kmf[4];
#pragma unroll
    for (int ks = 0; ks < 4; ++ks) {
        kmf[ks] = *(const bf16x8*)(ws.km + ((size_t)(s * NH + h) * 8 + fr8) * DH + ks * 32 + fg * 8);
#pragma unroll
        for (int t = 0; t < 2; ++t) kx[t][ks] = *(const bf16x8*)(ws.K + (size_t)(s * SEG + t * 16 + fr) * HD + h * DH + ks * 32 + fg * 8);
    }
    u32x4 px[2], pm[2];
#pragma unroll
    for (int qt = 0; qt < 2; ++qt) {
        f32x4 sm = {0.f, 0.f, 0.f, 0.f}, sx0 = sm, sx1 = sm;
#pragma unroll
        for (int ks = 0; ks < 4; ++ks) {
            bf16x8 qf = *(const bf16x8*)(ws.Q + (size_t)(s * SEG + qt * 16 + fr) * HD + h * DH + ks * 32 + fg * 8);
            sm = __builtin_amdgcn_mfma_f32_16x16x32_bf16(kmf[ks], qf, sm, 0, 0, 0);
            sx0 = __builtin_amdgcn_mfma_f32_16x16x32_bf16(kx[0][ks], qf, sx0, 0, 0, 0);
            sx1 = __builtin_amdgcn_mfma_f32_16x16x32_bf16(kx[1][ks], qf, sx1, 0, 0, 0);
        }
        const int qi = qt * 16 + fr;
        float sv[12];
#pragma unroll
        for (int q = 0; q < 4; ++q) {
            sv[q] = (fg < 2) ? rbf(rbf(sm[q]) * SCALE) : -3.0e38f;
            int k0 = fg * 4 + q, k1 = 16 + fg * 4 + q;
            sv[4 + q] = (k0 <= qi) ? rbf(rbf(sx0[q]) * SCALE) : -3.0e38f;
            sv[8 + q] = (k1 <= qi) ? rbf(rbf(sx1[q]) * SCALE) : -3.0e38f;
        }
        float mx = sv[0];
#pragma unroll
        for (int q = 1; q < 12; ++q) mx = fmaxf(mx, sv[q]);
        mx = fmaxf(mx, __shfl_xor(mx, 16));
        mx = fmaxf(mx, __shfl_xor(mx, 32));
        float sum = 0.f;
#pragma unroll
        for (int q = 0; q < 12; ++q) { sv[q] = __expf(sv[q] - mx); sum += sv[q]; }
        sum += __shfl_xor(sum, 16);
        sum += __shfl_xor(sum, 32);
        float inv = 1.f / sum;
        px[qt] = (u32x4){pack2(sv[4] * inv, sv[5] * inv), pack2(sv[6] * inv, sv[7] * inv), pack2(sv[8] * inv, sv[9] * inv), pack2(sv[10] * inv, sv[11] * inv)};
        pm[qt] = (u32x4){pack2(sv[0] * inv, sv[1] * inv), pack2(sv[2] * inv, sv[3] * inv), 0u, 0u};
    }
#pragma unroll
    for (int et = 0; et < 8; ++et) {
        const u16* p = ws.VT + (size_t)(h * DH + et * 16 + fr) * S_TOK + s * SEG + fg * 4;
        u32x2 a0 = *(const u32x2*)p, a1 = *(const u32x2*)(p + 16);
        u32x2 m0 = {0u, 0u};
        if (fg < 2) m0 = *(const u32x2*)(ws.vmT + ((size_t)(s * NH + h) * DH + et * 16 + fr) * 8 + fg * 4);
        u32x4 ax = {a0[0], a0[1], a1[0], a1[1]};
        u32x4 am = {m0[0], m0[1], 0u, 0u};
#pragma unroll
        for (int qt = 0; qt < 2; ++qt) {
            f32x4 o = {0.f, 0.f, 0.f, 0.f};
            o = __builtin_amdgcn_mfma_f32_16x16x32_bf16(__builtin_bit_cast(bf16x8, ax), __builtin_bit_cast(bf16x8, px[qt]), o, 0, 0, 0);
            o = __builtin_amdgcn_mfma_f32_16x16x32_bf16(__builtin_bit_cast(bf16x8, am), __builtin_bit_cast(bf16x8, pm[qt]), o, 0, 0, 0);
            u32x2 ov = {pack2(o[0], o[1]), pack2(o[2], o[3])};
            *(u32x2*)(ws.Ox + (size_t)(s * SEG + qt * 16 + fr) * HD + h * DH + et * 16 + fg * 4) = ov;
        }
    }
}

extern "C" void kernel_launch(void* const* d_in, const int* in_sizes, int n_in, void* d_out, int out_size, void* d_ws, size_t ws_size,
                              hipStream_t stream) {
    const u16* x = (const u16*)d_in[0];
    const u16* mem0 = (const u16*)d_in[1];
    const u16* Wq = (const u16*)d_in[2];
    const u16* Wk = (const u16*)d_in[3];
    const u16* Wv = (const u16*)d_in[4];
    const u16* Wo = (const u16*)d_in[5];
    const u16* w1 = (const u16*)d_in[8];
    const u16* b1 = (const u16*)d_in[9];
    const u16* w2 = (const u16*)d_in[10];
    const u16* b2 = (const u16*)d_in[11];
    u16* out = (u16*)d_out;

    char* p = (char*)d_ws;
    auto carve = [&](size_t bytes) { char* r = p; p += (bytes + 255) & ~(size_t)255; return r; };
    WS ws;
    ws.exA = (unsigned*)carve((size_t)32 * 32 * 128 * 4);
    ws.exB = (unsigned*)carve((size_t)8 * 512 * 4);
    ws.xn = (u16*)carve((size_t)S_TOK * DM * 2);
    ws.y = (u16*)carve((size_t)S_TOK * DM * 2);
    ws.WqkvT = (u16*)carve((size_t)3 * HD * DM * 2);
    ws.WoT = (u16*)carve((size_t)DM * HD * 2);
    ws.W1T = (u16*)carve((size_t)FFP * DM * 2);
    ws.W2T = (u16*)carve((size_t)DM * FFP * 2);
    ws.Q = (u16*)carve((size_t)S_TOK * HD * 2);
    ws.K = (u16*)carve((size_t)S_TOK * HD * 2);
    ws.VT = (u16*)carve((size_t)S_TOK * HD * 2);
    ws.hmid = (u16*)carve((size_t)S_TOK * FFP * 2);
    ws.g = (u16*)carve((size_t)S_TOK * DM * 2);
    ws.Ox = (u16*)carve((size_t)S_TOK * HD * 2);
    ws.km = (u16*)carve((size_t)NSEG * NH * 8 * DH * 2);
    ws.vmT = (u16*)carve((size_t)NSEG * NH * 8 * DH * 2);
    ws.dbg = (unsigned long long*)((char*)d_ws + (size_t)200 * 1024 * 1024);

    zero_k<<<(32 * 32 * 128 + 8 * 512 + 64 + 255) / 256, 256, 0, stream>>>(ws.exA, 32 * 32 * 128 + 8 * 512);
    prep_ln<<<S_TOK / 4, 256, 0, stream>>>(x, ws.xn, ws.y);
    transpose_k<<<dim3(HD / 64, DM / 64), 256, 0, stream>>>(Wq, ws.WqkvT, DM, HD, DM, HD);
    transpose_k<<<dim3(HD / 64, DM / 64), 256, 0, stream>>>(Wk, ws.WqkvT + (size_t)HD * DM, DM, HD, DM, HD);
    transpose_k<<<dim3(HD / 64, DM / 64), 256, 0, stream>>>(Wv, ws.WqkvT + (size_t)2 * HD * DM, DM, HD, DM, HD);
    transpose_k<<<dim3(DM / 64, HD / 64), 256, 0, stream>>>(Wo, ws.WoT, HD, DM, HD, DM);
    transpose_k<<<dim3(FFP / 64, DM / 64), 256, 0, stream>>>(w1, ws.W1T, DM, FF, DM, FFP);
    transpose_k<<<dim3(DM / 64, FFP / 64), 256, 0, stream>>>(w2, ws.W2T, FF, DM, FFP, DM);

    EpiArgs ea;
    ea = EpiArgs{ws.Q, ws.K, ws.VT, nullptr, nullptr, nullptr};
    gemm_k<EPI_QKV><<<dim3(3 * HD / 128, S_TOK / 128), 256, 0, stream>>>(ws.xn, ws.WqkvT, DM, DM, DM, ea);
    hipFuncSetAttribute((const void*)chain_k, hipFuncAttributeMaxDynamicSharedMemorySize, 147456);
    chain_k<<<NH, 256, 147456, stream>>>(mem0, ws);
    ea = EpiArgs{ws.hmid, nullptr, nullptr, b1, nullptr, nullptr};
    gemm_k<EPI_H><<<dim3(FFP / 128, S_TOK / 128), 256, 0, stream>>>(ws.y, ws.W1T, DM, DM, DM, ea);
    ea = EpiArgs{ws.g, nullptr, nullptr, b2, nullptr, nullptr};
    gemm_k<EPI_G><<<dim3(DM / 128, S_TOK / 128), 256, 0, stream>>>(ws.hmid, ws.W2T, FFP, FFP, FFP, ea);
    attn_k<<<NSEG * 8, 256, 0, stream>>>(ws);
    ea = EpiArgs{out, nullptr, nullptr, nullptr, x, ws.g};
    gemm_k<EPI_OUT><<<dim3(DM / 128, S_TOK / 128), 256, 0, stream>>>(ws.Ox, ws.WoT, HD, HD, HD, ea);
}
```

```cpp
#include <hip/hip_runtime.h>
#include <hip/hip_bf16.h>
#include <stdint.h>

typedef __attribute__((ext_vector_type(8))) short bf16x8;
typedef __attribute__((ext_vector_type(4))) short bf16x4;
typedef __attribute__((ext_vector_type(4))) float f32x4;
typedef __attribute__((ext_vector_type(4))) unsigned u32x4;
typedef __attribute__((ext_vector_type(2))) unsigned u32x2;
typedef unsigned short u16;

#define S_TOK 2048
#define DM 512
#define NH 32
#define DH 128
#define HD 4096
#define NMEM 8
#define SEG 32
#define NSEG 64
#define NSTEPS 64
#define FF 1376
#define FFP 1408

__device__ __forceinline__ u16 f2bf(float f) { return __builtin_bit_cast(u16, (__bf16)f); }
__device__ __forceinline__ float bf2f(u16 h) { return __builtin_bit_cast(float, (unsigned)h << 16); }
__device__ __forceinline__ float rbf(float f) { return bf2f(f2bf(f)); }
typedef __attribute__((ext_vector_type(2))) float f32x2;
typedef __attribute__((ext_vector_type(2))) __bf16 bf16x2v;
__device__ __forceinline__ unsigned pack2(float a, float b) { f32x2 v = {a, b}; return __builtin_bit_cast(unsigned, __builtin_convertvector(v, bf16x2v)); }
__device__ __forceinline__ f32x2 rbf2(f32x2 v) { unsigned u = __builtin_bit_cast(unsigned, __builtin_convertvector(v, bf16x2v)); f32x2 r = {__uint_as_float(u << 16), __uint_as_float(u & 0xffff0000u)}; return r; }

struct WS {
    u16 *xn, *y, *WqkvT, *WoT, *W1T, *W2T, *Q, *K, *VT, *hmid, *g, *Ox, *km, *vmT;
    unsigned *exA, *exB;
    unsigned long long* dbg;
};

__device__ __forceinline__ float wave_sum(float v) {
#pragma unroll
    for (int m = 32; m >= 1; m >>= 1) v += __shfl_xor(v, m);
    return v;
}
__device__ __forceinline__ void ln8(float (&v)[8]) {
    float s = 0.f;
#pragma unroll
    for (int i = 0; i < 8; ++i) s += v[i];
    s = wave_sum(s);
    float mean = s * (1.f / 512.f);
    float q = 0.f;
#pragma unroll
    for (int i = 0; i < 8; ++i) { float d = v[i] - mean; q += d * d; }
    q = wave_sum(q);
    float var = rbf(q * (1.f / 512.f));
    float mb = rbf(mean);
    float sd = rbf(sqrtf(rbf(var + 1e-5f)));
#pragma unroll
    for (int i = 0; i < 8; ++i) v[i] = rbf(rbf(v[i] - mb) / sd);
}

__global__ void __launch_bounds__(256) prep_ln(const u16* __restrict__ x, u16* __restrict__ xn, u16* __restrict__ y) {
    int row = blockIdx.x * 4 + (threadIdx.x >> 6);
    int lane = threadIdx.x & 63;
    u32x4 r = *(const u32x4*)(x + (size_t)row * DM + lane * 8);
    float v[8];
#pragma unroll
    for (int i = 0; i < 4; ++i) { v[2 * i] = bf2f(r[i] & 0xffff); v[2 * i + 1] = bf2f(r[i] >> 16); }
    ln8(v);
    u32x4 o;
#pragma unroll
    for (int i = 0; i < 4; ++i) o[i] = pack2(v[2 * i], v[2 * i + 1]);
    *(u32x4*)(xn + (size_t)row * DM + lane * 8) = o;
    ln8(v);
#pragma unroll
    for (int i = 0; i < 4; ++i) o[i] = pack2(v[2 * i], v[2 * i + 1]);
    *(u32x4*)(y + (size_t)row * DM + lane * 8) = o;
}

__global__ void __launch_bounds__(256) transpose_k(const u16* __restrict__ src, u16* __restrict__ dst, int R, int C, int ldd, int Cpad) {
    __shared__ u16 t[64][66];
    int c0 = blockIdx.x * 64, r0 = blockIdx.y * 64;
    int tx = threadIdx.x & 63, ty = threadIdx.x >> 6;
    for (int i = ty; i < 64; i += 4) {
        int r = r0 + i, c = c0 + tx;
        t[i][tx] = (r < R && c < C) ? src[(size_t)r * C + c] : (u16)0;
    }
    __syncthreads();
    for (int i = ty; i < 64; i += 4) {
        int c = c0 + i, r = r0 + tx;
        if (c < Cpad && r < ldd) dst[(size_t)c * ldd + r] = t[tx][i];
    }
}

__global__ void zero_k(unsigned* p, int n) {
    int i = blockIdx.x * blockDim.x + threadIdx.x;
    if (i < n) p[i] = 0;
}

enum { EPI_QKV = 0, EPI_H = 1, EPI_G = 2, EPI_OUT = 3 };
struct EpiArgs {
    u16* o0; u16* o1; u16* o2;
    const u16* bias; const u16* x; const u16* g;
};

__device__ __forceinline__ float gelu_tanh(float v) {
    float u = 0.7978845608028654f * (v + 0.044715f * v * v * v);
    return 0.5f * v * (1.f + tanhf(u));
}

template <int EPI>
__global__ void __launch_bounds__(256) gemm_k(const u16* __restrict__ A, const u16* __restrict__ Bt, int lda, int ldb, int K, EpiArgs ea) {
    __shared__ __attribute__((aligned(16))) char lds[2][2][128 * 128];
    const int tid = threadIdx.x, lane = tid & 63, w = tid >> 6;
    const int wr = w >> 1, wc = w & 1;
    const int brow = blockIdx.y * 128, bcol = blockIdx.x * 128;
    const int fr = lane & 15, fg = lane >> 4;
    f32x4 acc[4][4];
#pragma unroll
    for (int i = 0; i < 4; ++i)
#pragma unroll
        for (int j = 0; j < 4; ++j) acc[i][j] = (f32x4){0.f, 0.f, 0.f, 0.f};
    const int srow = tid >> 3, sslot = tid & 7;
    const u16* ga = A + (size_t)(brow + srow) * lda + sslot * 8;
    const u16* gb = Bt + (size_t)(bcol + srow) * ldb + sslot * 8;
    u32x4 ra[4], rb[4];
    auto gload = [&](int kt) {
#pragma unroll
        for (int i = 0; i < 4; ++i) {
            ra[i] = *(const u32x4*)(ga + (size_t)(32 * i) * lda + kt * 64);
            rb[i] = *(const u32x4*)(gb + (size_t)(32 * i) * ldb + kt * 64);
        }
    };
    auto swrite = [&](int buf) {
#pragma unroll
        for (int i = 0; i < 4; ++i) {
            int r = srow + 32 * i;
            int off = r * 128 + ((sslot ^ (r & 7)) << 4);
            *(u32x4*)(&lds[buf][0][off]) = ra[i];
            *(u32x4*)(&lds[buf][1][off]) = rb[i];
        }
    };
    const int nt = K / 64;
    gload(0);
    swrite(0);
    __syncthreads();
    const bool vtile = (EPI == EPI_QKV) && (bcol >= 2 * HD);
    for (int kt = 0; kt < nt; ++kt) {
        int buf = kt & 1;
        if (kt + 1 < nt) gload(kt + 1);
#pragma unroll
        for (int kk = 0; kk < 2; ++kk) {
            bf16x8 af[4], bfr[4];
#pragma unroll
            for (int i = 0; i < 4; ++i) {
                int r = wr * 64 + i * 16 + fr;
                af[i] = *(const bf16x8*)(&lds[buf][0][r * 128 + (((kk * 4 + fg) ^ (r & 7)) << 4)]);
                int c = wc * 64 + i * 16 + fr;
                bfr[i] = *(const bf16x8*)(&lds[buf][1][c * 128 + (((kk * 4 + fg) ^ (c & 7)) << 4)]);
            }
            if (vtile) {
#pragma unroll
                for (int i = 0; i < 4; ++i)
#pragma unroll
                    for (int j = 0; j < 4; ++j) acc[i][j] = __builtin_amdgcn_mfma_f32_16x16x32_bf16(af[i], bfr[j], acc[i][j], 0, 0, 0);
            } else {
#pragma unroll
                for (int i = 0; i < 4; ++i)
#pragma unroll
                    for (int j = 0; j < 4; ++j) acc[i][j] = __builtin_amdgcn_mfma_f32_16x16x32_bf16(bfr[j], af[i], acc[i][j], 0, 0, 0);
            }
        }
        if (kt + 1 < nt) swrite(buf ^ 1);
        __syncthreads();
    }
#pragma unroll
    for (int i = 0; i < 4; ++i)
#pragma unroll
        for (int j = 0; j < 4; ++j) {
            f32x4 a = acc[i][j];
            if (vtile) {
                int col = bcol + wc * 64 + j * 16 + fr - 2 * HD;
                int tok = brow + wr * 64 + i * 16 + fg * 4;
                u32x2 o = {pack2(a[0], a[1]), pack2(a[2], a[3])};
                *(u32x2*)(ea.o2 + (size_t)col * S_TOK + tok) = o;
            } else {
                int tok = brow + wr * 64 + i * 16 + fr;
                int col = bcol + wc * 64 + j * 16 + fg * 4;
                if (EPI == EPI_QKV) {
                    u16* dst = (col >= HD) ? (ea.o1 + (size_t)tok * HD + (col - HD)) : (ea.o0 + (size_t)tok * HD + col);
                    u32x2 o = {pack2(a[0], a[1]), pack2(a[2], a[3])};
                    *(u32x2*)dst = o;
                } else if (EPI == EPI_H) {
                    float r[4];
#pragma unroll
                    for (int q = 0; q < 4; ++q) {
                        float b = (col + q < FF) ? bf2f(ea.bias[col + q]) : 0.f;
                        float v = rbf(rbf(a[q]) + b);
                        r[q] = (col + q < FF) ? gelu_tanh(v) : 0.f;
                    }
                    u32x2 o = {pack2(r[0], r[1]), pack2(r[2], r[3])};
                    *(u32x2*)(ea.o0 + (size_t)tok * FFP + col) = o;
                } else if (EPI == EPI_G) {
                    float r[4];
#pragma unroll
                    for (int q = 0; q < 4; ++q) {
                        float v = rbf(rbf(a[q]) + bf2f(ea.bias[col + q]));
                        r[q] = v / (1.f + __expf(-v));
                    }
                    u32x2 o = {pack2(r[0], r[1]), pack2(r[2], r[3])};
                    *(u32x2*)(ea.o0 + (size_t)tok * DM + col) = o;
                } else {
                    u32x2 xv = *(const u32x2*)(ea.x + (size_t)tok * DM + col);
                    u32x2 gv = *(const u32x2*)(ea.g + (size_t)tok * DM + col);
                    float xs[4] = {bf2f(xv[0] & 0xffff), bf2f(xv[0] >> 16), bf2f(xv[1] & 0xffff), bf2f(xv[1] >> 16)};
                    float gs[4] = {bf2f(gv[0] & 0xffff), bf2f(gv[0] >> 16), bf2f(gv[1] & 0xffff), bf2f(gv[1] >> 16)};
                    float r[4];
#pragma unroll
                    for (int q = 0; q < 4; ++q) r[q] = xs[q] + rbf(rbf(a[q]) * gs[q]);
                    u32x2 o = {pack2(r[0], r[1]), pack2(r[2], r[3])};
                    *(u32x2*)(ea.o0 + (size_t)tok * DM + col) = o;
                }
            }
        }
}

#define SCALE 0.08838834764831845f
#define MEM_STRIDE 1056
#define QK_STRIDE 288

typedef __amdgpu_buffer_rsrc_t rsrc_t;
__device__ __forceinline__ rsrc_t mk_rsrc(const void* p) { return __builtin_amdgcn_make_buffer_rsrc((void*)p, 0, 0x7fffffff, 0x00020000); }
__device__ __forceinline__ u32x4 bld16(rsrc_t r, unsigned vo, unsigned so) { return __builtin_amdgcn_raw_buffer_load_b128(r, vo, so, 0); }
__device__ __forceinline__ u32x4 bld16_sc1(rsrc_t r, unsigned vo, unsigned so) { return __builtin_amdgcn_raw_buffer_load_b128(r, vo, so, 16); }
__device__ __forceinline__ u32x2 bld8(rsrc_t r, unsigned vo, unsigned so) { return __builtin_amdgcn_raw_buffer_load_b64(r, vo, so, 0); }
__device__ __forceinline__ void bst16_sc1(rsrc_t r, unsigned vo, unsigned so, u32x4 v) { __builtin_amdgcn_raw_buffer_store_b128(v, r, vo, so, 16); }
__device__ __forceinline__ void bst8(rsrc_t r, unsigned vo, unsigned so, u32x2 v) { __builtin_amdgcn_raw_buffer_store_b64(v, r, vo, so, 0); }

#define STAMP(i)
#define MFMA0_A(acc, a, b) asm volatile("v_mfma_f32_16x16x32_bf16 %0, %1, %2, 0" : "=&v"(acc) : "a"(a), "v"(b))
#define MFMA_A(acc, a, b) asm volatile("v_mfma_f32_16x16x32_bf16 %0, %1, %2, %0" : "+v"(acc) : "a"(a), "v"(b))
#define MFMA0_V(acc, a, b) asm volatile("v_mfma_f32_16x16x32_bf16 %0, %1, %2, 0" : "=&v"(acc) : "v"(a), "v"(b))
#define MFMA_V(acc, a, b) asm volatile("v_mfma_f32_16x16x32_bf16 %0, %1, %2, %0" : "+v"(acc) : "v"(a), "v"(b))
#define BARRIER() asm volatile("s_waitcnt lgkmcnt(0)\n\ts_barrier" ::: "memory")

__device__ __forceinline__ float xmax16(float x) {
    unsigned u = __float_as_uint(x);
    auto r = __builtin_amdgcn_permlane16_swap(u, u, false, false);
    return fmaxf(__uint_as_float(r[0]), __uint_as_float(r[1]));
}
__device__ __forceinline__ float xmax32(float x) {
    unsigned u = __float_as_uint(x);
    auto r = __builtin_amdgcn_permlane32_swap(u, u, false, false);
    return fmaxf(__uint_as_float(r[0]), __uint_as_float(r[1]));
}
__device__ __forceinline__ float xsum16(float x) {
    unsigned u = __float_as_uint(x);
    auto r = __builtin_amdgcn_permlane16_swap(u, u, false, false);
    return __uint_as_float(r[0]) + __uint_as_float(r[1]);
}
__device__ __forceinline__ float xsum32(float x) {
    unsigned u = __float_as_uint(x);
    auto r = __builtin_amdgcn_permlane32_swap(u, u, false, false);
    return __uint_as_float(r[0]) + __uint_as_float(r[1]);
}
__device__ __forceinline__ float xsum8(float x) {
    return x + __uint_as_float(__builtin_amdgcn_update_dpp(0u, __float_as_uint(x), 0x128, 0xf, 0xf, false));
}

__global__ void __launch_bounds__(256, 1) chain_k(const u16* __restrict__ mem0, WS ws) {
    extern __shared__ __attribute__((aligned(16))) char smem[];
    char* sWo = smem;
    char* sMem = smem + 131072;
    char* sQK = sMem + 8 * MEM_STRIDE;
    char* sO = sQK + 16 * QK_STRIDE;
    const int h = blockIdx.x;
    const int tid = threadIdx.x, lane = tid & 63;
    const int w = __builtin_amdgcn_readfirstlane(tid >> 6);
    const int fr = lane & 15, fg = lane >> 4, fr8 = lane & 7;

    bf16x8 wq[6][16];
#pragma unroll
    for (int t = 0; t < 6; ++t) {
        int col = (t >> 1) * HD + h * DH + w * 32 + (t & 1) * 16 + fr;
        const u16* p = ws.WqkvT + (size_t)col * DM + fg * 8;
#pragma unroll
        for (int ks = 0; ks < 16; ++ks) wq[t][ks] = *(const bf16x8*)(p + ks * 32);
    }
    for (int i = w; i < 128; i += 4) {
        int t = i >> 2, ks = i & 3;
        bf16x8 v = *(const bf16x8*)(ws.WoT + (size_t)(t * 16 + fr) * HD + h * DH + ks * 32 + fg * 8);
        *(bf16x8*)(sWo + (size_t)i * 1024 + lane * 16) = v;
    }
    const rsrc_t rA = mk_rsrc(ws.exA), rB = mk_rsrc(ws.exB), rK = mk_rsrc(ws.K), rV = mk_rsrc(ws.VT), rKm = mk_rsrc(ws.km), rVm = mk_rsrc(ws.vmT);
    const unsigned l16 = lane * 16;
    const unsigned kx_vo = (unsigned)fr * (HD * 2) + fg * 16;
    const unsigned vx_vo = (unsigned)fr * (S_TOK * 2) + fg * 8;
    const unsigned stA_vo = (unsigned)fr * 64 + fg * 16;
    const unsigned rdA_vo = (unsigned)(lane >> 3) * 512 + (lane & 7) * 16;
    const char* memrd = sMem + fr8 * MEM_STRIDE + fg * 16;
    const char* qrd = sQK + fr8 * QK_STRIDE + fg * 16;
    const char* ord_ = sO + fr8 * QK_STRIDE + fg * 16;
    const char* word = sWo + w * 32768 + l16;

    for (int s = 0; s < NSTEPS; ++s) {
        STAMP(0);
        u32x4 kx[2][4];
#pragma unroll
        for (int t = 0; t < 2; ++t)
#pragma unroll
            for (int ks = 0; ks < 4; ++ks) kx[t][ks] = bld16(rK, kx_vo, (unsigned)((s * SEG + t * 16) * (HD * 2) + h * 256 + ks * 64));
        u32x2 vx[2][2];
#pragma unroll
        for (int t = 0; t < 2; ++t) {
            unsigned so = (unsigned)((h * DH + w * 32 + t * 16) * (S_TOK * 2) + s * 64);
            vx[t][0] = bld8(rV, vx_vo, so);
            vx[t][1] = bld8(rV, vx_vo, so + 32);
        }
        if (s == 0) {
            for (int i = tid; i < 8 * 64; i += 256) {
                int row = i >> 6, c = (i & 63) * 8;
                *(u32x4*)(sMem + row * MEM_STRIDE + c * 2) = *(const u32x4*)(mem0 + row * DM + c);
            }
        } else {
            const unsigned tag = (unsigned)s;
            u32x4 vv[4];
            for (unsigned spin = 0;; ++spin) {
                if (spin > (1u << 20)) break;
                asm volatile("" ::: "memory");
#pragma unroll
                for (int i = 0; i < 4; ++i) vv[i] = bld16_sc1(rB, l16, (unsigned)(i * 4096 + w * 1024));
                bool ok = true;
#pragma unroll
                for (int i = 0; i < 4; ++i)
#pragma unroll
                    for (int q = 0; q < 4; ++q) ok &= ((vv[i][q] & 0xffff) == tag);
                if (__all(ok)) break;
            }
#pragma unroll
            for (int i = 0; i < 4; ++i) {
                int row = 2 * i + (w >> 1), c = ((w & 1) * 64 + lane) * 4;
                u32x2 o = {(vv[i][0] >> 16) | (vv[i][1] & 0xffff0000u), (vv[i][2] >> 16) | (vv[i][3] & 0xffff0000u)};
                *(u32x2*)(sMem + row * MEM_STRIDE + c * 2) = o;
            }
        }
        STAMP(1);
        BARRIER();
        STAMP(2);
        f32x4 acc[4];
        {
            bf16x8 mb[4];
#pragma unroll
            for (int i = 0; i < 4; ++i) mb[i] = *(const bf16x8*)(memrd + i * 64);
#pragma unroll
            for (int ks = 0; ks < 16; ++ks) {
                bf16x8 m = mb[ks & 3];
                if (ks == 0) {
                    MFMA0_A(acc[0], wq[0][ks], m); MFMA0_A(acc[1], wq[1][ks], m); MFMA0_A(acc[2], wq[2][ks], m); MFMA0_A(acc[3], wq[3][ks], m);
                } else {
                    MFMA_A(acc[0], wq[0][ks], m); MFMA_A(acc[1], wq[1][ks], m); MFMA_A(acc[2], wq[2][ks], m); MFMA_A(acc[3], wq[3][ks], m);
                }
                if (ks + 4 < 16) mb[ks & 3] = *(const bf16x8*)(memrd + (ks + 4) * 64);
            }
        }
        STAMP(3);
        bf16x8 mv[4];
#pragma unroll
        for (int i = 0; i < 4; ++i) mv[i] = *(const bf16x8*)(memrd + i * 64);
        asm volatile("s_nop 7" : "+v"(acc[0]), "+v"(acc[1]), "+v"(acc[2]), "+v"(acc[3]));
        {
            u32x2 o[4];
#pragma unroll
            for (int t = 0; t < 4; ++t) o[t] = (u32x2){pack2(acc[t][0], acc[t][1]), pack2(acc[t][2], acc[t][3])};
            if (fr < 8) {
#pragma unroll
                for (int t = 0; t < 4; ++t) {
                    int row = (t >> 1) * 8 + fr;
                    int c = w * 32 + (t & 1) * 16 + fg * 4;
                    *(u32x2*)(sQK + row * QK_STRIDE + c * 2) = o[t];
                }
                bst8(rKm, (unsigned)fr * 256 + fg * 8, (unsigned)((s * NH + h) * 2048 + (w * 32) * 2), o[2]);
                bst8(rKm, (unsigned)fr * 256 + fg * 8, (unsigned)((s * NH + h) * 2048 + (w * 32 + 16) * 2), o[3]);
            }
        }
        STAMP(4);
        BARRIER();
        STAMP(5);
        f32x4 sm, sx0, sx1;
        {
            bf16x8 qf[4], kf[4];
#pragma unroll
            for (int ks = 0; ks < 4; ++ks) { qf[ks] = *(const bf16x8*)(qrd + ks * 64); kf[ks] = *(const bf16x8*)(qrd + 8 * QK_STRIDE + ks * 64); }
            MFMA0_V(sx0, kx[0][0], qf[0]); MFMA0_V(sx1, kx[1][0], qf[0]); MFMA0_V(sm, kf[0], qf[0]);
#pragma unroll
            for (int ks = 1; ks < 4; ++ks) { MFMA_V(sx0, kx[0][ks], qf[ks]); MFMA_V(sx1, kx[1][ks], qf[ks]); MFMA_V(sm, kf[ks], qf[ks]); }
        }
        f32x4 av[2];
#define VSTEP(ks) do { bf16x8 m_ = mv[(ks) & 3]; \
            if ((ks) == 0) { MFMA0_V(av[0], m_, wq[4][ks]); MFMA0_V(av[1], m_, wq[5][ks]); } \
            else { MFMA_V(av[0], m_, wq[4][ks]); MFMA_V(av[1], m_, wq[5][ks]); } \
            if ((ks) + 4 < 16) mv[(ks) & 3] = *(const bf16x8*)(memrd + ((ks) + 4) * 64); \
            __builtin_amdgcn_sched_barrier(0); } while (0)
        VSTEP(0); VSTEP(1); VSTEP(2); VSTEP(3);
        asm volatile("" : "+v"(sm), "+v"(sx0), "+v"(sx1));
        const f32x2 sc2 = {SCALE, SCALE};
        f32x2 p0 = rbf2(rbf2((f32x2){sx0[0], sx0[1]}) * sc2); __builtin_amdgcn_sched_barrier(0);
        VSTEP(4);
        f32x2 p1 = rbf2(rbf2((f32x2){sx0[2], sx0[3]}) * sc2); __builtin_amdgcn_sched_barrier(0);
        VSTEP(5);
        f32x2 p2 = rbf2(rbf2((f32x2){sx1[0], sx1[1]}) * sc2); __builtin_amdgcn_sched_barrier(0);
        VSTEP(6);
        f32x2 p3 = rbf2(rbf2((f32x2){sx1[2], sx1[3]}) * sc2); __builtin_amdgcn_sched_barrier(0);
        VSTEP(7);
        f32x2 p4 = rbf2(rbf2((f32x2){sm[0], sm[1]}) * sc2); __builtin_amdgcn_sched_barrier(0);
        VSTEP(8);
        f32x2 p5 = rbf2(rbf2((f32x2){sm[2], sm[3]}) * sc2);
        if (fg >= 2) { p4 = (f32x2){-3.0e38f, -3.0e38f}; p5 = p4; }
        __builtin_amdgcn_sched_barrier(0);
        VSTEP(9);
        float mx = fmaxf(fmaxf(fmaxf(p0[0], p0[1]), fmaxf(p1[0], p1[1])), fmaxf(fmaxf(p2[0], p2[1]), fmaxf(p3[0], p3[1])));
        mx = fmaxf(mx, fmaxf(fmaxf(p4[0], p4[1]), fmaxf(p5[0], p5[1])));
        mx = xmax16(mx);
        mx = xmax32(mx);
        __builtin_amdgcn_sched_barrier(0);
        VSTEP(10);
        const float L2E = 1.4426950408889634f;
        const f32x2 l2 = {L2E, L2E};
        const f32x2 mxl = {mx, mx};
        p0 = (p0 - mxl) * l2; p1 = (p1 - mxl) * l2; p2 = (p2 - mxl) * l2; p3 = (p3 - mxl) * l2; p4 = (p4 - mxl) * l2; p5 = (p5 - mxl) * l2;
        __builtin_amdgcn_sched_barrier(0);
        VSTEP(11);
        p0 = (f32x2){__builtin_amdgcn_exp2f(p0[0]), __builtin_amdgcn_exp2f(p0[1])};
        p1 = (f32x2){__builtin_amdgcn_exp2f(p1[0]), __builtin_amdgcn_exp2f(p1[1])};
        __builtin_amdgcn_sched_barrier(0);
        VSTEP(12);
        p2 = (f32x2){__builtin_amdgcn_exp2f(p2[0]), __builtin_amdgcn_exp2f(p2[1])};
        p3 = (f32x2){__builtin_amdgcn_exp2f(p3[0]), __builtin_amdgcn_exp2f(p3[1])};
        __builtin_amdgcn_sched_barrier(0);
        VSTEP(13);
        p4 = (f32x2){__builtin_amdgcn_exp2f(p4[0]), __builtin_amdgcn_exp2f(p4[1])};
        p5 = (f32x2){__builtin_amdgcn_exp2f(p5[0]), __builtin_amdgcn_exp2f(p5[1])};
        __builtin_amdgcn_sched_barrier(0);
        VSTEP(14);
        f32x2 s2 = ((p0 + p1) + (p2 + p3)) + (p4 + p5);
        float sum = s2[0] + s2[1];
        __builtin_amdgcn_sched_barrier(0);
        VSTEP(15);
        sum = xsum16(sum);
        sum = xsum32(sum);
        float inv = __builtin_amdgcn_rcpf(sum);
        const f32x2 iv = {inv, inv};
        p0 *= iv; p1 *= iv; p2 *= iv; p3 *= iv; p4 *= iv; p5 *= iv;
        u32x4 px = {pack2(p0[0], p0[1]), pack2(p1[0], p1[1]), pack2(p2[0], p2[1]), pack2(p3[0], p3[1])};
        u32x4 pm = {pack2(p4[0], p4[1]), pack2(p5[0], p5[1]), 0u, 0u};
        asm volatile("s_nop 7" : "+v"(av[0]), "+v"(av[1]));
        u32x4 am[2], ax[2];
#pragma unroll
        for (int t = 0; t < 2; ++t) {
            am[t] = (u32x4){pack2(av[t][0], av[t][1]), pack2(av[t][2], av[t][3]), 0u, 0u};
            if (fg >= 2) { am[t][0] = 0u; am[t][1] = 0u; }
            ax[t] = (u32x4){vx[t][0][0], vx[t][0][1], vx[t][1][0], vx[t][1][1]};
        }
        f32x4 o0, o1;
        asm volatile("s_nop 1" : "+v"(px), "+v"(pm), "+v"(am[0]), "+v"(am[1]), "+v"(ax[0]), "+v"(ax[1]));
        MFMA0_V(o0, ax[0], px); MFMA0_V(o1, ax[1], px);
        MFMA_V(o0, am[0], pm); MFMA_V(o1, am[1], pm);
        if (fg < 2) {
            bst8(rVm, (unsigned)fr * 16 + fg * 8, (unsigned)((s * NH + h) * 2048 + (w * 32) * 16), (u32x2){am[0][0], am[0][1]});
            bst8(rVm, (unsigned)fr * 16 + fg * 8, (unsigned)((s * NH + h) * 2048 + (w * 32 + 16) * 16), (u32x2){am[1][0], am[1][1]});
        }
        bf16x8 wf[8];
#pragma unroll
        for (int i = 0; i < 8; ++i) wf[i] = *(const bf16x8*)(word + i * 1024);
        asm volatile("s_nop 7" : "+v"(o0), "+v"(o1));
        if (fr < 8) {
            *(u32x2*)(sO + fr * QK_STRIDE + (w * 32 + fg * 4) * 2) = (u32x2){pack2(o0[0], o0[1]), pack2(o0[2], o0[3])};
            *(u32x2*)(sO + fr * QK_STRIDE + (w * 32 + 16 + fg * 4) * 2) = (u32x2){pack2(o1[0], o1[1]), pack2(o1[2], o1[3])};
        }
        STAMP(6);
        BARRIER();
        STAMP(7);
        bf16x8 of[4];
#pragma unroll
        for (int ks = 0; ks < 4; ++ks) of[ks] = *(const bf16x8*)(ord_ + ks * 64);
        const unsigned tagA = (unsigned)((s + 1) & 1);
        {
            f32x4 a[2];
#pragma unroll
            for (int t = 0; t <= 8; ++t) {
                if (t < 8) {
#pragma unroll
                    for (int ks = 0; ks < 4; ++ks) {
                        bf16x8 f = wf[(t & 1) * 4 + ks];
                        if (ks == 0) MFMA0_V(a[t & 1], f, of[ks]); else MFMA_V(a[t & 1], f, of[ks]);
                        if (t + 2 < 8) wf[(t & 1) * 4 + ks] = *(const bf16x8*)(word + ((t + 2) * 4 + ks) * 1024);
                    }
                }
                if (t >= 1) {
                    if (t == 8) asm volatile("s_nop 7" : "+v"(a[(t - 1) & 1]));
                    else asm volatile("s_nop 3" : "+v"(a[(t - 1) & 1]));
                    f32x4 r = a[(t - 1) & 1];
                    u32x4 o;
#pragma unroll
                    for (int q = 0; q < 4; ++q) o[q] = (__float_as_uint(r[q]) & ~1u) | tagA;
                    if (fr < 8) bst16_sc1(rA, stA_vo, (unsigned)(((w * 8 + t - 1) * NH + h) * 512), o);
                }
            }
        }
        STAMP(8);
        {
            float tot[4];
            for (unsigned spin = 0;; ++spin) {
                if (spin > (1u << 20)) break;
                asm volatile("" ::: "memory");
                u32x4 v[4];
#pragma unroll
                for (int i = 0; i < 4; ++i) v[i] = bld16_sc1(rA, rdA_vo, (unsigned)(h * NH * 512 + i * 4096 + w * 128));
                bool ok = true;
#pragma unroll
                for (int q = 0; q < 4; ++q) {
                    ok &= ((v[0][q] & 1u) == tagA) & ((v[1][q] & 1u) == tagA) & ((v[2][q] & 1u) == tagA) & ((v[3][q] & 1u) == tagA);
                    tot[q] = (__uint_as_float(v[0][q] & ~1u) + __uint_as_float(v[1][q] & ~1u)) +
                             (__uint_as_float(v[2][q] & ~1u) + __uint_as_float(v[3][q] & ~1u));
                }
                if (__all(ok)) break;
            }
#pragma unroll
            for (int q = 0; q < 4; ++q) {
                tot[q] = xsum8(tot[q]);
                tot[q] = xsum16(tot[q]);
                tot[q] = xsum32(tot[q]);
            }
            STAMP(9);
            if (lane < 8) {
                u32x4 o;
#pragma unroll
                for (int q = 0; q < 4; ++q) o[q] = ((unsigned)f2bf(tot[q]) << 16) | (unsigned)(s + 1);
                bst16_sc1(rB, (unsigned)(lane >> 2) * 2048 + (lane & 3) * 16, (unsigned)(w * 4096 + h * 64), o);
            }
        }
    }
}

__global__ void __launch_bounds__(256) attn_k(WS ws) {
    const int lane = threadIdx.x & 63, w = threadIdx.x >> 6;
    const int s = blockIdx.x >> 3, h = (blockIdx.x & 7) * 4 + w;
    const int fr = lane & 15, fg = lane >> 4, fr8 = lane & 7;
    bf16x8 kx[2][4], kmf[4];
#pragma unroll
    for (int ks = 0; ks < 4; ++ks) {
        kmf[ks] = *(const bf16x8*)(ws.km + ((size_t)(s * NH + h) * 8 + fr8) * DH + ks * 32 + fg * 8);
#pragma unroll
        for (int t = 0; t < 2; ++t) kx[t][ks] = *(const bf16x8*)(ws.K + (size_t)(s * SEG + t * 16 + fr) * HD + h * DH + ks * 32 + fg * 8);
    }
    u32x4 px[2], pm[2];
#pragma unroll
    for (int qt = 0; qt < 2; ++qt) {
        f32x4 sm = {0.f, 0.f, 0.f, 0.f}, sx0 = sm, sx1 = sm;
#pragma unroll
        for (int ks = 0; ks < 4; ++ks) {
            bf16x8 qf = *(const bf16x8*)(ws.Q + (size_t)(s * SEG + qt * 16 + fr) * HD + h * DH + ks * 32 + fg * 8);
            sm = __builtin_amdgcn_mfma_f32_16x16x32_bf16(kmf[ks], qf, sm, 0, 0, 0);
            sx0 = __builtin_amdgcn_mfma_f32_16x16x32_bf16(kx[0][ks], qf, sx0, 0, 0, 0);
            sx1 = __builtin_amdgcn_mfma_f32_16x16x32_bf16(kx[1][ks], qf, sx1, 0, 0, 0);
        }
        const int qi = qt * 16 + fr;
        float sv[12];
#pragma unroll
        for (int q = 0; q < 4; ++q) {
            sv[q] = (fg < 2) ? rbf(rbf(sm[q]) * SCALE) : -3.0e38f;
            int k0 = fg * 4 + q, k1 = 16 + fg * 4 + q;
            sv[4 + q] = (k0 <= qi) ? rbf(rbf(sx0[q]) * SCALE) : -3.0e38f;
            sv[8 + q] = (k1 <= qi) ? rbf(rbf(sx1[q]) * SCALE) : -3.0e38f;
        }
        float mx = sv[0];
#pragma unroll
        for (int q = 1; q < 12; ++q) mx = fmaxf(mx, sv[q]);
        mx = fmaxf(mx, __shfl_xor(mx, 16));
        mx = fmaxf(mx, __shfl_xor(mx, 32));
        float sum = 0.f;
#pragma unroll
        for (int q = 0; q < 12; ++q) { sv[q] = __expf(sv[q] - mx); sum += sv[q]; }
        sum += __shfl_xor(sum, 16);
        sum += __shfl_xor(sum, 32);
        float inv = 1.f / sum;
        px[qt] = (u32x4){pack2(sv[4] * inv, sv[5] * inv), pack2(sv[6] * inv, sv[7] * inv), pack2(sv[8] * inv, sv[9] * inv), pack2(sv[10] * inv, sv[11] * inv)};
        pm[qt] = (u32x4){pack2(sv[0] * inv, sv[1] * inv), pack2(sv[2] * inv, sv[3] * inv), 0u, 0u};
    }
#pragma unroll
    for (int et = 0; et < 8; ++et) {
        const u16* p = ws.VT + (size_t)(h * DH + et * 16 + fr) * S_TOK + s * SEG + fg * 4;
        u32x2 a0 = *(const u32x2*)p, a1 = *(const u32x2*)(p + 16);
        u32x2 m0 = {0u, 0u};
        if (fg < 2) m0 = *(const u32x2*)(ws.vmT + ((size_t)(s * NH + h) * DH + et * 16 + fr) * 8 + fg * 4);
        u32x4 ax = {a0[0], a0[1], a1[0], a1[1]};
        u32x4 am = {m0[0], m0[1], 0u, 0u};
#pragma unroll
        for (int qt = 0; qt < 2; ++qt) {
            f32x4 o = {0.f, 0.f, 0.f, 0.f};
            o = __builtin_amdgcn_mfma_f32_16x16x32_bf16(__builtin_bit_cast(bf16x8, ax), __builtin_bit_cast(bf16x8, px[qt]), o, 0, 0, 0);
            o = __builtin_amdgcn_mfma_f32_16x16x32_bf16(__builtin_bit_cast(bf16x8, am), __builtin_bit_cast(bf16x8, pm[qt]), o, 0, 0, 0);
            u32x2 ov = {pack2(o[0], o[1]), pack2(o[2], o[3])};
            *(u32x2*)(ws.Ox + (size_t)(s * SEG + qt * 16 + fr) * HD + h * DH + et * 16 + fg * 4) = ov;
        }
    }
}

extern "C" void kernel_launch(void* const* d_in, const int* in_sizes, int n_in, void* d_out, int out_size, void* d_ws, size_t ws_size,
                              hipStream_t stream) {
    const u16* x = (const u16*)d_in[0];
    const u16* mem0 = (const u16*)d_in[1];
    const u16* Wq = (const u16*)d_in[2];
    const u16* Wk = (const u16*)d_in[3];
    const u16* Wv = (const u16*)d_in[4];
    const u16* Wo = (const u16*)d_in[5];
    const u16* w1 = (const u16*)d_in[8];
    const u16* b1 = (const u16*)d_in[9];
    const u16* w2 = (const u16*)d_in[10];
    const u16* b2 = (const u16*)d_in[11];
    u16* out = (u16*)d_out;

    char* p = (char*)d_ws;
    auto carve = [&](size_t bytes) { char* r = p; p += (bytes + 255) & ~(size_t)255; return r; };
    WS ws;
    ws.exA = (unsigned*)carve((size_t)32 * 32 * 128 * 4);
    ws.exB = (unsigned*)carve((size_t)8 * 512 * 4);
    ws.xn = (u16*)carve((size_t)S_TOK * DM * 2);
    ws.y = (u16*)carve((size_t)S_TOK * DM * 2);
    ws.WqkvT = (u16*)carve((size_t)3 * HD * DM * 2);
    ws.WoT = (u16*)carve((size_t)DM * HD * 2);
    ws.W1T = (u16*)carve((size_t)FFP * DM * 2);
    ws.W2T = (u16*)carve((size_t)DM * FFP * 2);
    ws.Q = (u16*)carve((size_t)S_TOK * HD * 2);
    ws.K = (u16*)carve((size_t)S_TOK * HD * 2);
    ws.VT = (u16*)carve((size_t)S_TOK * HD * 2);
    ws.hmid = (u16*)carve((size_t)S_TOK * FFP * 2);
    ws.g = (u16*)carve((size_t)S_TOK * DM * 2);
    ws.Ox = (u16*)carve((size_t)S_TOK * HD * 2);
    ws.km = (u16*)carve((size_t)NSEG * NH * 8 * DH * 2);
    ws.vmT = (u16*)carve((size_t)NSEG * NH * 8 * DH * 2);
    ws.dbg = (unsigned long long*)((char*)d_ws + (size_t)200 * 1024 * 1024);

    zero_k<<<(32 * 32 * 128 + 8 * 512 + 64 + 255) / 256, 256, 0, stream>>>(ws.exA, 32 * 32 * 128 + 8 * 512);
    prep_ln<<<S_TOK / 4, 256, 0, stream>>>(x, ws.xn, ws.y);
    transpose_k<<<dim3(HD / 64, DM / 64), 256, 0, stream>>>(Wq, ws.WqkvT, DM, HD, DM, HD);
    transpose_k<<<dim3(HD / 64, DM / 64), 256, 0, stream>>>(Wk, ws.WqkvT + (size_t)HD * DM, DM, HD, DM, HD);
    transpose_k<<<dim3(HD / 64, DM / 64), 256, 0, stream>>>(Wv, ws.WqkvT + (size_t)2 * HD * DM, DM, HD, DM, HD);
    transpose_k<<<dim3(DM / 64, HD / 64), 256, 0, stream>>>(Wo, ws.WoT, HD, DM, HD, DM);
    transpose_k<<<dim3(FFP / 64, DM / 64), 256, 0, stream>>>(w1, ws.W1T, DM, FF, DM, FFP);
    transpose_k<<<dim3(DM / 64, FFP / 64), 256, 0, stream>>>(w2, ws.W2T, FF, DM, FFP, DM);

    EpiArgs ea;
    ea = EpiArgs{ws.Q, ws.K, ws.VT, nullptr, nullptr, nullptr};
    gemm_k<EPI_QKV><<<dim3(3 * HD / 128, S_TOK / 128), 256, 0, stream>>>(ws.xn, ws.WqkvT, DM, DM, DM, ea);
    hipFuncSetAttribute((const void*)chain_k, hipFuncAttributeMaxDynamicSharedMemorySize, 147456);
    chain_k<<<NH, 256, 147456, stream>>>(mem0, ws);
    ea = EpiArgs{ws.hmid, nullptr, nullptr, b1, nullptr, nullptr};
    gemm_k<EPI_H><<<dim3(FFP / 128, S_TOK / 128), 256, 0, stream>>>(ws.y, ws.W1T, DM, DM, DM, ea);
    ea = EpiArgs{ws.g, nullptr, nullptr, b2, nullptr, nullptr};
    gemm_k<EPI_G><<<dim3(DM / 128, S_TOK / 128), 256, 0, stream>>>(ws.hmid, ws.W2T, FFP, FFP, FFP, ea);
    attn_k<<<NSEG * 8, 256, 0, stream>>>(ws);
    ea = EpiArgs{out, nullptr, nullptr, nullptr, x, ws.g};
    gemm_k<EPI_OUT><<<dim3(DM / 128, S_TOK / 128), 256, 0, stream>>>(ws.Ox, ws.WoT, HD, HD, HD, ea);
}
```

```cpp
#include <hip/hip_runtime.h>
#include <hip/hip_bf16.h>
#include <stdint.h>

typedef __attribute__((ext_vector_type(8))) short bf16x8;
typedef __attribute__((ext_vector_type(4))) float f32x4;
typedef __attribute__((ext_vector_type(4))) unsigned u32x4;
typedef __attribute__((ext_vector_type(2))) unsigned u32x2;
typedef unsigned short u16;

#define S_TOK 2048
#define DM 512
#define NH 32
#define DH 128
#define HD 4096
#define NMEM 8
#define SEG 32
#define NSEG 64
#define NSTEPS 64
#define FF 1376
#define FFP 1408

__device__ __forceinline__ u16 f2bf(float f) { return __builtin_bit_cast(u16, (__bf16)f); }
__device__ __forceinline__ float bf2f(u16 h) { return __builtin_bit_cast(float, (unsigned)h << 16); }
__device__ __forceinline__ float rbf(float f) { return bf2f(f2bf(f)); }
typedef __attribute__((ext_vector_type(2))) float f32x2;
typedef __attribute__((ext_vector_type(2))) __bf16 bf16x2v;
__device__ __forceinline__ unsigned pack2(float a, float b) { f32x2 v = {a, b}; return __builtin_bit_cast(unsigned, __builtin_convertvector(v, bf16x2v)); }
__device__ __forceinline__ f32x2 rbf2(f32x2 v) { unsigned u = __builtin_bit_cast(unsigned, __builtin_convertvector(v, bf16x2v)); f32x2 r = {__uint_as_float(u << 16), __uint_as_float(u & 0xffff0000u)}; return r; }

struct WS {
    u16 *xn, *y, *WqkvT, *WoT, *W1T, *W2T, *Q, *K, *VT, *hmid, *g, *km, *vmT;
    float* part;
    unsigned *exA, *exB;
    unsigned *flagKV, *flagQ, *cntH, *flagG, *flagM, *cntO;
    unsigned long long* dbg;
};

__device__ __forceinline__ float wave_sum(float v) {
#pragma unroll
    for (int m = 32; m >= 1; m >>= 1) v += __shfl_xor(v, m);
    return v;
}
__device__ __forceinline__ void ln8(float (&v)[8]) {
    float s = 0.f;
#pragma unroll
    for (int i = 0; i < 8; ++i) s += v[i];
    s = wave_sum(s);
    float mean = s * (1.f / 512.f);
    float q = 0.f;
#pragma unroll
    for (int i = 0; i < 8; ++i) { float d = v[i] - mean; q += d * d; }
    q = wave_sum(q);
    float var = rbf(q * (1.f / 512.f));
    float mb = rbf(mean);
    float sd = rbf(sqrtf(rbf(var + 1e-5f)));
#pragma unroll
    for (int i = 0; i < 8; ++i) v[i] = rbf(rbf(v[i] - mb) / sd);
}
__device__ __forceinline__ void transpose_tile(const u16* __restrict__ src, u16* __restrict__ dst, int R, int C, int ldd, int Cpad, int bx, int by, u16 (*t)[66]) {
    int c0 = bx * 64, r0 = by * 64;
    int tx = threadIdx.x & 63, ty = threadIdx.x >> 6;
    for (int i = ty; i < 64; i += 4) {
        int r = r0 + i, c = c0 + tx;
        t[i][tx] = (r < R && c < C) ? src[(size_t)r * C + c] : (u16)0;
    }
    __syncthreads();
    for (int i = ty; i < 64; i += 4) {
        int c = c0 + i, r = r0 + tx;
        if (c < Cpad && r < ldd) dst[(size_t)c * ldd + r] = t[tx][i];
    }
}
#define PREP_LN 512
#define PREP_TQKV 1536
#define PREP_TWO 512
#define PREP_TW1 176
#define PREP_TW2 176
#define ZERO_BYTES ((size_t)64 * 524288 + 68 * 16384 + 65536)
#define PREP_ZERO ((int)(ZERO_BYTES / 65536))
__global__ void __launch_bounds__(256) prep_k(const u16* __restrict__ x, const u16* __restrict__ Wq, const u16* __restrict__ Wk, const u16* __restrict__ Wv,
                                              const u16* __restrict__ Wo, const u16* __restrict__ w1, const u16* __restrict__ w2, WS ws) {
    __shared__ u16 t[64][66];
    int b = blockIdx.x;
    if (b < PREP_ZERO) {
        u32x4 z = {0u, 0u, 0u, 0u};
        u32x4* p = (u32x4*)((char*)ws.exA + (size_t)b * 65536) + threadIdx.x;
#pragma unroll
        for (int i = 0; i < 16; ++i) p[i * 256] = z;
        return;
    }
    b -= PREP_ZERO;
    if (b < PREP_LN) {
        int row = b * 4 + (threadIdx.x >> 6);
        int lane = threadIdx.x & 63;
        u32x4 r = *(const u32x4*)(x + (size_t)row * DM + lane * 8);
        float v[8];
#pragma unroll
        for (int i = 0; i < 4; ++i) { v[2 * i] = bf2f(r[i] & 0xffff); v[2 * i + 1] = bf2f(r[i] >> 16); }
        ln8(v);
        u32x4 o;
#pragma unroll
        for (int i = 0; i < 4; ++i) o[i] = pack2(v[2 * i], v[2 * i + 1]);
        *(u32x4*)(ws.xn + (size_t)row * DM + lane * 8) = o;
        ln8(v);
#pragma unroll
        for (int i = 0; i < 4; ++i) o[i] = pack2(v[2 * i], v[2 * i + 1]);
        *(u32x4*)(ws.y + (size_t)row * DM + lane * 8) = o;
        return;
    }
    b -= PREP_LN;
    if (b < PREP_TQKV) {
        int m = b / 512, r = b % 512;
        const u16* src = m == 0 ? Wq : (m == 1 ? Wk : Wv);
        transpose_tile(src, ws.WqkvT + (size_t)m * HD * DM, DM, HD, DM, HD, r % 64, r / 64, t);
        return;
    }
    b -= PREP_TQKV;
    if (b < PREP_TWO) { transpose_tile(Wo, ws.WoT, HD, DM, HD, DM, b % 8, b / 8, t); return; }
    b -= PREP_TWO;
    if (b < PREP_TW1) { transpose_tile(w1, ws.W1T, DM, FF, DM, FFP, b % 22, b / 22, t); return; }
    b -= PREP_TW1;
    transpose_tile(w2, ws.W2T, FF, DM, FFP, DM, b % 8, b / 8, t);
}

#define SCALE 0.08838834764831845f
#define MEM_STRIDE 1056
#define QK_STRIDE 288

typedef __amdgpu_buffer_rsrc_t rsrc_t;
__device__ __forceinline__ rsrc_t mk_rsrc(const void* p) { return __builtin_amdgcn_make_buffer_rsrc((void*)p, 0, 0x7fffffff, 0x00020000); }
__device__ __forceinline__ u32x4 bld16(rsrc_t r, unsigned vo, unsigned so) { return __builtin_amdgcn_raw_buffer_load_b128(r, vo, so, 0); }
__device__ __forceinline__ u32x4 bld16_sc1(rsrc_t r, unsigned vo, unsigned so) { return __builtin_amdgcn_raw_buffer_load_b128(r, vo, so, 16); }
__device__ __forceinline__ u32x2 bld8(rsrc_t r, unsigned vo, unsigned so) { return __builtin_amdgcn_raw_buffer_load_b64(r, vo, so, 0); }
__device__ __forceinline__ void bst16_sc1(rsrc_t r, unsigned vo, unsigned so, u32x4 v) { __builtin_amdgcn_raw_buffer_store_b128(v, r, vo, so, 16); }
__device__ __forceinline__ void bst8(rsrc_t r, unsigned vo, unsigned so, u32x2 v) { __builtin_amdgcn_raw_buffer_store_b64(v, r, vo, so, 0); }
__device__ __forceinline__ void bst8_sc1(rsrc_t r, unsigned vo, unsigned so, u32x2 v) { __builtin_amdgcn_raw_buffer_store_b64(v, r, vo, so, 16); }

#define STAMP(i)
#define MFMA0_A(acc, a, b) asm volatile("v_mfma_f32_16x16x32_bf16 %0, %1, %2, 0" : "=&v"(acc) : "a"(a), "v"(b))
#define MFMA_A(acc, a, b) asm volatile("v_mfma_f32_16x16x32_bf16 %0, %1, %2, %0" : "+v"(acc) : "a"(a), "v"(b))
#define MFMA0_V(acc, a, b) asm volatile("v_mfma_f32_16x16x32_bf16 %0, %1, %2, 0" : "=&v"(acc) : "v"(a), "v"(b))
#define MFMA_V(acc, a, b) asm volatile("v_mfma_f32_16x16x32_bf16 %0, %1, %2, %0" : "+v"(acc) : "v"(a), "v"(b))
#define BARRIER() asm volatile("s_waitcnt lgkmcnt(0)\n\ts_barrier" ::: "memory")
#define LAS __attribute__((address_space(3)))

__device__ __forceinline__ float xmax16(float x) {
    unsigned u = __float_as_uint(x);
    auto r = __builtin_amdgcn_permlane16_swap(u, u, false, false);
    return fmaxf(__uint_as_float(r[0]), __uint_as_float(r[1]));
}
__device__ __forceinline__ float xmax32(float x) {
    unsigned u = __float_as_uint(x);
    auto r = __builtin_amdgcn_permlane32_swap(u, u, false, false);
    return fmaxf(__uint_as_float(r[0]), __uint_as_float(r[1]));
}
__device__ __forceinline__ float xsum16(float x) {
    unsigned u = __float_as_uint(x);
    auto r = __builtin_amdgcn_permlane16_swap(u, u, false, false);
    return __uint_as_float(r[0]) + __uint_as_float(r[1]);
}
__device__ __forceinline__ float xsum32(float x) {
    unsigned u = __float_as_uint(x);
    auto r = __builtin_amdgcn_permlane32_swap(u, u, false, false);
    return __uint_as_float(r[0]) + __uint_as_float(r[1]);
}
__device__ __forceinline__ float xsum8(float x) {
    return x + __uint_as_float(__builtin_amdgcn_update_dpp(0u, __float_as_uint(x), 0x128, 0xf, 0xf, false));
}

#define DELAY_A 12
#define DELAY_B 12
#define EXA_STEP 524288u
#define EXB_STEP 16384u
__device__ __forceinline__ void chain_body(const u16* __restrict__ mem0, const WS& ws, char* smem, const int h) {
    char* sW = smem;
    char* sKV = smem + 131072;
    char* sMem = smem + 147456;
    char* sQK = sMem + 8 * MEM_STRIDE;
    char* sO = sQK + 16 * QK_STRIDE;
    const int tid = threadIdx.x, lane = tid & 63;
    const int w = __builtin_amdgcn_readfirstlane(tid >> 6);
    const int fr = lane & 15, fg = lane >> 4, fr8 = lane & 7;
    const unsigned l16 = lane * 16;
    char* lw = sW + w * 32768 + l16;

    bf16x8 wqk[3][16], wv[24], wo[24];
#pragma unroll
    for (int t = 0; t < 6; ++t) {
        int col = (t >> 1) * HD + h * DH + w * 32 + (t & 1) * 16 + fr;
        const u16* p = ws.WqkvT + (size_t)col * DM + fg * 8;
#pragma unroll
        for (int ks = 0; ks < 16; ++ks) {
            bf16x8 v = *(const bf16x8*)(p + ks * 32);
            if (t < 3) wqk[t][ks] = v;
            else if (t == 3) *(bf16x8*)(lw + ks * 1024) = v;
            else if (t == 4) wv[ks] = v;
            else if (ks < 8) wv[16 + ks] = v;
            else *(bf16x8*)(lw + (16 + ks - 8) * 1024) = v;
        }
    }
#pragma unroll
    for (int t = 0; t < 8; ++t)
#pragma unroll
        for (int ks = 0; ks < 4; ++ks) {
            bf16x8 v = *(const bf16x8*)(ws.WoT + (size_t)((w * 8 + t) * 16 + fr) * HD + h * DH + ks * 32 + fg * 8);
            if (t < 6) wo[t * 4 + ks] = v;
            else *(bf16x8*)(lw + (24 + (t - 6) * 4 + ks) * 1024) = v;
        }
    const rsrc_t rA = mk_rsrc(ws.exA), rB = mk_rsrc(ws.exB), rK = mk_rsrc(ws.K), rV = mk_rsrc(ws.VT), rKm = mk_rsrc(ws.km), rVm = mk_rsrc(ws.vmT);
    const unsigned stA_vo = (unsigned)fg * 128 + (fr & 7) * 16 + (fr >> 3) * 16384;
    const unsigned rdA_vo = (unsigned)(lane >> 3) * 512 + (lane & 7) * 16;
    const char* memrd = sMem + fr8 * MEM_STRIDE + fg * 16;
    const char* qrd = sQK + fr8 * QK_STRIDE + fg * 16;
    const char* ord_ = sO + fr8 * QK_STRIDE + fg * 16;
    char* memwr = sMem + (lane & 7) * MEM_STRIDE + ((w * 2 + (lane >> 5)) * 16 + ((lane >> 3) & 3) * 4) * 2;

    for (int s = 0; s < NSTEPS; ++s) {
        STAMP(0);
        if ((s & 3) == 0) {
            const unsigned* f = ws.flagKV + (s >> 2) * 64 + (lane & 1) * 32 + h;
            for (unsigned spin = 0; spin < (1u << 22); ++spin) {
                unsigned v = __hip_atomic_load(f, __ATOMIC_RELAXED, __HIP_MEMORY_SCOPE_AGENT);
                if (__all(v != 0)) break;
                __builtin_amdgcn_s_sleep(2);
            }
        }
        {
            const u16* gk = ws.K + (size_t)(s * NH + h) * 4096 + w * 1024 + lane * 8;
            const u16* gv = ws.VT + (size_t)(s * NH + h) * 4096 + w * 1024 + lane * 8;
            __builtin_amdgcn_global_load_lds((const unsigned*)gk, (LAS unsigned*)(sKV + w * 2048), 16, 0, 16);
            __builtin_amdgcn_global_load_lds((const unsigned*)(gk + 512), (LAS unsigned*)(sKV + w * 2048 + 1024), 16, 0, 16);
            __builtin_amdgcn_global_load_lds((const unsigned*)gv, (LAS unsigned*)(sKV + 8192 + w * 2048), 16, 0, 16);
            __builtin_amdgcn_global_load_lds((const unsigned*)(gv + 512), (LAS unsigned*)(sKV + 8192 + w * 2048 + 1024), 16, 0, 16);
        }
        if (s == 0) {
            for (int i = tid; i < 8 * 64; i += 256) {
                int row = i >> 6, c = (i & 63) * 8;
                *(u32x4*)(sMem + row * MEM_STRIDE + c * 2) = *(const u32x4*)(mem0 + row * DM + c);
            }
            asm volatile("s_waitcnt vmcnt(0)" ::: "memory");
        } else {
#pragma unroll 1
            for (int d = 0; d < DELAY_B; ++d) __builtin_amdgcn_s_sleep(1);
            u32x4 vv[4];
            for (unsigned spin = 0;; ++spin) {
                if (spin > (1u << 20)) break;
                asm volatile("" ::: "memory");
#pragma unroll
                for (int i = 0; i < 4; ++i) vv[i] = bld16_sc1(rB, l16, (unsigned)(i * 4096 + w * 1024) + (unsigned)s * EXB_STEP);
                bool ok = true;
#pragma unroll
                for (int i = 0; i < 4; ++i)
#pragma unroll
                    for (int q = 0; q < 4; ++q) ok &= ((vv[i][q] & 0xffff) != 0);
                if (__all(ok)) break;
            }
#pragma unroll
            for (int i = 0; i < 4; ++i) {
                u32x2 o = {(vv[i][0] >> 16) | (vv[i][1] & 0xffff0000u), (vv[i][2] >> 16) | (vv[i][3] & 0xffff0000u)};
                *(u32x2*)(memwr + i * 256) = o;
            }
        }
        bf16x8 kw[2], vw[2];
#pragma unroll
        for (int i = 0; i < 2; ++i) kw[i] = *(const bf16x8*)(lw + i * 1024);
        vw[0] = *(const bf16x8*)(lw + 16 * 1024); vw[1] = *(const bf16x8*)(lw + 17 * 1024);
        STAMP(1);
        BARRIER();
        STAMP(2);
        if (s > 0 && tid == 0) __hip_atomic_store(ws.flagM + (s - 1) * NH + h, 1u, __ATOMIC_RELAXED, __HIP_MEMORY_SCOPE_AGENT);
        f32x4 acc[4], av[2];
        {
            bf16x8 mb[3];
#pragma unroll
            for (int i = 0; i < 3; ++i) mb[i] = *(const bf16x8*)(memrd + i * 64);
#pragma unroll
            for (int ks = 0; ks < 16; ++ks) {
                bf16x8 m = mb[ks % 3];
                bf16x8 k3 = kw[ks & 1];
                bf16x8 v5 = (ks < 8) ? wv[16 + ks] : vw[ks & 1];
                if (ks == 0) {
                    MFMA0_A(acc[0], wqk[0][ks], m); MFMA0_A(acc[1], wqk[1][ks], m); MFMA0_A(acc[2], wqk[2][ks], m); MFMA0_V(acc[3], k3, m);
                    MFMA0_V(av[0], m, wv[ks]); MFMA0_V(av[1], m, v5);
                } else {
                    MFMA_A(acc[0], wqk[0][ks], m); MFMA_A(acc[1], wqk[1][ks], m); MFMA_A(acc[2], wqk[2][ks], m); MFMA_V(acc[3], k3, m);
                    MFMA_V(av[0], m, wv[ks]); MFMA_V(av[1], m, v5);
                }
                if (ks + 3 < 16) mb[ks % 3] = *(const bf16x8*)(memrd + (ks + 3) * 64);
                if (ks + 2 < 16) kw[ks & 1] = *(const bf16x8*)(lw + (ks + 2) * 1024);
                if (ks >= 8 && ks + 2 < 16) vw[ks & 1] = *(const bf16x8*)(lw + (16 + ks + 2 - 8) * 1024);
            }
        }
        STAMP(3);
        asm volatile("s_nop 7" : "+v"(acc[0]), "+v"(acc[1]), "+v"(acc[2]), "+v"(acc[3]), "+v"(av[0]), "+v"(av[1]));
        {
            u32x2 o[4];
#pragma unroll
            for (int t = 0; t < 4; ++t) o[t] = (u32x2){pack2(acc[t][0], acc[t][1]), pack2(acc[t][2], acc[t][3])};
            if (fr < 8) {
#pragma unroll
                for (int t = 0; t < 4; ++t) {
                    int row = (t >> 1) * 8 + fr;
                    int c = w * 32 + (t & 1) * 16 + fg * 4;
                    *(u32x2*)(sQK + row * QK_STRIDE + c * 2) = o[t];
                }
                bst8_sc1(rKm, (unsigned)fr * 256 + fg * 8, (unsigned)((s * NH + h) * 2048 + (w * 32) * 2), o[2]);
                bst8_sc1(rKm, (unsigned)fr * 256 + fg * 8, (unsigned)((s * NH + h) * 2048 + (w * 32 + 16) * 2), o[3]);
            }
        }
        u32x4 am[2], ax[2];
#pragma unroll
        for (int t = 0; t < 2; ++t) {
            am[t] = (u32x4){pack2(av[t][0], av[t][1]), pack2(av[t][2], av[t][3]), 0u, 0u};
            if (fg >= 2) { am[t][0] = 0u; am[t][1] = 0u; }
        }
        if (fg < 2) {
            bst8_sc1(rVm, (unsigned)fr * 16 + fg * 8, (unsigned)((s * NH + h) * 2048 + (w * 32) * 16), (u32x2){am[0][0], am[0][1]});
            bst8_sc1(rVm, (unsigned)fr * 16 + fg * 8, (unsigned)((s * NH + h) * 2048 + (w * 32 + 16) * 16), (u32x2){am[1][0], am[1][1]});
        }
        bf16x8 kx[2][4];
#pragma unroll
        for (int ks = 0; ks < 4; ++ks) {
            kx[0][ks] = *(const bf16x8*)(sKV + ks * 1024 + l16);
            kx[1][ks] = *(const bf16x8*)(sKV + 4096 + ks * 1024 + l16);
        }
        ax[0] = *(const u32x4*)(sKV + 8192 + (w * 2) * 1024 + l16);
        ax[1] = *(const u32x4*)(sKV + 8192 + (w * 2 + 1) * 1024 + l16);
        STAMP(4);
        BARRIER();
        STAMP(5);
        f32x4 sm, sx0, sx1;
        {
            bf16x8 qf[4], kf[4];
#pragma unroll
            for (int ks = 0; ks < 4; ++ks) {
                qf[ks] = *(const bf16x8*)(qrd + ks * 64);
                kf[ks] = *(const bf16x8*)(qrd + 8 * QK_STRIDE + ks * 64);
            }
            MFMA0_V(sx0, kx[0][0], qf[0]); MFMA0_V(sx1, kx[1][0], qf[0]); MFMA0_V(sm, kf[0], qf[0]);
#pragma unroll
            for (int ks = 1; ks < 4; ++ks) { MFMA_V(sx0, kx[0][ks], qf[ks]); MFMA_V(sx1, kx[1][ks], qf[ks]); MFMA_V(sm, kf[ks], qf[ks]); }
        }
        STAMP(10);
        bf16x8 wf[8];
#pragma unroll
        for (int i = 0; i < 8; ++i) wf[i] = *(const bf16x8*)(lw + (24 + i) * 1024);
        asm volatile("s_nop 7" : "+v"(sm), "+v"(sx0), "+v"(sx1));
        const f32x2 sc2 = {SCALE, SCALE};
        f32x2 p0 = rbf2(rbf2((f32x2){sx0[0], sx0[1]}) * sc2);
        f32x2 p1 = rbf2(rbf2((f32x2){sx0[2], sx0[3]}) * sc2);
        f32x2 p2 = rbf2(rbf2((f32x2){sx1[0], sx1[1]}) * sc2);
        f32x2 p3 = rbf2(rbf2((f32x2){sx1[2], sx1[3]}) * sc2);
        f32x2 p4 = rbf2(rbf2((f32x2){sm[0], sm[1]}) * sc2);
        f32x2 p5 = rbf2(rbf2((f32x2){sm[2], sm[3]}) * sc2);
        if (fg >= 2) { p4 = (f32x2){-3.0e38f, -3.0e38f}; p5 = p4; }
        float mx = fmaxf(fmaxf(fmaxf(p0[0], p0[1]), fmaxf(p1[0], p1[1])), fmaxf(fmaxf(p2[0], p2[1]), fmaxf(p3[0], p3[1])));
        mx = fmaxf(mx, fmaxf(fmaxf(p4[0], p4[1]), fmaxf(p5[0], p5[1])));
        mx = xmax16(mx);
        mx = xmax32(mx);
        const float L2E = 1.4426950408889634f;
        const f32x2 l2 = {L2E, L2E};
        const f32x2 mxl = {mx, mx};
        p0 = (p0 - mxl) * l2; p1 = (p1 - mxl) * l2; p2 = (p2 - mxl) * l2; p3 = (p3 - mxl) * l2; p4 = (p4 - mxl) * l2; p5 = (p5 - mxl) * l2;
        p0 = (f32x2){__builtin_amdgcn_exp2f(p0[0]), __builtin_amdgcn_exp2f(p0[1])};
        p1 = (f32x2){__builtin_amdgcn_exp2f(p1[0]), __builtin_amdgcn_exp2f(p1[1])};
        p2 = (f32x2){__builtin_amdgcn_exp2f(p2[0]), __builtin_amdgcn_exp2f(p2[1])};
        p3 = (f32x2){__builtin_amdgcn_exp2f(p3[0]), __builtin_amdgcn_exp2f(p3[1])};
        p4 = (f32x2){__builtin_amdgcn_exp2f(p4[0]), __builtin_amdgcn_exp2f(p4[1])};
        p5 = (f32x2){__builtin_amdgcn_exp2f(p5[0]), __builtin_amdgcn_exp2f(p5[1])};
        f32x2 s2 = ((p0 + p1) + (p2 + p3)) + (p4 + p5);
        float sum = s2[0] + s2[1];
        sum = xsum16(sum);
        sum = xsum32(sum);
        float inv = __builtin_amdgcn_rcpf(sum);
        const f32x2 iv = {inv, inv};
        p0 *= iv; p1 *= iv; p2 *= iv; p3 *= iv; p4 *= iv; p5 *= iv;
        u32x4 px = {pack2(p0[0], p0[1]), pack2(p1[0], p1[1]), pack2(p2[0], p2[1]), pack2(p3[0], p3[1])};
        u32x4 pm = {pack2(p4[0], p4[1]), pack2(p5[0], p5[1]), 0u, 0u};
        STAMP(12);
        f32x4 o0, o1;
        asm volatile("s_nop 1" : "+v"(px), "+v"(pm), "+v"(am[0]), "+v"(am[1]), "+v"(ax[0]), "+v"(ax[1]));
        MFMA0_V(o0, ax[0], px); MFMA0_V(o1, ax[1], px);
        MFMA_V(o0, am[0], pm); MFMA_V(o1, am[1], pm);
        asm volatile("s_nop 7" : "+v"(o0), "+v"(o1));
        if (fr < 8) {
            *(u32x2*)(sO + fr * QK_STRIDE + (w * 32 + fg * 4) * 2) = (u32x2){pack2(o0[0], o0[1]), pack2(o0[2], o0[3])};
            *(u32x2*)(sO + fr * QK_STRIDE + (w * 32 + 16 + fg * 4) * 2) = (u32x2){pack2(o1[0], o1[1]), pack2(o1[2], o1[3])};
        }
        STAMP(6);
        BARRIER();
        STAMP(7);
        bf16x8 of[4];
#pragma unroll
        for (int ks = 0; ks < 4; ++ks) of[ks] = *(const bf16x8*)(ord_ + ks * 64);
        {
            f32x4 a[4];
#pragma unroll
            for (int p = 0; p <= 4; ++p) {
                if (p < 4) {
#pragma unroll
                    for (int u = 0; u < 2; ++u) {
                        const int t = 2 * p + u;
                        f32x4& ac = a[(p & 1) * 2 + u];
#pragma unroll
                        for (int ks = 0; ks < 4; ++ks) {
                            if (t < 4) { if (ks == 0) MFMA0_A(ac, wo[t * 4 + ks], of[ks]); else MFMA_A(ac, wo[t * 4 + ks], of[ks]); }
                            else {
                                bf16x8 f = (t < 6) ? wo[t * 4 + ks] : wf[(t - 6) * 4 + ks];
                                if (ks == 0) MFMA0_V(ac, f, of[ks]); else MFMA_V(ac, f, of[ks]);
                            }
                        }
                    }
                }
                if (p >= 1) {
                    const int q0 = ((p - 1) & 1) * 2;
                    if (p == 4) asm volatile("s_nop 7" : "+v"(a[q0]), "+v"(a[q0 + 1]));
                    u32x4 o;
#pragma unroll
                    for (int q = 0; q < 4; ++q) o[q] = __float_as_uint(fr < 8 ? a[q0][q] : a[q0 + 1][q]) | 1u;
                    bst16_sc1(rA, stA_vo, (unsigned)(((w * 8 + 2 * (p - 1)) * NH + h) * 512) + (unsigned)s * EXA_STEP, o);
                }
            }
        }
        STAMP(8);
        {
#pragma unroll 1
            for (int d = 0; d < DELAY_A; ++d) __builtin_amdgcn_s_sleep(1);
            float tot[4];
            for (unsigned spin = 0;; ++spin) {
                if (spin > (1u << 20)) break;
                asm volatile("" ::: "memory");
                u32x4 v[4];
#pragma unroll
                for (int i = 0; i < 4; ++i) v[i] = bld16_sc1(rA, rdA_vo, (unsigned)(h * NH * 512 + i * 4096 + w * 128) + (unsigned)s * EXA_STEP);
                bool ok = true;
#pragma unroll
                for (int q = 0; q < 4; ++q) {
                    ok &= ((v[0][q] & v[1][q] & v[2][q] & v[3][q] & 1u) != 0);
                    tot[q] = (__uint_as_float(v[0][q]) + __uint_as_float(v[1][q])) + (__uint_as_float(v[2][q]) + __uint_as_float(v[3][q]));
                }
                if (__all(ok)) break;
            }
#pragma unroll
            for (int q = 0; q < 4; ++q) {
                tot[q] = xsum8(tot[q]);
                tot[q] = xsum16(tot[q]);
                tot[q] = xsum32(tot[q]);
            }
            STAMP(9);
            if (lane < 8) {
                u32x4 o;
#pragma unroll
                for (int q = 0; q < 4; ++q) o[q] = ((unsigned)f2bf(tot[q]) << 16) | 1u;
                bst16_sc1(rB, l16, (unsigned)(h * 512 + w * 128) + (unsigned)(s + 1) * EXB_STEP, o);
            }
        }
    }
    asm volatile("s_waitcnt vmcnt(0)" ::: "memory");
    __syncthreads();
    if (tid == 0) __hip_atomic_store(ws.flagM + (NSTEPS - 1) * NH + h, 1u, __ATOMIC_RELAXED, __HIP_MEMORY_SCOPE_AGENT);
}

enum { EPI_QKV = 0, EPI_H = 1, EPI_G = 2 };
__device__ __forceinline__ float gelu_tanh(float v) {
    float u = 0.7978845608028654f * (v + 0.044715f * v * v * v);
    return 0.5f * v * (1.f + tanhf(u));
}
__device__ __forceinline__ void wait_flag_ge(const unsigned* f, unsigned want) {
    for (unsigned spin = 0; spin < (1u << 22); ++spin) {
        unsigned v = __hip_atomic_load(f, __ATOMIC_RELAXED, __HIP_MEMORY_SCOPE_AGENT);
        if (__all(v >= want)) break;
        __builtin_amdgcn_s_sleep(8);
    }
}
__device__ __forceinline__ void acquire_all() {
    __builtin_amdgcn_fence(__ATOMIC_ACQUIRE, "agent");
    asm volatile("s_waitcnt vmcnt(0)" ::: "memory");
    __syncthreads();
}
__device__ __forceinline__ void publish_done() {
    asm volatile("s_waitcnt vmcnt(0)" ::: "memory");
    __syncthreads();
}

template <int EPI>
__device__ __forceinline__ void gemm_tile(const u16* __restrict__ A, const u16* __restrict__ Bt, int lda, int ldb, int K, int brow, int bcol,
                                          const WS& ws, const u16* __restrict__ bias, char* lds_) {
    char (*lds)[2][128 * 128] = (char (*)[2][128 * 128])lds_;
    const int tid = threadIdx.x, lane = tid & 63, w = tid >> 6;
    const int wr = w >> 1, wc = w & 1;
    const int fr = lane & 15, fg = lane >> 4;
    f32x4 acc[4][4];
#pragma unroll
    for (int i = 0; i < 4; ++i)
#pragma unroll
        for (int j = 0; j < 4; ++j) acc[i][j] = (f32x4){0.f, 0.f, 0.f, 0.f};
    const int srow = tid >> 3, sslot = tid & 7;
    const u16* ga = A + (size_t)(brow + srow) * lda + sslot * 8;
    const u16* gb = Bt + (size_t)(bcol + srow) * ldb + sslot * 8;
    u32x4 ra[4], rb[4];
    auto gload = [&](int kt) {
#pragma unroll
        for (int i = 0; i < 4; ++i) {
            ra[i] = *(const u32x4*)(ga + (size_t)(32 * i) * lda + kt * 64);
            rb[i] = *(const u32x4*)(gb + (size_t)(32 * i) * ldb + kt * 64);
        }
    };
    auto swrite = [&](int buf) {
#pragma unroll
        for (int i = 0; i < 4; ++i) {
            int r = srow + 32 * i;
            int off = r * 128 + ((sslot ^ (r & 7)) << 4);
            *(u32x4*)(&lds[buf][0][off]) = ra[i];
            *(u32x4*)(&lds[buf][1][off]) = rb[i];
        }
    };
    const int nt = K / 64;
    gload(0);
    swrite(0);
    __syncthreads();
    const bool vtile = (EPI == EPI_QKV) && (bcol >= 2 * HD);
    for (int kt = 0; kt < nt; ++kt) {
        int buf = kt & 1;
        if (kt + 1 < nt) gload(kt + 1);
#pragma unroll
        for (int kk = 0; kk < 2; ++kk) {
            bf16x8 af[4], bfr[4];
#pragma unroll
            for (int i = 0; i < 4; ++i) {
                int r = wr * 64 + i * 16 + fr;
                af[i] = *(const bf16x8*)(&lds[buf][0][r * 128 + (((kk * 4 + fg) ^ (r & 7)) << 4)]);
                int c = wc * 64 + i * 16 + fr;
                bfr[i] = *(const bf16x8*)(&lds[buf][1][c * 128 + (((kk * 4 + fg) ^ (c & 7)) << 4)]);
            }
            if (vtile) {
#pragma unroll
                for (int i = 0; i < 4; ++i)
#pragma unroll
                    for (int j = 0; j < 4; ++j) acc[i][j] = __builtin_amdgcn_mfma_f32_16x16x32_bf16(af[i], bfr[j], acc[i][j], 0, 0, 0);
            } else {
#pragma unroll
                for (int i = 0; i < 4; ++i)
#pragma unroll
                    for (int j = 0; j < 4; ++j) acc[i][j] = __builtin_amdgcn_mfma_f32_16x16x32_bf16(bfr[j], af[i], acc[i][j], 0, 0, 0);
            }
        }
        if (kt + 1 < nt) swrite(buf ^ 1);
        __syncthreads();
    }
    const rsrc_t rQ = mk_rsrc(ws.Q), rK = mk_rsrc(ws.K), rV = mk_rsrc(ws.VT), rH = mk_rsrc(ws.hmid), rG = mk_rsrc(ws.g);
#pragma unroll
    for (int i = 0; i < 4; ++i)
#pragma unroll
        for (int j = 0; j < 4; ++j) {
            f32x4 a = acc[i][j];
            if (vtile) {
                int col = bcol + wc * 64 + j * 16 + fr - 2 * HD;
                int tok = brow + wr * 64 + i * 16 + fg * 4;
                int seg = tok >> 5, tt = (tok >> 4) & 1, hh = col >> 7, et = (col >> 4) & 7;
                u32x2 o = {pack2(a[0], a[1]), pack2(a[2], a[3])};
                bst8_sc1(rV, (unsigned)((((seg * NH + hh) * 8 + et) * 512 + (fg * 16 + fr) * 8 + tt * 4) * 2), 0, o);
            } else {
                int tok = brow + wr * 64 + i * 16 + fr;
                int col = bcol + wc * 64 + j * 16 + fg * 4;
                if (EPI == EPI_QKV) {
                    int cc = col & (HD - 1);
                    int seg = tok >> 5, tt = (tok >> 4) & 1, hh = cc >> 7, e = cc & 127;
                    u32x2 o = {pack2(a[0], a[1]), pack2(a[2], a[3])};
                    unsigned off = (unsigned)((((seg * NH + hh) * 8 + tt * 4 + (e >> 5)) * 512 + (((e >> 3) & 3) * 16 + (tok & 15)) * 8 + (e & 4)) * 2);
                    if (col >= HD) bst8_sc1(rK, off, 0, o); else bst8_sc1(rQ, off, 0, o);
                } else if (EPI == EPI_H) {
                    float r[4];
#pragma unroll
                    for (int q = 0; q < 4; ++q) {
                        float b = (col + q < FF) ? bf2f(bias[col + q]) : 0.f;
                        float v = rbf(rbf(a[q]) + b);
                        r[q] = (col + q < FF) ? gelu_tanh(v) : 0.f;
                    }
                    u32x2 o = {pack2(r[0], r[1]), pack2(r[2], r[3])};
                    bst8_sc1(rH, (unsigned)((tok * FFP + col) * 2), 0, o);
                } else {
                    float r[4];
#pragma unroll
                    for (int q = 0; q < 4; ++q) {
                        float v = rbf(rbf(a[q]) + bf2f(bias[col + q]));
                        r[q] = v / (1.f + __expf(-v));
                    }
                    u32x2 o = {pack2(r[0], r[1]), pack2(r[2], r[3])};
                    bst8_sc1(rG, (unsigned)((tok * DM + col) * 2), 0, o);
                }
            }
        }
}

__device__ __forceinline__ void attn_task(int s, int hg, const WS& ws, char* lds) {
    const int lane = threadIdx.x & 63, w = threadIdx.x >> 6;
    const int h = hg * 4 + w;
    const int fr = lane & 15, fg = lane >> 4, fr8 = lane & 7;
    char* sO = lds;
    const u16* KF = ws.K + (size_t)(s * NH + h) * 4096;
    const u16* QF = ws.Q + (size_t)(s * NH + h) * 4096;
    const u16* VF = ws.VT + (size_t)(s * NH + h) * 4096;
    bf16x8 kx[2][4], kmf[4];
#pragma unroll
    for (int ks = 0; ks < 4; ++ks) {
        kmf[ks] = *(const bf16x8*)(ws.km + ((size_t)(s * NH + h) * 8 + fr8) * DH + ks * 32 + fg * 8);
#pragma unroll
        for (int t = 0; t < 2; ++t) kx[t][ks] = *(const bf16x8*)(KF + (t * 4 + ks) * 512 + lane * 8);
    }
    u32x4 px[2], pm[2];
#pragma unroll
    for (int qt = 0; qt < 2; ++qt) {
        f32x4 sm = {0.f, 0.f, 0.f, 0.f}, sx0 = sm, sx1 = sm;
#pragma unroll
        for (int ks = 0; ks < 4; ++ks) {
            bf16x8 qf = *(const bf16x8*)(QF + (qt * 4 + ks) * 512 + lane * 8);
            sm = __builtin_amdgcn_mfma_f32_16x16x32_bf16(kmf[ks], qf, sm, 0, 0, 0);
            sx0 = __builtin_amdgcn_mfma_f32_16x16x32_bf16(kx[0][ks], qf, sx0, 0, 0, 0);
            sx1 = __builtin_amdgcn_mfma_f32_16x16x32_bf16(kx[1][ks], qf, sx1, 0, 0, 0);
        }
        const int qi = qt * 16 + fr;
        float sv[12];
#pragma unroll
        for (int q = 0; q < 4; ++q) {
            sv[q] = (fg < 2) ? rbf(rbf(sm[q]) * SCALE) : -3.0e38f;
            int k0 = fg * 4 + q, k1 = 16 + fg * 4 + q;
            sv[4 + q] = (k0 <= qi) ? rbf(rbf(sx0[q]) * SCALE) : -3.0e38f;
            sv[8 + q] = (k1 <= qi) ? rbf(rbf(sx1[q]) * SCALE) : -3.0e38f;
        }
        float mx = sv[0];
#pragma unroll
        for (int q = 1; q < 12; ++q) mx = fmaxf(mx, sv[q]);
        mx = xmax16(mx); mx = xmax32(mx);
        float sum = 0.f;
#pragma unroll
        for (int q = 0; q < 12; ++q) { sv[q] = __expf(sv[q] - mx); sum += sv[q]; }
        sum = xsum16(sum); sum = xsum32(sum);
        float inv = 1.f / sum;
        px[qt] = (u32x4){pack2(sv[4] * inv, sv[5] * inv), pack2(sv[6] * inv, sv[7] * inv), pack2(sv[8] * inv, sv[9] * inv), pack2(sv[10] * inv, sv[11] * inv)};
        pm[qt] = (u32x4){pack2(sv[0] * inv, sv[1] * inv), pack2(sv[2] * inv, sv[3] * inv), 0u, 0u};
    }
#pragma unroll
    for (int et = 0; et < 8; ++et) {
        u32x4 ax = *(const u32x4*)(VF + et * 512 + lane * 8);
        u32x2 m0 = {0u, 0u};
        if (fg < 2) m0 = *(const u32x2*)(ws.vmT + ((size_t)(s * NH + h) * DH + et * 16 + fr) * 8 + fg * 4);
        u32x4 am = {m0[0], m0[1], 0u, 0u};
#pragma unroll
        for (int qt = 0; qt < 2; ++qt) {
            f32x4 o = {0.f, 0.f, 0.f, 0.f};
            o = __builtin_amdgcn_mfma_f32_16x16x32_bf16(__builtin_bit_cast(bf16x8, ax), __builtin_bit_cast(bf16x8, px[qt]), o, 0, 0, 0);
            o = __builtin_amdgcn_mfma_f32_16x16x32_bf16(__builtin_bit_cast(bf16x8, am), __builtin_bit_cast(bf16x8, pm[qt]), o, 0, 0, 0);
            u32x2 ov = {pack2(o[0], o[1]), pack2(o[2], o[3])};
            *(u32x2*)(sO + (qt * 16 + fr) * 1040 + (w * DH + et * 16 + fg * 4) * 2) = ov;
        }
    }
    __syncthreads();
    f32x4 acc[8][2];
#pragma unroll
    for (int t = 0; t < 8; ++t) { acc[t][0] = (f32x4){0.f, 0.f, 0.f, 0.f}; acc[t][1] = acc[t][0]; }
    const u16* wp = ws.WoT + (size_t)(w * 128 + fr) * HD + hg * 512 + fg * 8;
#pragma unroll 2
    for (int ks = 0; ks < 16; ++ks) {
        bf16x8 b0 = *(const bf16x8*)(sO + fr * 1040 + (ks * 32 + fg * 8) * 2);
        bf16x8 b1 = *(const bf16x8*)(sO + (16 + fr) * 1040 + (ks * 32 + fg * 8) * 2);
#pragma unroll
        for (int t = 0; t < 8; ++t) {
            bf16x8 a = *(const bf16x8*)(wp + (size_t)t * 16 * HD + ks * 32);
            acc[t][0] = __builtin_amdgcn_mfma_f32_16x16x32_bf16(a, b0, acc[t][0], 0, 0, 0);
            acc[t][1] = __builtin_amdgcn_mfma_f32_16x16x32_bf16(a, b1, acc[t][1], 0, 0, 0);
        }
    }
    const rsrc_t rP = mk_rsrc(ws.part);
#pragma unroll
    for (int t = 0; t < 8; ++t)
#pragma unroll
        for (int qt = 0; qt < 2; ++qt) {
            u32x4 o = {__float_as_uint(acc[t][qt][0]), __float_as_uint(acc[t][qt][1]), __float_as_uint(acc[t][qt][2]), __float_as_uint(acc[t][qt][3])};
            unsigned off = (unsigned)((((s * 8 + hg) * 32 + qt * 16 + fr) * 512 + w * 128 + t * 16 + fg * 4) * 4);
            bst16_sc1(rP, off, 0, o);
        }
}

__device__ __forceinline__ void reduce_task(int s, const u16* __restrict__ x, u16* __restrict__ out, const WS& ws) {
    const int tid = threadIdx.x;
    for (int i = tid; i < 32 * 128; i += 256) {
        int row = i >> 7, c = (i & 127) * 4;
        f32x4 t = {0.f, 0.f, 0.f, 0.f};
#pragma unroll
        for (int hg = 0; hg < 8; ++hg) t += *(const f32x4*)(ws.part + ((size_t)(s * 8 + hg) * 32 + row) * 512 + c);
        int tok = s * SEG + row;
        u32x2 xv = *(const u32x2*)(x + (size_t)tok * DM + c);
        u32x2 gv = *(const u32x2*)(ws.g + (size_t)tok * DM + c);
        float xs[4] = {bf2f(xv[0] & 0xffff), bf2f(xv[0] >> 16), bf2f(xv[1] & 0xffff), bf2f(xv[1] >> 16)};
        float gs[4] = {bf2f(gv[0] & 0xffff), bf2f(gv[0] >> 16), bf2f(gv[1] & 0xffff), bf2f(gv[1] >> 16)};
        float r[4];
#pragma unroll
        for (int q = 0; q < 4; ++q) r[q] = xs[q] + rbf(rbf(t[q]) * gs[q]);
        u32x2 o = {pack2(r[0], r[1]), pack2(r[2], r[3])};
        *(u32x2*)(out + (size_t)tok * DM + c) = o;
    }
}

#define NWORK 224
#define T_KV 1024
#define T_H 176
#define T_Q 512
#define T_G 64
#define T_A 512
#define T_TOTAL (T_KV + T_H + T_Q + T_G + T_A)
#define LDS_BYTES 162816

__global__ void __launch_bounds__(256, 1) mega_k(const u16* __restrict__ x, const u16* __restrict__ mem0, const u16* __restrict__ b1, const u16* __restrict__ b2,
                                                 u16* __restrict__ out, WS ws) {
    extern __shared__ __attribute__((aligned(16))) char smem[];
    if (blockIdx.x < NH) {
        chain_body(mem0, ws, smem, blockIdx.x);
        return;
    }
    const int tid = threadIdx.x, lane = tid & 63, w = tid >> 6;
    __shared__ int s_last;
    for (int task = blockIdx.x - NH; task < T_TOTAL; task += NWORK) {
        int t = task;
        if (t < T_KV) {
            int tb = t >> 6, cb = t & 63;
            gemm_tile<EPI_QKV>(ws.xn, ws.WqkvT, DM, DM, DM, tb * 128, HD + cb * 128, ws, nullptr, smem);
            publish_done();
            if (tid == 0) __hip_atomic_store(ws.flagKV + tb * 64 + cb, 1u, __ATOMIC_RELAXED, __HIP_MEMORY_SCOPE_AGENT);
            continue;
        }
        t -= T_KV;
        if (t < T_H) {
            int tb = t / 11, cb = t % 11;
            gemm_tile<EPI_H>(ws.y, ws.W1T, DM, DM, DM, tb * 128, cb * 128, ws, b1, smem);
            publish_done();
            if (tid == 0) __hip_atomic_fetch_add(ws.cntH + tb, 1u, __ATOMIC_RELAXED, __HIP_MEMORY_SCOPE_AGENT);
            continue;
        }
        t -= T_H;
        if (t < T_Q) {
            int tb = t >> 5, cb = t & 31;
            gemm_tile<EPI_QKV>(ws.xn, ws.WqkvT, DM, DM, DM, tb * 128, cb * 128, ws, nullptr, smem);
            publish_done();
            if (tid == 0) __hip_atomic_store(ws.flagQ + tb * 32 + cb, 1u, __ATOMIC_RELAXED, __HIP_MEMORY_SCOPE_AGENT);
            continue;
        }
        t -= T_Q;
        if (t < T_G) {
            int tb = t >> 2, cb = t & 3;
            if (w == 0) wait_flag_ge(ws.cntH + tb, 11u);
            acquire_all();
            gemm_tile<EPI_G>(ws.hmid, ws.W2T, FFP, FFP, FFP, tb * 128, cb * 128, ws, b2, smem);
            publish_done();
            if (tid == 0) __hip_atomic_store(ws.flagG + tb * 4 + cb, 1u, __ATOMIC_RELAXED, __HIP_MEMORY_SCOPE_AGENT);
            continue;
        }
        t -= T_G;
        {
            int s = t >> 3, hg = t & 7, tb = s >> 2;
            if (w == 0) {
                const unsigned* f;
                int l = lane & 15, hh = hg * 4 + (l & 3);
                if (l < 4) f = ws.flagKV + tb * 64 + hh;
                else if (l < 8) f = ws.flagKV + tb * 64 + 32 + hh;
                else if (l < 12) f = ws.flagQ + tb * 32 + hh;
                else f = ws.flagM + s * NH + hh;
                wait_flag_ge(f, 1u);
            }
            acquire_all();
            attn_task(s, hg, ws, smem);
            publish_done();
            if (tid == 0) s_last = (__hip_atomic_fetch_add(ws.cntO + s, 1u, __ATOMIC_RELAXED, __HIP_MEMORY_SCOPE_AGENT) == 7u);
            __syncthreads();
            if (s_last) {
                if (w == 0) wait_flag_ge(ws.flagG + tb * 4 + (lane & 3), 1u);
                acquire_all();
                reduce_task(s, x, out, ws);
            }
            __syncthreads();
        }
    }
}

extern "C" void kernel_launch(void* const* d_in, const int* in_sizes, int n_in, void* d_out, int out_size, void* d_ws, size_t ws_size,
                              hipStream_t stream) {
    const u16* x = (const u16*)d_in[0];
    const u16* mem0 = (const u16*)d_in[1];
    const u16* Wq = (const u16*)d_in[2];
    const u16* Wk = (const u16*)d_in[3];
    const u16* Wv = (const u16*)d_in[4];
    const u16* Wo = (const u16*)d_in[5];
    const u16* w1 = (const u16*)d_in[8];
    const u16* b1 = (const u16*)d_in[9];
    const u16* w2 = (const u16*)d_in[10];
    const u16* b2 = (const u16*)d_in[11];
    u16* out = (u16*)d_out;

    char* p = (char*)d_ws;
    auto carve = [&](size_t bytes) { char* r = p; p += (bytes + 255) & ~(size_t)255; return r; };
    WS ws;
    ws.exA = (unsigned*)carve((size_t)64 * 524288);
    ws.exB = (unsigned*)carve((size_t)68 * 16384);
    unsigned* fl = (unsigned*)carve(65536);
    ws.flagKV = fl; ws.flagQ = fl + 1024; ws.cntH = fl + 1536; ws.flagG = fl + 1600; ws.flagM = fl + 2048; ws.cntO = fl + 4096;
    ws.xn = (u16*)carve((size_t)S_TOK * DM * 2);
    ws.y = (u16*)carve((size_t)S_TOK * DM * 2);
    ws.WqkvT = (u16*)carve((size_t)3 * HD * DM * 2);
    ws.WoT = (u16*)carve((size_t)DM * HD * 2);
    ws.W1T = (u16*)carve((size_t)FFP * DM * 2);
    ws.W2T = (u16*)carve((size_t)DM * FFP * 2);
    ws.Q = (u16*)carve((size_t)S_TOK * HD * 2);
    ws.K = (u16*)carve((size_t)S_TOK * HD * 2);
    ws.VT = (u16*)carve((size_t)S_TOK * HD * 2);
    ws.hmid = (u16*)carve((size_t)S_TOK * FFP * 2);
    ws.g = (u16*)carve((size_t)S_TOK * DM * 2);
    ws.km = (u16*)carve((size_t)NSEG * NH * 8 * DH * 2);
    ws.vmT = (u16*)carve((size_t)NSEG * NH * 8 * DH * 2);
    ws.part = (float*)carve((size_t)NSEG * 8 * 32 * 512 * 4);
    ws.dbg = (unsigned long long*)((char*)d_ws + (size_t)240 * 1024 * 1024);

    const int nprep = PREP_ZERO + PREP_LN + PREP_TQKV + PREP_TWO + PREP_TW1 + PREP_TW2;
    prep_k<<<nprep, 256, 0, stream>>>(x, Wq, Wk, Wv, Wo, w1, w2, ws);
    (void)hipFuncSetAttribute((const void*)mega_k, hipFuncAttributeMaxDynamicSharedMemorySize, LDS_BYTES);
    mega_k<<<256, 256, LDS_BYTES, stream>>>(x, mem0, b1, b2, out, ws);
}
```

```cpp
#include <hip/hip_runtime.h>
#include <hip/hip_bf16.h>
#include <stdint.h>

typedef __attribute__((ext_vector_type(8))) short bf16x8;
typedef __attribute__((ext_vector_type(4))) float f32x4;
typedef __attribute__((ext_vector_type(4))) unsigned u32x4;
typedef __attribute__((ext_vector_type(2))) unsigned u32x2;
typedef unsigned short u16;

#define S_TOK 2048
#define DM 512
#define NH 32
#define DH 128
#define HD 4096
#define NMEM 8
#define SEG 32
#define NSEG 64
#define NSTEPS 64
#define FF 1376
#define FFP 1408

__device__ __forceinline__ u16 f2bf(float f) { return __builtin_bit_cast(u16, (__bf16)f); }
__device__ __forceinline__ float bf2f(u16 h) { return __builtin_bit_cast(float, (unsigned)h << 16); }
__device__ __forceinline__ float rbf(float f) { return bf2f(f2bf(f)); }
typedef __attribute__((ext_vector_type(2))) float f32x2;
typedef __attribute__((ext_vector_type(2))) __bf16 bf16x2v;
__device__ __forceinline__ unsigned pack2(float a, float b) { f32x2 v = {a, b}; return __builtin_bit_cast(unsigned, __builtin_convertvector(v, bf16x2v)); }
__device__ __forceinline__ f32x2 rbf2(f32x2 v) { unsigned u = __builtin_bit_cast(unsigned, __builtin_convertvector(v, bf16x2v)); f32x2 r = {__uint_as_float(u << 16), __uint_as_float(u & 0xffff0000u)}; return r; }

constexpr size_t OFF_exA = 0;
constexpr size_t OFF_exB = OFF_exA + (((size_t)64 * 524288) + 255) / 256 * 256;
constexpr size_t OFF_flags = OFF_exB + (((size_t)68 * 16384) + 255) / 256 * 256;
constexpr size_t OFF_xn = OFF_flags + (((size_t)65536) + 255) / 256 * 256;
constexpr size_t OFF_y = OFF_xn + (((size_t)S_TOK * DM * 2) + 255) / 256 * 256;
constexpr size_t OFF_WqkvT = OFF_y + (((size_t)S_TOK * DM * 2) + 255) / 256 * 256;
constexpr size_t OFF_WqkvF = OFF_WqkvT + (((size_t)3 * HD * DM * 2) + 255) / 256 * 256;
constexpr size_t OFF_WoF = OFF_WqkvF + (((size_t)3 * HD * DM * 2) + 255) / 256 * 256;
constexpr size_t OFF_W1T = OFF_WoF + (((size_t)DM * HD * 2) + 255) / 256 * 256;
constexpr size_t OFF_W2T = OFF_W1T + (((size_t)FFP * DM * 2) + 255) / 256 * 256;
constexpr size_t OFF_Q = OFF_W2T + (((size_t)DM * FFP * 2) + 255) / 256 * 256;
constexpr size_t OFF_K = OFF_Q + (((size_t)S_TOK * HD * 2) + 255) / 256 * 256;
constexpr size_t OFF_VT = OFF_K + (((size_t)S_TOK * HD * 2) + 255) / 256 * 256;
constexpr size_t OFF_hmid = OFF_VT + (((size_t)S_TOK * HD * 2) + 255) / 256 * 256;
constexpr size_t OFF_g = OFF_hmid + (((size_t)S_TOK * FFP * 2) + 255) / 256 * 256;
constexpr size_t OFF_km = OFF_g + (((size_t)S_TOK * DM * 2) + 255) / 256 * 256;
constexpr size_t OFF_vmT = OFF_km + (((size_t)NSEG * NH * 8 * DH * 2) + 255) / 256 * 256;
constexpr size_t OFF_part = OFF_vmT + (((size_t)NSEG * NH * 8 * DH * 2) + 255) / 256 * 256;
constexpr size_t OFF_dbg = (size_t)240 * 1024 * 1024;
struct WS {
    char* base;
    __host__ __device__ unsigned* exA_() const { return (unsigned*)(base + OFF_exA); }
    __host__ __device__ unsigned* exB_() const { return (unsigned*)(base + OFF_exB); }
    __host__ __device__ unsigned* flags_() const { return (unsigned*)(base + OFF_flags); }
    __host__ __device__ u16* xn_() const { return (u16*)(base + OFF_xn); }
    __host__ __device__ u16* y_() const { return (u16*)(base + OFF_y); }
    __host__ __device__ u16* WqkvT_() const { return (u16*)(base + OFF_WqkvT); }
    __host__ __device__ u16* WqkvF_() const { return (u16*)(base + OFF_WqkvF); }
    __host__ __device__ u16* WoF_() const { return (u16*)(base + OFF_WoF); }
    __host__ __device__ u16* W1T_() const { return (u16*)(base + OFF_W1T); }
    __host__ __device__ u16* W2T_() const { return (u16*)(base + OFF_W2T); }
    __host__ __device__ u16* Q_() const { return (u16*)(base + OFF_Q); }
    __host__ __device__ u16* K_() const { return (u16*)(base + OFF_K); }
    __host__ __device__ u16* VT_() const { return (u16*)(base + OFF_VT); }
    __host__ __device__ u16* hmid_() const { return (u16*)(base + OFF_hmid); }
    __host__ __device__ u16* g_() const { return (u16*)(base + OFF_g); }
    __host__ __device__ u16* km_() const { return (u16*)(base + OFF_km); }
    __host__ __device__ u16* vmT_() const { return (u16*)(base + OFF_vmT); }
    __host__ __device__ float* part_() const { return (float*)(base + OFF_part); }
    __host__ __device__ unsigned* flagKV_() const { return flags_(); }
    __host__ __device__ unsigned* flagQ_() const { return flags_() + 1024; }
    __host__ __device__ unsigned* cntH_() const { return flags_() + 1536; }
    __host__ __device__ unsigned* flagG_() const { return flags_() + 1600; }
    __host__ __device__ unsigned* flagM_() const { return flags_() + 2048; }
    __host__ __device__ unsigned* cntO_() const { return flags_() + 4096; }
    __host__ __device__ unsigned long long* dbg_() const { return (unsigned long long*)(base + OFF_dbg); }
};

__device__ __forceinline__ float wave_sum(float v) {
#pragma unroll
    for (int m = 32; m >= 1; m >>= 1) v += __shfl_xor(v, m);
    return v;
}
__device__ __forceinline__ void ln8(float (&v)[8]) {
    float s = 0.f;
#pragma unroll
    for (int i = 0; i < 8; ++i) s += v[i];
    s = wave_sum(s);
    float mean = s * (1.f / 512.f);
    float q = 0.f;
#pragma unroll
    for (int i = 0; i < 8; ++i) { float d = v[i] - mean; q += d * d; }
    q = wave_sum(q);
    float var = rbf(q * (1.f / 512.f));
    float mb = rbf(mean);
    float sd = rbf(sqrtf(rbf(var + 1e-5f)));
#pragma unroll
    for (int i = 0; i < 8; ++i) v[i] = rbf(rbf(v[i] - mb) / sd);
}
__device__ __forceinline__ void transpose_tile(const u16* __restrict__ src, u16* __restrict__ dst, int R, int C, int ldd, int Cpad, int bx, int by, u16 (*t)[66]) {
    int c0 = bx * 64, r0 = by * 64;
    int tx = threadIdx.x & 63, ty = threadIdx.x >> 6;
    for (int i = ty; i < 64; i += 4) {
        int r = r0 + i, c = c0 + tx;
        t[i][tx] = (r < R && c < C) ? src[(size_t)r * C + c] : (u16)0;
    }
    __syncthreads();
    if (dst) {
        for (int i = ty; i < 64; i += 4) {
            int c = c0 + i, r = r0 + tx;
            if (c < Cpad && r < ldd) dst[(size_t)c * ldd + r] = t[tx][i];
        }
    }
}
template <class F>
__device__ __forceinline__ void emit_frags(u16 (*t)[66], F addr) {
    int l = threadIdx.x & 63, q = threadIdx.x >> 6;
#pragma unroll
    for (int i = 0; i < 2; ++i) {
        int f = q * 2 + i, ksl = f >> 2, ct = f & 3;
        u16 v[8];
#pragma unroll
        for (int j = 0; j < 8; ++j) v[j] = t[ksl * 32 + (l >> 4) * 8 + j][ct * 16 + (l & 15)];
        u32x4 o = {(unsigned)v[0] | ((unsigned)v[1] << 16), (unsigned)v[2] | ((unsigned)v[3] << 16), (unsigned)v[4] | ((unsigned)v[5] << 16), (unsigned)v[6] | ((unsigned)v[7] << 16)};
        *(u32x4*)(addr(ksl, ct) + l * 8) = o;
    }
}
#define PREP_LN 512
#define PREP_TQKV 1536
#define PREP_TWO 512
#define PREP_TW1 176
#define PREP_TW2 176
#define ZERO_BYTES ((size_t)64 * 524288 + 68 * 16384 + 65536)
#define PREP_ZERO ((int)(ZERO_BYTES / 65536))
__global__ void __launch_bounds__(256) prep_k(const u16* __restrict__ x, const u16* __restrict__ Wq, const u16* __restrict__ Wk, const u16* __restrict__ Wv,
                                              const u16* __restrict__ Wo, const u16* __restrict__ w1, const u16* __restrict__ w2, WS ws) {
    __shared__ u16 t[64][66];
    int b = blockIdx.x;
    if (b < PREP_ZERO) {
        u32x4 z = {0u, 0u, 0u, 0u};
        u32x4* p = (u32x4*)((char*)ws.exA_() + (size_t)b * 65536) + threadIdx.x;
#pragma unroll
        for (int i = 0; i < 16; ++i) p[i * 256] = z;
        return;
    }
    b -= PREP_ZERO;
    if (b < PREP_LN) {
        int row = b * 4 + (threadIdx.x >> 6);
        int lane = threadIdx.x & 63;
        u32x4 r = *(const u32x4*)(x + (size_t)row * DM + lane * 8);
        float v[8];
#pragma unroll
        for (int i = 0; i < 4; ++i) { v[2 * i] = bf2f(r[i] & 0xffff); v[2 * i + 1] = bf2f(r[i] >> 16); }
        ln8(v);
        u32x4 o;
#pragma unroll
        for (int i = 0; i < 4; ++i) o[i] = pack2(v[2 * i], v[2 * i + 1]);
        *(u32x4*)(ws.xn_() + (size_t)row * DM + lane * 8) = o;
        ln8(v);
#pragma unroll
        for (int i = 0; i < 4; ++i) o[i] = pack2(v[2 * i], v[2 * i + 1]);
        *(u32x4*)(ws.y_() + (size_t)row * DM + lane * 8) = o;
        return;
    }
    b -= PREP_LN;
    if (b < PREP_TQKV) {
        int m = b / 512, r = b % 512;
        const u16* src = m == 0 ? Wq : (m == 1 ? Wk : Wv);
        const int bx = r % 64, by = r / 64;
        transpose_tile(src, ws.WqkvT_() + (size_t)m * HD * DM, DM, HD, DM, HD, bx, by, t);
        emit_frags(t, [&](int ksl, int ct) {
            int n = bx * 64 + ct * 16, ks = by * 2 + ksl;
            int hh = n >> 7, ww = (n >> 5) & 3, tt = (n >> 4) & 1;
            return ws.WqkvF_() + ((size_t)(((hh * 4 + ww) * 6 + m * 2 + tt) * 16 + ks)) * 512;
        });
        return;
    }
    b -= PREP_TQKV;
    if (b < PREP_TWO) {
        const int bx = b % 8, by = b / 8;
        transpose_tile(Wo, nullptr, HD, DM, HD, DM, bx, by, t);
        emit_frags(t, [&](int ksl, int ct) {
            int tile = bx * 4 + ct, k = by * 64 + ksl * 32;
            return ws.WoF_() + ((size_t)((tile * 32 + (k >> 7)) * 4 + ((k >> 5) & 3))) * 512;
        });
        return;
    }
    b -= PREP_TWO;
    if (b < PREP_TW1) { transpose_tile(w1, ws.W1T_(), DM, FF, DM, FFP, b % 22, b / 22, t); return; }
    b -= PREP_TW1;
    transpose_tile(w2, ws.W2T_(), FF, DM, FFP, DM, b % 8, b / 8, t);
}

#define SCALE 0.08838834764831845f
#define MEM_STRIDE 1056
#define QK_STRIDE 288

typedef __amdgpu_buffer_rsrc_t rsrc_t;
__device__ __forceinline__ rsrc_t mk_rsrc(const void* p) { return __builtin_amdgcn_make_buffer_rsrc((void*)p, 0, 0x7fffffff, 0x00020000); }
__device__ __forceinline__ u32x4 bld16(rsrc_t r, unsigned vo, unsigned so) { return __builtin_amdgcn_raw_buffer_load_b128(r, vo, so, 0); }
__device__ __forceinline__ u32x4 bld16_sc1(rsrc_t r, unsigned vo, unsigned so) { return __builtin_amdgcn_raw_buffer_load_b128(r, vo, so, 16); }
__device__ __forceinline__ u32x2 bld8(rsrc_t r, unsigned vo, unsigned so) { return __builtin_amdgcn_raw_buffer_load_b64(r, vo, so, 0); }
__device__ __forceinline__ void bst16_sc1(rsrc_t r, unsigned vo, unsigned so, u32x4 v) { __builtin_amdgcn_raw_buffer_store_b128(v, r, vo, so, 16); }
__device__ __forceinline__ void bst8(rsrc_t r, unsigned vo, unsigned so, u32x2 v) { __builtin_amdgcn_raw_buffer_store_b64(v, r, vo, so, 0); }
__device__ __forceinline__ void bst8_sc1(rsrc_t r, unsigned vo, unsigned so, u32x2 v) { __builtin_amdgcn_raw_buffer_store_b64(v, r, vo, so, 16); }

#define STAMP(i)
#define MFMA0_A(acc, a, b) asm volatile("v_mfma_f32_16x16x32_bf16 %0, %1, %2, 0" : "=&v"(acc) : "a"(a), "v"(b))
#define MFMA_A(acc, a, b) asm volatile("v_mfma_f32_16x16x32_bf16 %0, %1, %2, %0" : "+v"(acc) : "a"(a), "v"(b))
#define MFMA0_V(acc, a, b) asm volatile("v_mfma_f32_16x16x32_bf16 %0, %1, %2, 0" : "=&v"(acc) : "v"(a), "v"(b))
#define MFMA_V(acc, a, b) asm volatile("v_mfma_f32_16x16x32_bf16 %0, %1, %2, %0" : "+v"(acc) : "v"(a), "v"(b))
#define BARRIER() asm volatile("s_waitcnt lgkmcnt(0)\n\ts_barrier" ::: "memory")
#define LAS __attribute__((address_space(3)))

__device__ __forceinline__ float xmax16(float x) {
    unsigned u = __float_as_uint(x);
    auto r = __builtin_amdgcn_permlane16_swap(u, u, false, false);
    return fmaxf(__uint_as_float(r[0]), __uint_as_float(r[1]));
}
__device__ __forceinline__ float xmax32(float x) {
    unsigned u = __float_as_uint(x);
    auto r = __builtin_amdgcn_permlane32_swap(u, u, false, false);
    return fmaxf(__uint_as_float(r[0]), __uint_as_float(r[1]));
}
__device__ __forceinline__ float xsum16(float x) {
    unsigned u = __float_as_uint(x);
    auto r = __builtin_amdgcn_permlane16_swap(u, u, false, false);
    return __uint_as_float(r[0]) + __uint_as_float(r[1]);
}
__device__ __forceinline__ float xsum32(float x) {
    unsigned u = __float_as_uint(x);
    auto r = __builtin_amdgcn_permlane32_swap(u, u, false, false);
    return __uint_as_float(r[0]) + __uint_as_float(r[1]);
}
__device__ __forceinline__ float xsum8(float x) {
    return x + __uint_as_float(__builtin_amdgcn_update_dpp(0u, __float_as_uint(x), 0x128, 0xf, 0xf, false));
}

#define DELAY_A 12
#define DELAY_B 12
#define EXA_STEP 524288u
#define EXB_STEP 16384u
__device__ __forceinline__ void chain_body(const u16* __restrict__ mem0, const WS& ws, char* smem, const int h) {
    char* sW = smem;
    char* sKV = smem + 131072;
    char* sMem = smem + 147456;
    char* sQK = sMem + 8 * MEM_STRIDE;
    char* sO = sQK + 16 * QK_STRIDE;
    const int tid = threadIdx.x, lane = tid & 63;
    const int w = __builtin_amdgcn_readfirstlane(tid >> 6);
    const int fr = lane & 15, fg = lane >> 4, fr8 = lane & 7;
    const unsigned l16 = lane * 16;
    char* lw = sW + w * 32768 + l16;

    bf16x8 wqk[3][16], wv[24], wo[24];
#pragma unroll
    for (int t = 0; t < 6; ++t) {
        const u16* p = ws.WqkvF_() + ((size_t)((h * 4 + w) * 6 + t) * 16) * 512 + lane * 8;
#pragma unroll
        for (int ks = 0; ks < 16; ++ks) {
            bf16x8 v = *(const bf16x8*)(p + ks * 512);
            if (t < 3) wqk[t][ks] = v;
            else if (t == 3) *(bf16x8*)(lw + ks * 1024) = v;
            else if (t == 4) wv[ks] = v;
            else if (ks < 8) wv[16 + ks] = v;
            else *(bf16x8*)(lw + (16 + ks - 8) * 1024) = v;
        }
    }
#pragma unroll
    for (int t = 0; t < 8; ++t)
#pragma unroll
        for (int ks = 0; ks < 4; ++ks) {
            bf16x8 v = *(const bf16x8*)(ws.WoF_() + ((size_t)(((w * 8 + t) * 32 + h) * 4 + ks)) * 512 + lane * 8);
            if (t < 6) wo[t * 4 + ks] = v;
            else *(bf16x8*)(lw + (24 + (t - 6) * 4 + ks) * 1024) = v;
        }
    const rsrc_t rW = mk_rsrc(ws.base);
    const unsigned stA_vo = (unsigned)fg * 128 + (fr & 7) * 16 + (fr >> 3) * 16384;
    const unsigned rdA_vo = (unsigned)(lane >> 3) * 512 + (lane & 7) * 16;
    const char* memrd = sMem + fr8 * MEM_STRIDE + fg * 16;
    const char* qrd = sQK + fr8 * QK_STRIDE + fg * 16;
    const char* ord_ = sO + fr8 * QK_STRIDE + fg * 16;
    char* memwr = sMem + (lane & 7) * MEM_STRIDE + ((w * 2 + (lane >> 5)) * 16 + ((lane >> 3) & 3) * 4) * 2;

    for (int s = 0; s < NSTEPS; ++s) {
        STAMP(0);
        if ((s & 3) == 0) {
            const unsigned* f = ws.flagKV_() + (s >> 2) * 64 + (lane & 1) * 32 + h;
            for (unsigned spin = 0; spin < (1u << 22); ++spin) {
                unsigned v = __hip_atomic_load(f, __ATOMIC_RELAXED, __HIP_MEMORY_SCOPE_AGENT);
                if (__all(v != 0)) break;
                __builtin_amdgcn_s_sleep(2);
            }
        }
        {
            const u16* gk = ws.K_() + (size_t)(s * NH + h) * 4096 + w * 1024 + lane * 8;
            const u16* gv = ws.VT_() + (size_t)(s * NH + h) * 4096 + w * 1024 + lane * 8;
            __builtin_amdgcn_global_load_lds((const unsigned*)gk, (LAS unsigned*)(sKV + w * 2048), 16, 0, 16);
            __builtin_amdgcn_global_load_lds((const unsigned*)(gk + 512), (LAS unsigned*)(sKV + w * 2048 + 1024), 16, 0, 16);
            __builtin_amdgcn_global_load_lds((const unsigned*)gv, (LAS unsigned*)(sKV + 8192 + w * 2048), 16, 0, 16);
            __builtin_amdgcn_global_load_lds((const unsigned*)(gv + 512), (LAS unsigned*)(sKV + 8192 + w * 2048 + 1024), 16, 0, 16);
        }
        if (s == 0) {
            for (int i = tid; i < 8 * 64; i += 256) {
                int row = i >> 6, c = (i & 63) * 8;
                *(u32x4*)(sMem + row * MEM_STRIDE + c * 2) = *(const u32x4*)(mem0 + row * DM + c);
            }
            asm volatile("s_waitcnt vmcnt(0)" ::: "memory");
        } else {
#pragma unroll 1
            for (int d = 0; d < DELAY_B; ++d) __builtin_amdgcn_s_sleep(1);
            u32x4 vv[4];
            for (unsigned spin = 0;; ++spin) {
                if (spin > (1u << 20)) break;
                asm volatile("" ::: "memory");
#pragma unroll
                for (int i = 0; i < 4; ++i) vv[i] = bld16_sc1(rW, l16, ((unsigned)(i * 4096 + w * 1024) + (unsigned)s * EXB_STEP) + (unsigned)OFF_exB);
                bool ok = true;
#pragma unroll
                for (int i = 0; i < 4; ++i)
#pragma unroll
                    for (int q = 0; q < 4; ++q) ok &= ((vv[i][q] & 0xffff) != 0);
                if (__all(ok)) break;
            }
#pragma unroll
            for (int i = 0; i < 4; ++i) {
                u32x2 o = {(vv[i][0] >> 16) | (vv[i][1] & 0xffff0000u), (vv[i][2] >> 16) | (vv[i][3] & 0xffff0000u)};
                *(u32x2*)(memwr + i * 256) = o;
            }
        }
        bf16x8 kw[2], vw[2];
#pragma unroll
        for (int i = 0; i < 2; ++i) kw[i] = *(const bf16x8*)(lw + i * 1024);
        vw[0] = *(const bf16x8*)(lw + 16 * 1024); vw[1] = *(const bf16x8*)(lw + 17 * 1024);
        STAMP(1);
        BARRIER();
        STAMP(2);
        if (s > 0 && tid == 0) __hip_atomic_store(ws.flagM_() + (s - 1) * NH + h, 1u, __ATOMIC_RELAXED, __HIP_MEMORY_SCOPE_AGENT);
        f32x4 acc[4], av[2];
        {
            bf16x8 mb[3];
#pragma unroll
            for (int i = 0; i < 3; ++i) mb[i] = *(const bf16x8*)(memrd + i * 64);
#pragma unroll
            for (int ks = 0; ks < 16; ++ks) {
                bf16x8 m = mb[ks % 3];
                bf16x8 k3 = kw[ks & 1];
                bf16x8 v5 = (ks < 8) ? wv[16 + ks] : vw[ks & 1];
                if (ks == 0) {
                    MFMA0_A(acc[0], wqk[0][ks], m); MFMA0_A(acc[1], wqk[1][ks], m); MFMA0_A(acc[2], wqk[2][ks], m); MFMA0_V(acc[3], k3, m);
                    MFMA0_V(av[0], m, wv[ks]); MFMA0_V(av[1], m, v5);
                } else {
                    MFMA_A(acc[0], wqk[0][ks], m); MFMA_A(acc[1], wqk[1][ks], m); MFMA_A(acc[2], wqk[2][ks], m); MFMA_V(acc[3], k3, m);
                    MFMA_V(av[0], m, wv[ks]); MFMA_V(av[1], m, v5);
                }
                if (ks + 3 < 16) mb[ks % 3] = *(const bf16x8*)(memrd + (ks + 3) * 64);
                if (ks + 2 < 16) kw[ks & 1] = *(const bf16x8*)(lw + (ks + 2) * 1024);
                if (ks >= 8 && ks + 2 < 16) vw[ks & 1] = *(const bf16x8*)(lw + (16 + ks + 2 - 8) * 1024);
            }
        }
        STAMP(3);
        asm volatile("s_nop 7" : "+v"(acc[0]), "+v"(acc[1]), "+v"(acc[2]), "+v"(acc[3]), "+v"(av[0]), "+v"(av[1]));
        {
            u32x2 o[4];
#pragma unroll
            for (int t = 0; t < 4; ++t) o[t] = (u32x2){pack2(acc[t][0], acc[t][1]), pack2(acc[t][2], acc[t][3])};
            if (fr < 8) {
#pragma unroll
                for (int t = 0; t < 4; ++t) {
                    int row = (t >> 1) * 8 + fr;
                    int c = w * 32 + (t & 1) * 16 + fg * 4;
                    *(u32x2*)(sQK + row * QK_STRIDE + c * 2) = o[t];
                }
                bst8_sc1(rW, (unsigned)fr * 256 + fg * 8, ((unsigned)((s * NH + h) * 2048 + (w * 32) * 2)) + (unsigned)OFF_km, o[2]);
                bst8_sc1(rW, (unsigned)fr * 256 + fg * 8, ((unsigned)((s * NH + h) * 2048 + (w * 32 + 16) * 2)) + (unsigned)OFF_km, o[3]);
            }
        }
        u32x4 am[2], ax[2];
#pragma unroll
        for (int t = 0; t < 2; ++t) {
            am[t] = (u32x4){pack2(av[t][0], av[t][1]), pack2(av[t][2], av[t][3]), 0u, 0u};
            if (fg >= 2) { am[t][0] = 0u; am[t][1] = 0u; }
        }
        if (fg < 2) {
            bst8_sc1(rW, (unsigned)fr * 16 + fg * 8, ((unsigned)((s * NH + h) * 2048 + (w * 32) * 16)) + (unsigned)OFF_vmT, (u32x2){am[0][0], am[0][1]});
            bst8_sc1(rW, (unsigned)fr * 16 + fg * 8, ((unsigned)((s * NH + h) * 2048 + (w * 32 + 16) * 16)) + (unsigned)OFF_vmT, (u32x2){am[1][0], am[1][1]});
        }
        bf16x8 kx[2][4];
#pragma unroll
        for (int ks = 0; ks < 4; ++ks) {
            kx[0][ks] = *(const bf16x8*)(sKV + ks * 1024 + l16);
            kx[1][ks] = *(const bf16x8*)(sKV + 4096 + ks * 1024 + l16);
        }
        ax[0] = *(const u32x4*)(sKV + 8192 + (w * 2) * 1024 + l16);
        ax[1] = *(const u32x4*)(sKV + 8192 + (w * 2 + 1) * 1024 + l16);
        STAMP(4);
        BARRIER();
        STAMP(5);
        f32x4 sm, sx0, sx1;
        {
            bf16x8 qf[4], kf[4];
#pragma unroll
            for (int ks = 0; ks < 4; ++ks) {
                qf[ks] = *(const bf16x8*)(qrd + ks * 64);
                kf[ks] = *(const bf16x8*)(qrd + 8 * QK_STRIDE + ks * 64);
            }
            MFMA0_V(sx0, kx[0][0], qf[0]); MFMA0_V(sx1, kx[1][0], qf[0]); MFMA0_V(sm, kf[0], qf[0]);
#pragma unroll
            for (int ks = 1; ks < 4; ++ks) { MFMA_V(sx0, kx[0][ks], qf[ks]); MFMA_V(sx1, kx[1][ks], qf[ks]); MFMA_V(sm, kf[ks], qf[ks]); }
        }
        STAMP(10);
        bf16x8 wf[8];
#pragma unroll
        for (int i = 0; i < 8; ++i) wf[i] = *(const bf16x8*)(lw + (24 + i) * 1024);
        asm volatile("s_nop 7" : "+v"(sm), "+v"(sx0), "+v"(sx1));
        const f32x2 sc2 = {SCALE, SCALE};
        f32x2 p0 = rbf2(rbf2((f32x2){sx0[0], sx0[1]}) * sc2);
        f32x2 p1 = rbf2(rbf2((f32x2){sx0[2], sx0[3]}) * sc2);
        f32x2 p2 = rbf2(rbf2((f32x2){sx1[0], sx1[1]}) * sc2);
        f32x2 p3 = rbf2(rbf2((f32x2){sx1[2], sx1[3]}) * sc2);
        f32x2 p4 = rbf2(rbf2((f32x2){sm[0], sm[1]}) * sc2);
        f32x2 p5 = rbf2(rbf2((f32x2){sm[2], sm[3]}) * sc2);
        if (fg >= 2) { p4 = (f32x2){-3.0e38f, -3.0e38f}; p5 = p4; }
        float mx = fmaxf(fmaxf(fmaxf(p0[0], p0[1]), fmaxf(p1[0], p1[1])), fmaxf(fmaxf(p2[0], p2[1]), fmaxf(p3[0], p3[1])));
        mx = fmaxf(mx, fmaxf(fmaxf(p4[0], p4[1]), fmaxf(p5[0], p5[1])));
        mx = xmax16(mx);
        mx = xmax32(mx);
        const float L2E = 1.4426950408889634f;
        const f32x2 l2 = {L2E, L2E};
        const f32x2 mxl = {mx, mx};
        p0 = (p0 - mxl) * l2; p1 = (p1 - mxl) * l2; p2 = (p2 - mxl) * l2; p3 = (p3 - mxl) * l2; p4 = (p4 - mxl) * l2; p5 = (p5 - mxl) * l2;
        p0 = (f32x2){__builtin_amdgcn_exp2f(p0[0]), __builtin_amdgcn_exp2f(p0[1])};
        p1 = (f32x2){__builtin_amdgcn_exp2f(p1[0]), __builtin_amdgcn_exp2f(p1[1])};
        p2 = (f32x2){__builtin_amdgcn_exp2f(p2[0]), __builtin_amdgcn_exp2f(p2[1])};
        p3 = (f32x2){__builtin_amdgcn_exp2f(p3[0]), __builtin_amdgcn_exp2f(p3[1])};
        p4 = (f32x2){__builtin_amdgcn_exp2f(p4[0]), __builtin_amdgcn_exp2f(p4[1])};
        p5 = (f32x2){__builtin_amdgcn_exp2f(p5[0]), __builtin_amdgcn_exp2f(p5[1])};
        f32x2 s2 = ((p0 + p1) + (p2 + p3)) + (p4 + p5);
        float sum = s2[0] + s2[1];
        sum = xsum16(sum);
        sum = xsum32(sum);
        float inv = __builtin_amdgcn_rcpf(sum);
        const f32x2 iv = {inv, inv};
        p0 *= iv; p1 *= iv; p2 *= iv; p3 *= iv; p4 *= iv; p5 *= iv;
        u32x4 px = {pack2(p0[0], p0[1]), pack2(p1[0], p1[1]), pack2(p2[0], p2[1]), pack2(p3[0], p3[1])};
        u32x4 pm = {pack2(p4[0], p4[1]), pack2(p5[0], p5[1]), 0u, 0u};
        STAMP(12);
        f32x4 o0, o1;
        asm volatile("s_nop 1" : "+v"(px), "+v"(pm), "+v"(am[0]), "+v"(am[1]), "+v"(ax[0]), "+v"(ax[1]));
        MFMA0_V(o0, ax[0], px); MFMA0_V(o1, ax[1], px);
        MFMA_V(o0, am[0], pm); MFMA_V(o1, am[1], pm);
        asm volatile("s_nop 7" : "+v"(o0), "+v"(o1));
        if (fr < 8) {
            *(u32x2*)(sO + fr * QK_STRIDE + (w * 32 + fg * 4) * 2) = (u32x2){pack2(o0[0], o0[1]), pack2(o0[2], o0[3])};
            *(u32x2*)(sO + fr * QK_STRIDE + (w * 32 + 16 + fg * 4) * 2) = (u32x2){pack2(o1[0], o1[1]), pack2(o1[2], o1[3])};
        }
        STAMP(6);
        BARRIER();
        STAMP(7);
        bf16x8 of[4];
#pragma unroll
        for (int ks = 0; ks < 4; ++ks) of[ks] = *(const bf16x8*)(ord_ + ks * 64);
        {
            f32x4 a[4];
#pragma unroll
            for (int p = 0; p <= 4; ++p) {
                if (p < 4) {
#pragma unroll
                    for (int u = 0; u < 2; ++u) {
                        const int t = 2 * p + u;
                        f32x4& ac = a[(p & 1) * 2 + u];
#pragma unroll
                        for (int ks = 0; ks < 4; ++ks) {
                            if (t < 4) { if (ks == 0) MFMA0_A(ac, wo[t * 4 + ks], of[ks]); else MFMA_A(ac, wo[t * 4 + ks], of[ks]); }
                            else {
                                bf16x8 f = (t < 6) ? wo[t * 4 + ks] : wf[(t - 6) * 4 + ks];
                                if (ks == 0) MFMA0_V(ac, f, of[ks]); else MFMA_V(ac, f, of[ks]);
                            }
                        }
                    }
                }
                if (p >= 1) {
                    const int q0 = ((p - 1) & 1) * 2;
                    if (p == 4) asm volatile("s_nop 7" : "+v"(a[q0]), "+v"(a[q0 + 1]));
                    u32x4 o;
#pragma unroll
                    for (int q = 0; q < 4; ++q) o[q] = __float_as_uint(fr < 8 ? a[q0][q] : a[q0 + 1][q]) | 1u;
                    bst16_sc1(rW, stA_vo, ((unsigned)(((w * 8 + 2 * (p - 1)) * NH + h) * 512) + (unsigned)s * EXA_STEP) + (unsigned)OFF_exA, o);
                }
            }
        }
        STAMP(8);
        {
#pragma unroll 1
            for (int d = 0; d < DELAY_A; ++d) __builtin_amdgcn_s_sleep(1);
            float tot[4];
            for (unsigned spin = 0;; ++spin) {
                if (spin > (1u << 20)) break;
                asm volatile("" ::: "memory");
                u32x4 v[4];
#pragma unroll
                for (int i = 0; i < 4; ++i) v[i] = bld16_sc1(rW, rdA_vo, ((unsigned)(h * NH * 512 + i * 4096 + w * 128) + (unsigned)s * EXA_STEP) + (unsigned)OFF_exA);
                bool ok = true;
#pragma unroll
                for (int q = 0; q < 4; ++q) {
                    ok &= ((v[0][q] & v[1][q] & v[2][q] & v[3][q] & 1u) != 0);
                    tot[q] = (__uint_as_float(v[0][q]) + __uint_as_float(v[1][q])) + (__uint_as_float(v[2][q]) + __uint_as_float(v[3][q]));
                }
                if (__all(ok)) break;
            }
#pragma unroll
            for (int q = 0; q < 4; ++q) {
                tot[q] = xsum8(tot[q]);
                tot[q] = xsum16(tot[q]);
                tot[q] = xsum32(tot[q]);
            }
            STAMP(9);
            if (lane < 8) {
                u32x4 o;
#pragma unroll
                for (int q = 0; q < 4; ++q) o[q] = ((unsigned)f2bf(tot[q]) << 16) | 1u;
                bst16_sc1(rW, l16, ((unsigned)(h * 512 + w * 128) + (unsigned)(s + 1) * EXB_STEP) + (unsigned)OFF_exB, o);
            }
        }
    }
    asm volatile("s_waitcnt vmcnt(0)" ::: "memory");
    __syncthreads();
    if (tid == 0) __hip_atomic_store(ws.flagM_() + (NSTEPS - 1) * NH + h, 1u, __ATOMIC_RELAXED, __HIP_MEMORY_SCOPE_AGENT);
}

enum { EPI_QKV = 0, EPI_H = 1, EPI_G = 2 };
__device__ __forceinline__ float gelu_tanh(float v) {
    float u = 0.7978845608028654f * (v + 0.044715f * v * v * v);
    return 0.5f * v * (1.f + tanhf(u));
}
__device__ __forceinline__ void wait_flag_ge(const unsigned* f, unsigned want) {
    for (unsigned spin = 0; spin < (1u << 22); ++spin) {
        unsigned v = __hip_atomic_load(f, __ATOMIC_RELAXED, __HIP_MEMORY_SCOPE_AGENT);
        if (__all(v >= want)) break;
        __builtin_amdgcn_s_sleep(32);
    }
}
__device__ __forceinline__ void acquire_all() {
    __builtin_amdgcn_fence(__ATOMIC_ACQUIRE, "agent");
    asm volatile("s_waitcnt vmcnt(0)" ::: "memory");
    __syncthreads();
}
__device__ __forceinline__ void publish_done() {
    asm volatile("s_waitcnt vmcnt(0)" ::: "memory");
    __syncthreads();
}

template <int EPI>
__device__ __forceinline__ void gemm_tile(const u16* __restrict__ A, const u16* __restrict__ Bt, int lda, int ldb, int K, int brow, int bcol,
                                          const WS& ws, const u16* __restrict__ bias, char* lds_, const int tidx) {
    char (*lds)[2][128 * 128] = (char (*)[2][128 * 128])lds_;
    const int tid = tidx, lane = tid & 63, w = tid >> 6;
    const int wr = w >> 1, wc = w & 1;
    const int fr = lane & 15, fg = lane >> 4;
    f32x4 acc[4][4];
#pragma unroll
    for (int i = 0; i < 4; ++i)
#pragma unroll
        for (int j = 0; j < 4; ++j) acc[i][j] = (f32x4){0.f, 0.f, 0.f, 0.f};
    const int srow = tid >> 3, sslot = tid & 7;
    const u16* ga = A + (size_t)(brow + srow) * lda + sslot * 8;
    const u16* gb = Bt + (size_t)(bcol + srow) * ldb + sslot * 8;
    u32x4 ra[4], rb[4];
    auto gload = [&](int kt) {
#pragma unroll
        for (int i = 0; i < 4; ++i) {
            ra[i] = *(const u32x4*)(ga + (size_t)(32 * i) * lda + kt * 64);
            rb[i] = *(const u32x4*)(gb + (size_t)(32 * i) * ldb + kt * 64);
        }
    };
    auto swrite = [&](int buf) {
#pragma unroll
        for (int i = 0; i < 4; ++i) {
            int r = srow + 32 * i;
            int off = r * 128 + ((sslot ^ (r & 7)) << 4);
            *(u32x4*)(&lds[buf][0][off]) = ra[i];
            *(u32x4*)(&lds[buf][1][off]) = rb[i];
        }
    };
    const int nt = K / 64;
    gload(0);
    swrite(0);
    __syncthreads();
    const bool vtile = (EPI == EPI_QKV) && (bcol >= 2 * HD);
    for (int kt = 0; kt < nt; ++kt) {
        int buf = kt & 1;
        if (kt + 1 < nt) gload(kt + 1);
#pragma unroll
        for (int kk = 0; kk < 2; ++kk) {
            bf16x8 af[4], bfr[4];
#pragma unroll
            for (int i = 0; i < 4; ++i) {
                int r = wr * 64 + i * 16 + fr;
                af[i] = *(const bf16x8*)(&lds[buf][0][r * 128 + (((kk * 4 + fg) ^ (r & 7)) << 4)]);
                int c = wc * 64 + i * 16 + fr;
                bfr[i] = *(const bf16x8*)(&lds[buf][1][c * 128 + (((kk * 4 + fg) ^ (c & 7)) << 4)]);
            }
            if (vtile) {
#pragma unroll
                for (int i = 0; i < 4; ++i)
#pragma unroll
                    for (int j = 0; j < 4; ++j) acc[i][j] = __builtin_amdgcn_mfma_f32_16x16x32_bf16(af[i], bfr[j], acc[i][j], 0, 0, 0);
            } else {
#pragma unroll
                for (int i = 0; i < 4; ++i)
#pragma unroll
                    for (int j = 0; j < 4; ++j) acc[i][j] = __builtin_amdgcn_mfma_f32_16x16x32_bf16(bfr[j], af[i], acc[i][j], 0, 0, 0);
            }
        }
        if (kt + 1 < nt) swrite(buf ^ 1);
        __syncthreads();
    }
    const rsrc_t rW = mk_rsrc(ws.base);
#pragma unroll
    for (int i = 0; i < 4; ++i)
#pragma unroll
        for (int j = 0; j < 4; ++j) {
            f32x4 a = acc[i][j];
            if (vtile) {
                int col = bcol + wc * 64 + j * 16 + fr - 2 * HD;
                int tok = brow + wr * 64 + i * 16 + fg * 4;
                int seg = tok >> 5, tt = (tok >> 4) & 1, hh = col >> 7, et = (col >> 4) & 7;
                u32x2 o = {pack2(a[0], a[1]), pack2(a[2], a[3])};
                bst8_sc1(rW, (unsigned)((((seg * NH + hh) * 8 + et) * 512 + (fg * 16 + fr) * 8 + tt * 4) * 2), (0) + (unsigned)OFF_VT, o);
            } else {
                int tok = brow + wr * 64 + i * 16 + fr;
                int col = bcol + wc * 64 + j * 16 + fg * 4;
                if (EPI == EPI_QKV) {
                    int cc = col & (HD - 1);
                    int seg = tok >> 5, tt = (tok >> 4) & 1, hh = cc >> 7, e = cc & 127;
                    u32x2 o = {pack2(a[0], a[1]), pack2(a[2], a[3])};
                    unsigned off = (unsigned)((((seg * NH + hh) * 8 + tt * 4 + (e >> 5)) * 512 + (((e >> 3) & 3) * 16 + (tok & 15)) * 8 + (e & 4)) * 2);
                    if (col >= HD) bst8_sc1(rW, off, (0) + (unsigned)OFF_K, o); else bst8_sc1(rW, off, (0) + (unsigned)OFF_Q, o);
                } else if (EPI == EPI_H) {
                    float r[4];
#pragma unroll
                    for (int q = 0; q < 4; ++q) {
                        float b = (col + q < FF) ? bf2f(bias[col + q]) : 0.f;
                        float v = rbf(rbf(a[q]) + b);
                        r[q] = (col + q < FF) ? gelu_tanh(v) : 0.f;
                    }
                    u32x2 o = {pack2(r[0], r[1]), pack2(r[2], r[3])};
                    bst8_sc1(rW, (unsigned)((tok * FFP + col) * 2), (0) + (unsigned)OFF_hmid, o);
                } else {
                    float r[4];
#pragma unroll
                    for (int q = 0; q < 4; ++q) {
                        float v = rbf(rbf(a[q]) + bf2f(bias[col + q]));
                        r[q] = v / (1.f + __expf(-v));
                    }
                    u32x2 o = {pack2(r[0], r[1]), pack2(r[2], r[3])};
                    bst8_sc1(rW, (unsigned)((tok * DM + col) * 2), (0) + (unsigned)OFF_g, o);
                }
            }
        }
}

__device__ __forceinline__ void attn_task(int s, int hg, const WS& ws, char* lds, const int tidx) {
    const int lane = tidx & 63, w = __builtin_amdgcn_readfirstlane(tidx >> 6);
    const int h = hg * 4 + w;
    const int fr = lane & 15, fg = lane >> 4, fr8 = lane & 7;
    char* sO = lds;
    const u16* KF = ws.K_() + (size_t)(s * NH + h) * 4096;
    const u16* QF = ws.Q_() + (size_t)(s * NH + h) * 4096;
    const u16* VF = ws.VT_() + (size_t)(s * NH + h) * 4096;
    bf16x8 kx[2][4], kmf[4];
#pragma unroll
    for (int ks = 0; ks < 4; ++ks) {
        kmf[ks] = *(const bf16x8*)(ws.km_() + ((size_t)(s * NH + h) * 8 + fr8) * DH + ks * 32 + fg * 8);
#pragma unroll
        for (int t = 0; t < 2; ++t) kx[t][ks] = *(const bf16x8*)(KF + (t * 4 + ks) * 512 + lane * 8);
    }
    u32x4 px[2], pm[2];
#pragma unroll
    for (int qt = 0; qt < 2; ++qt) {
        f32x4 sm = {0.f, 0.f, 0.f, 0.f}, sx0 = sm, sx1 = sm;
#pragma unroll
        for (int ks = 0; ks < 4; ++ks) {
            bf16x8 qf = *(const bf16x8*)(QF + (qt * 4 + ks) * 512 + lane * 8);
            sm = __builtin_amdgcn_mfma_f32_16x16x32_bf16(kmf[ks], qf, sm, 0, 0, 0);
            sx0 = __builtin_amdgcn_mfma_f32_16x16x32_bf16(kx[0][ks], qf, sx0, 0, 0, 0);
            sx1 = __builtin_amdgcn_mfma_f32_16x16x32_bf16(kx[1][ks], qf, sx1, 0, 0, 0);
        }
        const int qi = qt * 16 + fr;
        float sv[12];
#pragma unroll
        for (int q = 0; q < 4; ++q) {
            sv[q] = (fg < 2) ? rbf(rbf(sm[q]) * SCALE) : -3.0e38f;
            int k0 = fg * 4 + q, k1 = 16 + fg * 4 + q;
            sv[4 + q] = (k0 <= qi) ? rbf(rbf(sx0[q]) * SCALE) : -3.0e38f;
            sv[8 + q] = (k1 <= qi) ? rbf(rbf(sx1[q]) * SCALE) : -3.0e38f;
        }
        float mx = sv[0];
#pragma unroll
        for (int q = 1; q < 12; ++q) mx = fmaxf(mx, sv[q]);
        mx = xmax16(mx); mx = xmax32(mx);
        float sum = 0.f;
#pragma unroll
        for (int q = 0; q < 12; ++q) { sv[q] = __expf(sv[q] - mx); sum += sv[q]; }
        sum = xsum16(sum); sum = xsum32(sum);
        float inv = 1.f / sum;
        px[qt] = (u32x4){pack2(sv[4] * inv, sv[5] * inv), pack2(sv[6] * inv, sv[7] * inv), pack2(sv[8] * inv, sv[9] * inv), pack2(sv[10] * inv, sv[11] * inv)};
        pm[qt] = (u32x4){pack2(sv[0] * inv, sv[1] * inv), pack2(sv[2] * inv, sv[3] * inv), 0u, 0u};
    }
#pragma unroll
    for (int et = 0; et < 8; ++et) {
        u32x4 ax = *(const u32x4*)(VF + et * 512 + lane * 8);
        u32x2 m0 = {0u, 0u};
        if (fg < 2) m0 = *(const u32x2*)(ws.vmT_() + ((size_t)(s * NH + h) * DH + et * 16 + fr) * 8 + fg * 4);
        u32x4 am = {m0[0], m0[1], 0u, 0u};
#pragma unroll
        for (int qt = 0; qt < 2; ++qt) {
            f32x4 o = {0.f, 0.f, 0.f, 0.f};
            o = __builtin_amdgcn_mfma_f32_16x16x32_bf16(__builtin_bit_cast(bf16x8, ax), __builtin_bit_cast(bf16x8, px[qt]), o, 0, 0, 0);
            o = __builtin_amdgcn_mfma_f32_16x16x32_bf16(__builtin_bit_cast(bf16x8, am), __builtin_bit_cast(bf16x8, pm[qt]), o, 0, 0, 0);
            u32x2 ov = {pack2(o[0], o[1]), pack2(o[2], o[3])};
            *(u32x2*)(sO + (qt * 16 + fr) * 1040 + (w * DH + et * 16 + fg * 4) * 2) = ov;
        }
    }
    __syncthreads();
    f32x4 acc[8][2];
#pragma unroll
    for (int t = 0; t < 8; ++t) { acc[t][0] = (f32x4){0.f, 0.f, 0.f, 0.f}; acc[t][1] = acc[t][0]; }
    const u16* wp = ws.WoF_() + ((size_t)(w * 8 * 32 + hg * 4) * 4) * 512 + lane * 8;
    {
        char* ring = lds + 33280 + w * 24576;
        const rsrc_t rW2 = mk_rsrc(ws.base);
        const unsigned wbase = (unsigned)OFF_WoF + (unsigned)((w * 8 * 32 + hg * 4) * 4) * 1024u;
        auto issue = [&](int ks) {
#pragma unroll
            for (int t = 0; t < 8; ++t)
                __builtin_amdgcn_raw_ptr_buffer_load_lds(rW2, (LAS void*)(ring + ((ks % 3) * 8 + t) * 1024), 16, lane * 16, wbase + (unsigned)(t * 128 + ks) * 1024u, 0, 0);
        };
        issue(0); issue(1); issue(2);
#pragma unroll
        for (int ks = 0; ks < 16; ++ks) {
            if (ks <= 13) asm volatile("s_waitcnt vmcnt(16)" ::: "memory");
            else if (ks == 14) asm volatile("s_waitcnt vmcnt(8)" ::: "memory");
            else asm volatile("s_waitcnt vmcnt(0)" ::: "memory");
            bf16x8 b0 = *(const bf16x8*)(sO + fr * 1040 + (ks * 32 + fg * 8) * 2);
            bf16x8 b1 = *(const bf16x8*)(sO + (16 + fr) * 1040 + (ks * 32 + fg * 8) * 2);
            bf16x8 a[8];
#pragma unroll
            for (int t = 0; t < 8; ++t) a[t] = *(const bf16x8*)(ring + ((ks % 3) * 8 + t) * 1024 + lane * 16);
            if (ks + 3 < 16) {
                asm volatile("s_waitcnt lgkmcnt(0)" ::: "memory");
                issue(ks + 3);
            }
#pragma unroll
            for (int t = 0; t < 8; ++t) {
                acc[t][0] = __builtin_amdgcn_mfma_f32_16x16x32_bf16(a[t], b0, acc[t][0], 0, 0, 0);
                acc[t][1] = __builtin_amdgcn_mfma_f32_16x16x32_bf16(a[t], b1, acc[t][1], 0, 0, 0);
            }
        }
    }
    const rsrc_t rW = mk_rsrc(ws.base);
#pragma unroll
    for (int t = 0; t < 8; ++t)
#pragma unroll
        for (int qt = 0; qt < 2; ++qt) {
            u32x4 o = {__float_as_uint(acc[t][qt][0]), __float_as_uint(acc[t][qt][1]), __float_as_uint(acc[t][qt][2]), __float_as_uint(acc[t][qt][3])};
            unsigned off = (unsigned)((((s * 8 + hg) * 32 + qt * 16 + fr) * 512 + w * 128 + t * 16 + fg * 4) * 4);
            bst16_sc1(rW, off, (0) + (unsigned)OFF_part, o);
        }
}

__device__ __forceinline__ void reduce_task(int s, int r0, const u16* __restrict__ x, u16* __restrict__ out, const WS& ws, const int tidx) {
    const int i = tidx;
    {
        int row = r0 + (i >> 7), c = (i & 127) * 4;
        f32x4 t = {0.f, 0.f, 0.f, 0.f};
        f32x4 pv[8];
#pragma unroll
        for (int hg = 0; hg < 8; ++hg) pv[hg] = *(const f32x4*)(ws.part_() + ((size_t)(s * 8 + hg) * 32 + row) * 512 + c);
#pragma unroll
        for (int hg = 0; hg < 8; ++hg) t += pv[hg];
        int tok = s * SEG + row;
        u32x2 xv = *(const u32x2*)(x + (size_t)tok * DM + c);
        u32x2 gv = *(const u32x2*)(ws.g_() + (size_t)tok * DM + c);
        float xs[4] = {bf2f(xv[0] & 0xffff), bf2f(xv[0] >> 16), bf2f(xv[1] & 0xffff), bf2f(xv[1] >> 16)};
        float gs[4] = {bf2f(gv[0] & 0xffff), bf2f(gv[0] >> 16), bf2f(gv[1] & 0xffff), bf2f(gv[1] >> 16)};
        float r[4];
#pragma unroll
        for (int q = 0; q < 4; ++q) r[q] = xs[q] + rbf(rbf(t[q]) * gs[q]);
        u32x2 o = {pack2(r[0], r[1]), pack2(r[2], r[3])};
        *(u32x2*)(out + (size_t)tok * DM + c) = o;
    }
}

#define NWORK 224
#define T_KV 1024
#define T_H 176
#define T_Q 512
#define T_G 64
#define T_A 512
#define T_TOTAL (T_KV + T_H + T_Q + T_G + T_A)
#define LDS_BYTES 162816

__global__ void __launch_bounds__(256, 1) mega_k(const u16* __restrict__ x, const u16* __restrict__ mem0, const u16* __restrict__ b1, const u16* __restrict__ b2,
                                                 u16* __restrict__ out, WS ws) {
    extern __shared__ __attribute__((aligned(16))) char smem[];
    if (blockIdx.x < NH) {
        chain_body(mem0, ws, smem, blockIdx.x);
        return;
    }
    const int tid = threadIdx.x, lane = tid & 63, w = tid >> 6;
    for (int task = blockIdx.x - NH; task < T_TOTAL; task += NWORK) {
        int t = task;
        int tidx = threadIdx.x;
        asm volatile("" : "+v"(tidx));
        if (t < T_KV) {
            int tb = t >> 6, cb = t & 63;
            gemm_tile<EPI_QKV>(ws.xn_(), ws.WqkvT_(), DM, DM, DM, tb * 128, HD + cb * 128, ws, nullptr, smem, tidx);
            publish_done();
            if (tid == 0) __hip_atomic_store(ws.flagKV_() + tb * 64 + cb, 1u, __ATOMIC_RELAXED, __HIP_MEMORY_SCOPE_AGENT);
            continue;
        }
        t -= T_KV;
        if (t < T_H) {
            int tb = t / 11, cb = t % 11;
            gemm_tile<EPI_H>(ws.y_(), ws.W1T_(), DM, DM, DM, tb * 128, cb * 128, ws, b1, smem, tidx);
            publish_done();
            if (tid == 0) __hip_atomic_fetch_add(ws.cntH_() + tb, 1u, __ATOMIC_RELAXED, __HIP_MEMORY_SCOPE_AGENT);
            continue;
        }
        t -= T_H;
        if (t < T_Q) {
            int tb = t >> 5, cb = t & 31;
            gemm_tile<EPI_QKV>(ws.xn_(), ws.WqkvT_(), DM, DM, DM, tb * 128, cb * 128, ws, nullptr, smem, tidx);
            publish_done();
            if (tid == 0) __hip_atomic_store(ws.flagQ_() + tb * 32 + cb, 1u, __ATOMIC_RELAXED, __HIP_MEMORY_SCOPE_AGENT);
            continue;
        }
        t -= T_Q;
        if (t < T_G) {
            int tb = t >> 2, cb = t & 3;
            if (w == 0) wait_flag_ge(ws.cntH_() + tb, 11u);
            acquire_all();
            gemm_tile<EPI_G>(ws.hmid_(), ws.W2T_(), FFP, FFP, FFP, tb * 128, cb * 128, ws, b2, smem, tidx);
            publish_done();
            if (tid == 0) __hip_atomic_store(ws.flagG_() + tb * 4 + cb, 1u, __ATOMIC_RELAXED, __HIP_MEMORY_SCOPE_AGENT);
            continue;
        }
        t -= T_G;
        {
            int s = t >> 3, hg = t & 7, tb = s >> 2;
            if (w == 0) {
                const unsigned* f;
                int l = lane & 15, hh = hg * 4 + (l & 3);
                if (l < 4) f = ws.flagKV_() + tb * 64 + hh;
                else if (l < 8) f = ws.flagKV_() + tb * 64 + 32 + hh;
                else if (l < 12) f = ws.flagQ_() + tb * 32 + hh;
                else f = ws.flagM_() + s * NH + hh;
                wait_flag_ge(f, 1u);
            }
            acquire_all();
            attn_task(s, hg, ws, smem, tidx);
            publish_done();
            if (tid == 0) __hip_atomic_fetch_add(ws.cntO_() + s, 1u, __ATOMIC_RELAXED, __HIP_MEMORY_SCOPE_AGENT);
            if (w == 0) { wait_flag_ge(lane < 4 ? ws.flagG_() + tb * 4 + lane : ws.cntO_() + s, lane < 4 ? 1u : 8u); }
            acquire_all();
            reduce_task(s, hg * 4, x, out, ws, tidx);
            reduce_task(s, hg * 4 + 2, x, out, ws, tidx);
            __syncthreads();
        }
    }
}

extern "C" void kernel_launch(void* const* d_in, const int* in_sizes, int n_in, void* d_out, int out_size, void* d_ws, size_t ws_size,
                              hipStream_t stream) {
    const u16* x = (const u16*)d_in[0];
    const u16* mem0 = (const u16*)d_in[1];
    const u16* Wq = (const u16*)d_in[2];
    const u16* Wk = (const u16*)d_in[3];
    const u16* Wv = (const u16*)d_in[4];
    const u16* Wo = (const u16*)d_in[5];
    const u16* w1 = (const u16*)d_in[8];
    const u16* b1 = (const u16*)d_in[9];
    const u16* w2 = (const u16*)d_in[10];
    const u16* b2 = (const u16*)d_in[11];
    u16* out = (u16*)d_out;

    WS ws; ws.base = (char*)d_ws;

    const int nprep = PREP_ZERO + PREP_LN + PREP_TQKV + PREP_TWO + PREP_TW1 + PREP_TW2;
    prep_k<<<nprep, 256, 0, stream>>>(x, Wq, Wk, Wv, Wo, w1, w2, ws);
    (void)hipFuncSetAttribute((const void*)mega_k, hipFuncAttributeMaxDynamicSharedMemorySize, LDS_BYTES);
    mega_k<<<256, 256, LDS_BYTES, stream>>>(x, mem0, b1, b2, out, ws);
}
```

```cpp
#include <hip/hip_runtime.h>
#include <hip/hip_bf16.h>
#include <stdint.h>

typedef __attribute__((ext_vector_type(8))) short bf16x8;
typedef __attribute__((ext_vector_type(4))) float f32x4;
typedef __attribute__((ext_vector_type(4))) unsigned u32x4;
typedef __attribute__((ext_vector_type(2))) unsigned u32x2;
typedef unsigned short u16;

#define S_TOK 2048
#define DM 512
#define NH 32
#define DH 128
#define HD 4096
#define NMEM 8
#define SEG 32
#define NSEG 64
#define NSTEPS 64
#define FF 1376
#define FFP 1408

__device__ __forceinline__ u16 f2bf(float f) { return __builtin_bit_cast(u16, (__bf16)f); }
__device__ __forceinline__ float bf2f(u16 h) { return __builtin_bit_cast(float, (unsigned)h << 16); }
__device__ __forceinline__ float rbf(float f) { return bf2f(f2bf(f)); }
typedef __attribute__((ext_vector_type(2))) float f32x2;
typedef __attribute__((ext_vector_type(2))) __bf16 bf16x2v;
__device__ __forceinline__ unsigned pack2(float a, float b) { f32x2 v = {a, b}; return __builtin_bit_cast(unsigned, __builtin_convertvector(v, bf16x2v)); }
__device__ __forceinline__ f32x2 rbf2(f32x2 v) { unsigned u = __builtin_bit_cast(unsigned, __builtin_convertvector(v, bf16x2v)); f32x2 r = {__uint_as_float(u << 16), __uint_as_float(u & 0xffff0000u)}; return r; }

constexpr size_t OFF_exA = 0;
constexpr size_t OFF_exB = OFF_exA + (((size_t)64 * 524288) + 255) / 256 * 256;
constexpr size_t OFF_flags = OFF_exB + (((size_t)68 * 16384) + 255) / 256 * 256;
constexpr size_t OFF_xn = OFF_flags + (((size_t)65536) + 255) / 256 * 256;
constexpr size_t OFF_y = OFF_xn + (((size_t)S_TOK * DM * 2) + 255) / 256 * 256;
constexpr size_t OFF_WqkvT = OFF_y + (((size_t)S_TOK * DM * 2) + 255) / 256 * 256;
constexpr size_t OFF_WqkvF = OFF_WqkvT + (((size_t)3 * HD * DM * 2) + 255) / 256 * 256;
constexpr size_t OFF_WoF = OFF_WqkvF + (((size_t)3 * HD * DM * 2) + 255) / 256 * 256;
constexpr size_t OFF_W1T = OFF_WoF + (((size_t)DM * HD * 2) + 255) / 256 * 256;
constexpr size_t OFF_W2T = OFF_W1T + (((size_t)FFP * DM * 2) + 255) / 256 * 256;
constexpr size_t OFF_Q = OFF_W2T + (((size_t)DM * FFP * 2) + 255) / 256 * 256;
constexpr size_t OFF_K = OFF_Q + (((size_t)S_TOK * HD * 2) + 255) / 256 * 256;
constexpr size_t OFF_VT = OFF_K + (((size_t)S_TOK * HD * 2) + 255) / 256 * 256;
constexpr size_t OFF_hmid = OFF_VT + (((size_t)S_TOK * HD * 2) + 255) / 256 * 256;
constexpr size_t OFF_g = OFF_hmid + (((size_t)S_TOK * FFP * 2) + 255) / 256 * 256;
constexpr size_t OFF_km = OFF_g + (((size_t)S_TOK * DM * 2) + 255) / 256 * 256;
constexpr size_t OFF_vmT = OFF_km + (((size_t)NSEG * NH * 8 * DH * 2) + 255) / 256 * 256;
constexpr size_t OFF_part = OFF_vmT + (((size_t)NSEG * NH * 8 * DH * 2) + 255) / 256 * 256;
constexpr size_t OFF_dbg = (size_t)240 * 1024 * 1024;
struct WS {
    char* base;
    __host__ __device__ unsigned* exA_() const { return (unsigned*)(base + OFF_exA); }
    __host__ __device__ unsigned* exB_() const { return (unsigned*)(base + OFF_exB); }
    __host__ __device__ unsigned* flags_() const { return (unsigned*)(base + OFF_flags); }
    __host__ __device__ u16* xn_() const { return (u16*)(base + OFF_xn); }
    __host__ __device__ u16* y_() const { return (u16*)(base + OFF_y); }
    __host__ __device__ u16* WqkvT_() const { return (u16*)(base + OFF_WqkvT); }
    __host__ __device__ u16* WqkvF_() const { return (u16*)(base + OFF_WqkvF); }
    __host__ __device__ u16* WoF_() const { return (u16*)(base + OFF_WoF); }
    __host__ __device__ u16* W1T_() const { return (u16*)(base + OFF_W1T); }
    __host__ __device__ u16* W2T_() const { return (u16*)(base + OFF_W2T); }
    __host__ __device__ u16* Q_() const { return (u16*)(base + OFF_Q); }
    __host__ __device__ u16* K_() const { return (u16*)(base + OFF_K); }
    __host__ __device__ u16* VT_() const { return (u16*)(base + OFF_VT); }
    __host__ __device__ u16* hmid_() const { return (u16*)(base + OFF_hmid); }
    __host__ __device__ u16* g_() const { return (u16*)(base + OFF_g); }
    __host__ __device__ u16* km_() const { return (u16*)(base + OFF_km); }
    __host__ __device__ u16* vmT_() const { return (u16*)(base + OFF_vmT); }
    __host__ __device__ float* part_() const { return (float*)(base + OFF_part); }
    __host__ __device__ unsigned* flagKV_() const { return flags_(); }
    __host__ __device__ unsigned* flagQ_() const { return flags_() + 1024; }
    __host__ __device__ unsigned* cntH_() const { return flags_() + 1536; }
    __host__ __device__ unsigned* flagG_() const { return flags_() + 1600; }
    __host__ __device__ unsigned* flagM_() const { return flags_() + 2048; }
    __host__ __device__ unsigned* cntO_() const { return flags_() + 4096; }
    __host__ __device__ unsigned long long* dbg_() const { return (unsigned long long*)(base + OFF_dbg); }
};

__device__ __forceinline__ float wave_sum(float v) {
#pragma unroll
    for (int m = 32; m >= 1; m >>= 1) v += __shfl_xor(v, m);
    return v;
}
__device__ __forceinline__ void ln8(float (&v)[8]) {
    float s = 0.f;
#pragma unroll
    for (int i = 0; i < 8; ++i) s += v[i];
    s = wave_sum(s);
    float mean = s * (1.f / 512.f);
    float q = 0.f;
#pragma unroll
    for (int i = 0; i < 8; ++i) { float d = v[i] - mean; q += d * d; }
    q = wave_sum(q);
    float var = rbf(q * (1.f / 512.f));
    float mb = rbf(mean);
    float sd = rbf(sqrtf(rbf(var + 1e-5f)));
#pragma unroll
    for (int i = 0; i < 8; ++i) v[i] = rbf(rbf(v[i] - mb) / sd);
}
__device__ __forceinline__ void transpose_tile(const u16* __restrict__ src, u16* __restrict__ dst, int R, int C, int ldd, int Cpad, int bx, int by, u16 (*t)[66]) {
    int c0 = bx * 64, r0 = by * 64;
    int tx = threadIdx.x & 63, ty = threadIdx.x >> 6;
    for (int i = ty; i < 64; i += 4) {
        int r = r0 + i, c = c0 + tx;
        t[i][tx] = (r < R && c < C) ? src[(size_t)r * C + c] : (u16)0;
    }
    __syncthreads();
    if (dst) {
        for (int i = ty; i < 64; i += 4) {
            int c = c0 + i, r = r0 + tx;
            if (c < Cpad && r < ldd) dst[(size_t)c * ldd + r] = t[tx][i];
        }
    }
}
template <class F>
__device__ __forceinline__ void emit_frags(u16 (*t)[66], F addr) {
    int l = threadIdx.x & 63, q = threadIdx.x >> 6;
#pragma unroll
    for (int i = 0; i < 2; ++i) {
        int f = q * 2 + i, ksl = f >> 2, ct = f & 3;
        u16 v[8];
#pragma unroll
        for (int j = 0; j < 8; ++j) v[j] = t[ksl * 32 + (l >> 4) * 8 + j][ct * 16 + (l & 15)];
        u32x4 o = {(unsigned)v[0] | ((unsigned)v[1] << 16), (unsigned)v[2] | ((unsigned)v[3] << 16), (unsigned)v[4] | ((unsigned)v[5] << 16), (unsigned)v[6] | ((unsigned)v[7] << 16)};
        *(u32x4*)(addr(ksl, ct) + l * 8) = o;
    }
}
#define PREP_LN 512
#define PREP_TQKV 1536
#define PREP_TWO 512
#define PREP_TW1 176
#define PREP_TW2 176
#define ZERO_BYTES ((size_t)64 * 524288 + 68 * 16384 + 65536)
#define PREP_ZERO ((int)(ZERO_BYTES / 65536))
__global__ void __launch_bounds__(256) prep_k(const u16* __restrict__ x, const u16* __restrict__ Wq, const u16* __restrict__ Wk, const u16* __restrict__ Wv,
                                              const u16* __restrict__ Wo, const u16* __restrict__ w1, const u16* __restrict__ w2, WS ws) {
    __shared__ u16 t[64][66];
    int b = blockIdx.x;
    if (b < PREP_ZERO) {
        u32x4 z = {0u, 0u, 0u, 0u};
        u32x4* p = (u32x4*)((char*)ws.exA_() + (size_t)b * 65536) + threadIdx.x;
#pragma unroll
        for (int i = 0; i < 16; ++i) p[i * 256] = z;
        return;
    }
    b -= PREP_ZERO;
    if (b < PREP_LN) {
        int row = b * 4 + (threadIdx.x >> 6);
        int lane = threadIdx.x & 63;
        u32x4 r = *(const u32x4*)(x + (size_t)row * DM + lane * 8);
        float v[8];
#pragma unroll
        for (int i = 0; i < 4; ++i) { v[2 * i] = bf2f(r[i] & 0xffff); v[2 * i + 1] = bf2f(r[i] >> 16); }
        ln8(v);
        u32x4 o;
#pragma unroll
        for (int i = 0; i < 4; ++i) o[i] = pack2(v[2 * i], v[2 * i + 1]);
        *(u32x4*)(ws.xn_() + (size_t)row * DM + lane * 8) = o;
        ln8(v);
#pragma unroll
        for (int i = 0; i < 4; ++i) o[i] = pack2(v[2 * i], v[2 * i + 1]);
        *(u32x4*)(ws.y_() + (size_t)row * DM + lane * 8) = o;
        return;
    }
    b -= PREP_LN;
    if (b < PREP_TQKV) {
        int m = b / 512, r = b % 512;
        const u16* src = m == 0 ? Wq : (m == 1 ? Wk : Wv);
        const int bx = r % 64, by = r / 64;
        transpose_tile(src, ws.WqkvT_() + (size_t)m * HD * DM, DM, HD, DM, HD, bx, by, t);
        emit_frags(t, [&](int ksl, int ct) {
            int n = bx * 64 + ct * 16, ks = by * 2 + ksl;
            int hh = n >> 7, ww = (n >> 5) & 3, tt = (n >> 4) & 1;
            return ws.WqkvF_() + ((size_t)(((hh * 4 + ww) * 6 + m * 2 + tt) * 16 + ks)) * 512;
        });
        return;
    }
    b -= PREP_TQKV;
    if (b < PREP_TWO) {
        const int bx = b % 8, by = b / 8;
        transpose_tile(Wo, nullptr, HD, DM, HD, DM, bx, by, t);
        emit_frags(t, [&](int ksl, int ct) {
            int tile = bx * 4 + ct, k = by * 64 + ksl * 32;
            return ws.WoF_() + ((size_t)((tile * 32 + (k >> 7)) * 4 + ((k >> 5) & 3))) * 512;
        });
        return;
    }
    b -= PREP_TWO;
    if (b < PREP_TW1) { transpose_tile(w1, ws.W1T_(), DM, FF, DM, FFP, b % 22, b / 22, t); return; }
    b -= PREP_TW1;
    transpose_tile(w2, ws.W2T_(), FF, DM, FFP, DM, b % 8, b / 8, t);
}

#define SCALE 0.08838834764831845f
#define MEM_STRIDE 1056
#define QK_STRIDE 288

typedef __amdgpu_buffer_rsrc_t rsrc_t;
__device__ __forceinline__ rsrc_t mk_rsrc(const void* p) { return __builtin_amdgcn_make_buffer_rsrc((void*)p, 0, 0x7fffffff, 0x00020000); }
__device__ __forceinline__ u32x4 bld16(rsrc_t r, unsigned vo, unsigned so) { return __builtin_amdgcn_raw_buffer_load_b128(r, vo, so, 0); }
__device__ __forceinline__ u32x4 bld16_sc1(rsrc_t r, unsigned vo, unsigned so) { return __builtin_amdgcn_raw_buffer_load_b128(r, vo, so, 16); }
__device__ __forceinline__ u32x2 bld8(rsrc_t r, unsigned vo, unsigned so) { return __builtin_amdgcn_raw_buffer_load_b64(r, vo, so, 0); }
__device__ __forceinline__ void bst16_sc1(rsrc_t r, unsigned vo, unsigned so, u32x4 v) { __builtin_amdgcn_raw_buffer_store_b128(v, r, vo, so, 16); }
__device__ __forceinline__ void bst8(rsrc_t r, unsigned vo, unsigned so, u32x2 v) { __builtin_amdgcn_raw_buffer_store_b64(v, r, vo, so, 0); }
__device__ __forceinline__ void bst8_sc1(rsrc_t r, unsigned vo, unsigned so, u32x2 v) { __builtin_amdgcn_raw_buffer_store_b64(v, r, vo, so, 16); }

#define STAMP(i)
#define MFMA0_A(acc, a, b) asm volatile("v_mfma_f32_16x16x32_bf16 %0, %1, %2, 0" : "=&v"(acc) : "a"(a), "v"(b))
#define MFMA_A(acc, a, b) asm volatile("v_mfma_f32_16x16x32_bf16 %0, %1, %2, %0" : "+v"(acc) : "a"(a), "v"(b))
#define MFMA0_V(acc, a, b) asm volatile("v_mfma_f32_16x16x32_bf16 %0, %1, %2, 0" : "=&v"(acc) : "v"(a), "v"(b))
#define MFMA_V(acc, a, b) asm volatile("v_mfma_f32_16x16x32_bf16 %0, %1, %2, %0" : "+v"(acc) : "v"(a), "v"(b))
#define BARRIER() asm volatile("s_waitcnt lgkmcnt(0)\n\ts_barrier" ::: "memory")
#define LAS __attribute__((address_space(3)))

__device__ __forceinline__ float xmax16(float x) {
    unsigned u = __float_as_uint(x);
    auto r = __builtin_amdgcn_permlane16_swap(u, u, false, false);
    return fmaxf(__uint_as_float(r[0]), __uint_as_float(r[1]));
}
__device__ __forceinline__ float xmax32(float x) {
    unsigned u = __float_as_uint(x);
    auto r = __builtin_amdgcn_permlane32_swap(u, u, false, false);
    return fmaxf(__uint_as_float(r[0]), __uint_as_float(r[1]));
}
__device__ __forceinline__ float xsum16(float x) {
    unsigned u = __float_as_uint(x);
    auto r = __builtin_amdgcn_permlane16_swap(u, u, false, false);
    return __uint_as_float(r[0]) + __uint_as_float(r[1]);
}
__device__ __forceinline__ float xsum32(float x) {
    unsigned u = __float_as_uint(x);
    auto r = __builtin_amdgcn_permlane32_swap(u, u, false, false);
    return __uint_as_float(r[0]) + __uint_as_float(r[1]);
}
__device__ __forceinline__ unsigned ror8u(unsigned x) { return __builtin_amdgcn_update_dpp(0u, x, 0x128, 0xf, 0xf, false); }
__device__ __forceinline__ float ror8(float x) { return __uint_as_float(ror8u(__float_as_uint(x))); }
__device__ __forceinline__ float xsum8(float x) {
    return x + __uint_as_float(__builtin_amdgcn_update_dpp(0u, __float_as_uint(x), 0x128, 0xf, 0xf, false));
}

#define DELAY_A 12
#define DELAY_B 12
#define EXA_STEP 524288u
#define EXB_STEP 16384u
__device__ __forceinline__ void chain_body(const u16* __restrict__ mem0, const WS& ws, char* smem, const int h) {
    char* sW = smem;
    char* sKV = smem + 131072;
    char* sMem = smem + 147456;
    char* sQK = sMem + 8 * MEM_STRIDE;
    char* sO = sQK + 16 * QK_STRIDE;
    const int tid = threadIdx.x, lane = tid & 63;
    const int w = __builtin_amdgcn_readfirstlane(tid >> 6);
    const int fr = lane & 15, fg = lane >> 4, fr8 = lane & 7;
    const unsigned l16 = lane * 16;
    char* lw = sW + w * 32768 + l16;

    bf16x8 wqk[3][16], wv[24], wo[24];
#pragma unroll
    for (int t = 0; t < 6; ++t) {
        const u16* p = ws.WqkvF_() + ((size_t)((h * 4 + w) * 6 + t) * 16) * 512 + lane * 8;
#pragma unroll
        for (int ks = 0; ks < 16; ++ks) {
            bf16x8 v = *(const bf16x8*)(p + ks * 512);
            if (t < 3) wqk[t][ks] = v;
            else if (t == 3) *(bf16x8*)(lw + ks * 1024) = v;
            else if (t == 4) wv[ks] = v;
            else if (ks < 8) wv[16 + ks] = v;
            else *(bf16x8*)(lw + (16 + ks - 8) * 1024) = v;
        }
    }
#pragma unroll
    for (int t = 0; t < 8; ++t)
#pragma unroll
        for (int ks = 0; ks < 4; ++ks) {
            bf16x8 v = *(const bf16x8*)(ws.WoF_() + ((size_t)(((w * 8 + t) * 32 + h) * 4 + ks)) * 512 + lane * 8);
            if (t < 6) wo[t * 4 + ks] = v;
            else *(bf16x8*)(lw + (24 + (t - 6) * 4 + ks) * 1024) = v;
        }
    const rsrc_t rW = mk_rsrc(ws.base);
    const unsigned stA_vo = (unsigned)fg * 128 + (fr & 7) * 16 + (fr >> 3) * 16384;
    const unsigned rdA_vo = (unsigned)(lane >> 3) * 512 + (lane & 7) * 16;
    const char* memrd = sMem + fr8 * MEM_STRIDE + fg * 16;
    const char* qrd = sQK + fr8 * QK_STRIDE + fg * 16;
    const char* ord_ = sO + fr8 * QK_STRIDE + fg * 16;
    char* memwr = sMem + (lane & 7) * MEM_STRIDE + ((w * 2 + (lane >> 5)) * 16 + ((lane >> 3) & 3) * 4) * 2;

    for (int s = 0; s < NSTEPS; ++s) {
        STAMP(0);
        if ((s & 3) == 0) {
            const unsigned* f = ws.flagKV_() + (s >> 2) * 64 + (lane & 1) * 32 + h;
            for (unsigned spin = 0; spin < (1u << 22); ++spin) {
                unsigned v = __hip_atomic_load(f, __ATOMIC_RELAXED, __HIP_MEMORY_SCOPE_AGENT);
                if (__all(v != 0)) break;
                __builtin_amdgcn_s_sleep(2);
            }
        }
        {
            const u16* gk = ws.K_() + (size_t)(s * NH + h) * 4096 + w * 1024 + lane * 8;
            const u16* gv = ws.VT_() + (size_t)(s * NH + h) * 4096 + w * 1024 + lane * 8;
            __builtin_amdgcn_global_load_lds((const unsigned*)gk, (LAS unsigned*)(sKV + w * 2048), 16, 0, 16);
            __builtin_amdgcn_global_load_lds((const unsigned*)(gk + 512), (LAS unsigned*)(sKV + w * 2048 + 1024), 16, 0, 16);
            __builtin_amdgcn_global_load_lds((const unsigned*)gv, (LAS unsigned*)(sKV + 8192 + w * 2048), 16, 0, 16);
            __builtin_amdgcn_global_load_lds((const unsigned*)(gv + 512), (LAS unsigned*)(sKV + 8192 + w * 2048 + 1024), 16, 0, 16);
        }
        if (s == 0) {
            for (int i = tid; i < 8 * 64; i += 256) {
                int row = i >> 6, c = (i & 63) * 8;
                *(u32x4*)(sMem + row * MEM_STRIDE + c * 2) = *(const u32x4*)(mem0 + row * DM + c);
            }
            asm volatile("s_waitcnt vmcnt(0)" ::: "memory");
        } else {
#pragma unroll 1
            for (int d = 0; d < DELAY_B; ++d) __builtin_amdgcn_s_sleep(1);
            u32x4 vv[4];
            for (unsigned spin = 0;; ++spin) {
                if (spin > (1u << 20)) break;
                asm volatile("" ::: "memory");
#pragma unroll
                for (int i = 0; i < 4; ++i) vv[i] = bld16_sc1(rW, l16, ((unsigned)(i * 4096 + w * 1024) + (unsigned)s * EXB_STEP) + (unsigned)OFF_exB);
                unsigned am_ = 1u;
#pragma unroll
                for (int i = 0; i < 4; ++i) am_ &= (vv[i][0] & vv[i][1]) & (vv[i][2] & vv[i][3]);
                if (__all(am_ != 0)) break;
            }
#pragma unroll
            for (int i = 0; i < 4; ++i) {
                u32x2 o = {(vv[i][0] >> 16) | (vv[i][1] & 0xffff0000u), (vv[i][2] >> 16) | (vv[i][3] & 0xffff0000u)};
                *(u32x2*)(memwr + i * 256) = o;
            }
        }
        bf16x8 kw[2], vw[2];
#pragma unroll
        for (int i = 0; i < 2; ++i) kw[i] = *(const bf16x8*)(lw + i * 1024);
        vw[0] = *(const bf16x8*)(lw + 16 * 1024); vw[1] = *(const bf16x8*)(lw + 17 * 1024);
        STAMP(1);
        BARRIER();
        STAMP(2);
        if (s > 0 && tid == 0) __hip_atomic_store(ws.flagM_() + (s - 1) * NH + h, 1u, __ATOMIC_RELAXED, __HIP_MEMORY_SCOPE_AGENT);
        f32x4 acc[4], av[2];
        {
            bf16x8 mb[3];
#pragma unroll
            for (int i = 0; i < 3; ++i) mb[i] = *(const bf16x8*)(memrd + i * 64);
#pragma unroll
            for (int ks = 0; ks < 16; ++ks) {
                bf16x8 m = mb[ks % 3];
                bf16x8 k3 = kw[ks & 1];
                bf16x8 v5 = (ks < 8) ? wv[16 + ks] : vw[ks & 1];
                if (ks == 0) {
                    MFMA0_A(acc[0], wqk[0][ks], m); MFMA0_A(acc[1], wqk[1][ks], m); MFMA0_A(acc[2], wqk[2][ks], m); MFMA0_V(acc[3], k3, m);
                    MFMA0_V(av[0], m, wv[ks]); MFMA0_V(av[1], m, v5);
                } else {
                    MFMA_A(acc[0], wqk[0][ks], m); MFMA_A(acc[1], wqk[1][ks], m); MFMA_A(acc[2], wqk[2][ks], m); MFMA_V(acc[3], k3, m);
                    MFMA_V(av[0], m, wv[ks]); MFMA_V(av[1], m, v5);
                }
                if (ks + 3 < 16) mb[ks % 3] = *(const bf16x8*)(memrd + (ks + 3) * 64);
                if (ks + 2 < 16) kw[ks & 1] = *(const bf16x8*)(lw + (ks + 2) * 1024);
                if (ks >= 8 && ks + 2 < 16) vw[ks & 1] = *(const bf16x8*)(lw + (16 + ks + 2 - 8) * 1024);
            }
        }
        STAMP(3);
        asm volatile("s_nop 7" : "+v"(acc[0]), "+v"(acc[1]), "+v"(acc[2]), "+v"(acc[3]), "+v"(av[0]), "+v"(av[1]));
        u32x2 o[4];
        {
#pragma unroll
            for (int t = 0; t < 4; ++t) o[t] = (u32x2){pack2(acc[t][0], acc[t][1]), pack2(acc[t][2], acc[t][3])};
            if (fr < 8) {
#pragma unroll
                for (int t = 0; t < 4; ++t) {
                    int row = (t >> 1) * 8 + fr;
                    int c = w * 32 + (t & 1) * 16 + fg * 4;
                    *(u32x2*)(sQK + row * QK_STRIDE + c * 2) = o[t];
                }
            }
        }
        u32x4 am[2], ax[2];
#pragma unroll
        for (int t = 0; t < 2; ++t) {
            am[t] = (u32x4){pack2(av[t][0], av[t][1]), pack2(av[t][2], av[t][3]), 0u, 0u};
            if (fg >= 2) { am[t][0] = 0u; am[t][1] = 0u; }
        }
        bf16x8 kx[2][4];
#pragma unroll
        for (int ks = 0; ks < 4; ++ks) {
            kx[0][ks] = *(const bf16x8*)(sKV + ks * 1024 + l16);
            kx[1][ks] = *(const bf16x8*)(sKV + 4096 + ks * 1024 + l16);
        }
        ax[0] = *(const u32x4*)(sKV + 8192 + (w * 2) * 1024 + l16);
        ax[1] = *(const u32x4*)(sKV + 8192 + (w * 2 + 1) * 1024 + l16);
        STAMP(4);
        BARRIER();
        STAMP(5);
        f32x4 sm, sx0, sx1;
        {
            bf16x8 qf[4], kf[4];
#pragma unroll
            for (int ks = 0; ks < 4; ++ks) {
                qf[ks] = *(const bf16x8*)(qrd + ks * 64);
                kf[ks] = *(const bf16x8*)(qrd + 8 * QK_STRIDE + ks * 64);
            }
            MFMA0_V(sx0, kx[0][0], qf[0]); MFMA0_V(sx1, kx[1][0], qf[0]); MFMA0_V(sm, kf[0], qf[0]);
#pragma unroll
            for (int ks = 1; ks < 4; ++ks) { MFMA_V(sx0, kx[0][ks], qf[ks]); MFMA_V(sx1, kx[1][ks], qf[ks]); MFMA_V(sm, kf[ks], qf[ks]); }
        }
        STAMP(10);
        if (fr < 8) {
                bst8_sc1(rW, (unsigned)fr * 256 + fg * 8, ((unsigned)((s * NH + h) * 2048 + (w * 32) * 2)) + (unsigned)OFF_km, o[2]);
                bst8_sc1(rW, (unsigned)fr * 256 + fg * 8, ((unsigned)((s * NH + h) * 2048 + (w * 32 + 16) * 2)) + (unsigned)OFF_km, o[3]);
        }
        if (fg < 2) {
            bst8_sc1(rW, (unsigned)fr * 16 + fg * 8, ((unsigned)((s * NH + h) * 2048 + (w * 32) * 16)) + (unsigned)OFF_vmT, (u32x2){am[0][0], am[0][1]});
            bst8_sc1(rW, (unsigned)fr * 16 + fg * 8, ((unsigned)((s * NH + h) * 2048 + (w * 32 + 16) * 16)) + (unsigned)OFF_vmT, (u32x2){am[1][0], am[1][1]});
        }
        bf16x8 wf[8];
#pragma unroll
        for (int i = 0; i < 8; ++i) wf[i] = *(const bf16x8*)(lw + (24 + i) * 1024);
        asm volatile("s_nop 7" : "+v"(sm), "+v"(sx0), "+v"(sx1));
        const bool hi = fr >= 8;
        const f32x2 sc2 = {SCALE, SCALE};
        f32x2 p0 = {hi ? sx1[0] : sx0[0], hi ? sx1[1] : sx0[1]};
        f32x2 p1 = {hi ? sx1[2] : sx0[2], hi ? sx1[3] : sx0[3]};
        f32x2 p2 = {hi ? sm[2] : sm[0], hi ? sm[3] : sm[1]};
        p0 = rbf2(rbf2(p0) * sc2);
        p1 = rbf2(rbf2(p1) * sc2);
        p2 = rbf2(rbf2(p2) * sc2);
        if (fg >= 2) p2 = (f32x2){-3.0e38f, -3.0e38f};
        float mx = fmaxf(fmaxf(fmaxf(p0[0], p0[1]), fmaxf(p1[0], p1[1])), fmaxf(p2[0], p2[1]));
        mx = fmaxf(mx, ror8(mx));
        mx = xmax16(mx);
        mx = xmax32(mx);
        const float L2E = 1.4426950408889634f;
        const f32x2 l2 = {L2E, L2E};
        const f32x2 mxl = {mx, mx};
        p0 = (p0 - mxl) * l2; p1 = (p1 - mxl) * l2; p2 = (p2 - mxl) * l2;
        p0 = (f32x2){__builtin_amdgcn_exp2f(p0[0]), __builtin_amdgcn_exp2f(p0[1])};
        p1 = (f32x2){__builtin_amdgcn_exp2f(p1[0]), __builtin_amdgcn_exp2f(p1[1])};
        p2 = (f32x2){__builtin_amdgcn_exp2f(p2[0]), __builtin_amdgcn_exp2f(p2[1])};
        f32x2 s2 = (p0 + p1) + p2;
        float sum = s2[0] + s2[1];
        sum = sum + ror8(sum);
        sum = xsum16(sum);
        sum = xsum32(sum);
        float inv = __builtin_amdgcn_rcpf(sum);
        const f32x2 iv = {inv, inv};
        p0 *= iv; p1 *= iv; p2 *= iv;
        unsigned d0 = pack2(p0[0], p0[1]), d1 = pack2(p1[0], p1[1]), d2 = pack2(p2[0], p2[1]);
        unsigned e0 = ror8u(d0), e1 = ror8u(d1), e2 = ror8u(d2);
        u32x4 px = {d0, d1, e0, e1};
        u32x4 pm = {d2, e2, 0u, 0u};
        STAMP(12);
        f32x4 o0, o1;
        asm volatile("s_nop 1" : "+v"(px), "+v"(pm), "+v"(am[0]), "+v"(am[1]), "+v"(ax[0]), "+v"(ax[1]));
        MFMA0_V(o0, ax[0], px); MFMA0_V(o1, ax[1], px);
        MFMA_V(o0, am[0], pm); MFMA_V(o1, am[1], pm);
        asm volatile("s_nop 7" : "+v"(o0), "+v"(o1));
        if (fr < 8) {
            *(u32x2*)(sO + fr * QK_STRIDE + (w * 32 + fg * 4) * 2) = (u32x2){pack2(o0[0], o0[1]), pack2(o0[2], o0[3])};
            *(u32x2*)(sO + fr * QK_STRIDE + (w * 32 + 16 + fg * 4) * 2) = (u32x2){pack2(o1[0], o1[1]), pack2(o1[2], o1[3])};
        }
        STAMP(6);
        BARRIER();
        STAMP(7);
        bf16x8 of[4];
#pragma unroll
        for (int ks = 0; ks < 4; ++ks) of[ks] = *(const bf16x8*)(ord_ + ks * 64);
        {
            f32x4 a[4];
#pragma unroll
            for (int p = 0; p <= 4; ++p) {
                if (p < 4) {
#pragma unroll
                    for (int u = 0; u < 2; ++u) {
                        const int t = 2 * p + u;
                        f32x4& ac = a[(p & 1) * 2 + u];
#pragma unroll
                        for (int ks = 0; ks < 4; ++ks) {
                            if (t < 4) { if (ks == 0) MFMA0_A(ac, wo[t * 4 + ks], of[ks]); else MFMA_A(ac, wo[t * 4 + ks], of[ks]); }
                            else {
                                bf16x8 f = (t < 6) ? wo[t * 4 + ks] : wf[(t - 6) * 4 + ks];
                                if (ks == 0) MFMA0_V(ac, f, of[ks]); else MFMA_V(ac, f, of[ks]);
                            }
                        }
                    }
                }
                if (p >= 1) {
                    const int q0 = ((p - 1) & 1) * 2;
                    if (p == 4) asm volatile("s_nop 7" : "+v"(a[q0]), "+v"(a[q0 + 1]));
                    u32x4 o;
#pragma unroll
                    for (int q = 0; q < 4; ++q) o[q] = __float_as_uint(fr < 8 ? a[q0][q] : a[q0 + 1][q]) | 1u;
                    bst16_sc1(rW, stA_vo, ((unsigned)(((w * 8 + 2 * (p - 1)) * NH + h) * 512) + (unsigned)s * EXA_STEP) + (unsigned)OFF_exA, o);
                }
            }
        }
        STAMP(8);
        {
#pragma unroll 1
            for (int d = 0; d < DELAY_A; ++d) __builtin_amdgcn_s_sleep(1);
            float tot[4];
            for (unsigned spin = 0;; ++spin) {
                if (spin > (1u << 20)) break;
                asm volatile("" ::: "memory");
                u32x4 v[4];
#pragma unroll
                for (int i = 0; i < 4; ++i) v[i] = bld16_sc1(rW, rdA_vo, ((unsigned)(h * NH * 512 + i * 4096 + w * 128) + (unsigned)s * EXA_STEP) + (unsigned)OFF_exA);
                bool ok = true;
#pragma unroll
                for (int q = 0; q < 4; ++q) {
                    ok &= ((v[0][q] & v[1][q] & v[2][q] & v[3][q] & 1u) != 0);
                    tot[q] = (__uint_as_float(v[0][q]) + __uint_as_float(v[1][q])) + (__uint_as_float(v[2][q]) + __uint_as_float(v[3][q]));
                }
                if (__all(ok)) break;
            }
#pragma unroll
            for (int q = 0; q < 4; ++q) {
                tot[q] = xsum8(tot[q]);
                tot[q] = xsum16(tot[q]);
                tot[q] = xsum32(tot[q]);
            }
            STAMP(9);
            if (lane < 8) {
                u32x4 o;
#pragma unroll
                for (int q = 0; q < 4; ++q) o[q] = ((unsigned)f2bf(tot[q]) << 16) | 1u;
                bst16_sc1(rW, l16, ((unsigned)(h * 512 + w * 128) + (unsigned)(s + 1) * EXB_STEP) + (unsigned)OFF_exB, o);
            }
        }
    }
    asm volatile("s_waitcnt vmcnt(0)" ::: "memory");
    __syncthreads();
    if (tid == 0) __hip_atomic_store(ws.flagM_() + (NSTEPS - 1) * NH + h, 1u, __ATOMIC_RELAXED, __HIP_MEMORY_SCOPE_AGENT);
}

enum { EPI_QKV = 0, EPI_H = 1, EPI_G = 2 };
__device__ __forceinline__ float gelu_tanh(float v) {
    float u = 0.7978845608028654f * (v + 0.044715f * v * v * v);
    return 0.5f * v * (1.f + tanhf(u));
}
__device__ __forceinline__ void wait_flag_ge(const unsigned* f, unsigned want) {
    for (unsigned spin = 0; spin < (1u << 22); ++spin) {
        unsigned v = __hip_atomic_load(f, __ATOMIC_RELAXED, __HIP_MEMORY_SCOPE_AGENT);
        if (__all(v >= want)) break;
        __builtin_amdgcn_s_sleep(32);
    }
}
__device__ __forceinline__ void acquire_all() {
    __builtin_amdgcn_fence(__ATOMIC_ACQUIRE, "agent");
    asm volatile("s_waitcnt vmcnt(0)" ::: "memory");
    __syncthreads();
}
__device__ __forceinline__ void publish_done() {
    asm volatile("s_waitcnt vmcnt(0)" ::: "memory");
    __syncthreads();
}

template <int EPI>
__device__ __forceinline__ void gemm_tile(const u16* __restrict__ A, const u16* __restrict__ Bt, int lda, int ldb, int K, int brow, int bcol,
                                          const WS& ws, const u16* __restrict__ bias, char* lds_, const int tidx) {
    char (*lds)[2][128 * 128] = (char (*)[2][128 * 128])lds_;
    const int tid = tidx, lane = tid & 63, w = tid >> 6;
    const int wr = w >> 1, wc = w & 1;
    const int fr = lane & 15, fg = lane >> 4;
    f32x4 acc[4][4];
#pragma unroll
    for (int i = 0; i < 4; ++i)
#pragma unroll
        for (int j = 0; j < 4; ++j) acc[i][j] = (f32x4){0.f, 0.f, 0.f, 0.f};
    const int srow = tid >> 3, sslot = tid & 7;
    const u16* ga = A + (size_t)(brow + srow) * lda + sslot * 8;
    const u16* gb = Bt + (size_t)(bcol + srow) * ldb + sslot * 8;
    u32x4 ra[4], rb[4];
    auto gload = [&](int kt) {
#pragma unroll
        for (int i = 0; i < 4; ++i) {
            ra[i] = *(const u32x4*)(ga + (size_t)(32 * i) * lda + kt * 64);
            rb[i] = *(const u32x4*)(gb + (size_t)(32 * i) * ldb + kt * 64);
        }
    };
    auto swrite = [&](int buf) {
#pragma unroll
        for (int i = 0; i < 4; ++i) {
            int r = srow + 32 * i;
            int off = r * 128 + ((sslot ^ (r & 7)) << 4);
            *(u32x4*)(&lds[buf][0][off]) = ra[i];
            *(u32x4*)(&lds[buf][1][off]) = rb[i];
        }
    };
    const int nt = K / 64;
    gload(0);
    swrite(0);
    __syncthreads();
    const bool vtile = (EPI == EPI_QKV) && (bcol >= 2 * HD);
    for (int kt = 0; kt < nt; ++kt) {
        int buf = kt & 1;
        if (kt + 1 < nt) gload(kt + 1);
#pragma unroll
        for (int kk = 0; kk < 2; ++kk) {
            bf16x8 af[4], bfr[4];
#pragma unroll
            for (int i = 0; i < 4; ++i) {
                int r = wr * 64 + i * 16 + fr;
                af[i] = *(const bf16x8*)(&lds[buf][0][r * 128 + (((kk * 4 + fg) ^ (r & 7)) << 4)]);
                int c = wc * 64 + i * 16 + fr;
                bfr[i] = *(const bf16x8*)(&lds[buf][1][c * 128 + (((kk * 4 + fg) ^ (c & 7)) << 4)]);
            }
            if (vtile) {
#pragma unroll
                for (int i = 0; i < 4; ++i)
#pragma unroll
                    for (int j = 0; j < 4; ++j) acc[i][j] = __builtin_amdgcn_mfma_f32_16x16x32_bf16(af[i], bfr[j], acc[i][j], 0, 0, 0);
            } else {
#pragma unroll
                for (int i = 0; i < 4; ++i)
#pragma unroll
                    for (int j = 0; j < 4; ++j) acc[i][j] = __builtin_amdgcn_mfma_f32_16x16x32_bf16(bfr[j], af[i], acc[i][j], 0, 0, 0);
            }
        }
        if (kt + 1 < nt) swrite(buf ^ 1);
        __syncthreads();
    }
    const rsrc_t rW = mk_rsrc(ws.base);
#pragma unroll
    for (int i = 0; i < 4; ++i)
#pragma unroll
        for (int j = 0; j < 4; ++j) {
            f32x4 a = acc[i][j];
            if (vtile) {
                int col = bcol + wc * 64 + j * 16 + fr - 2 * HD;
                int tok = brow + wr * 64 + i * 16 + fg * 4;
                int seg = tok >> 5, tt = (tok >> 4) & 1, hh = col >> 7, et = (col >> 4) & 7;
                u32x2 o = {pack2(a[0], a[1]), pack2(a[2], a[3])};
                bst8_sc1(rW, (unsigned)((((seg * NH + hh) * 8 + et) * 512 + (fg * 16 + fr) * 8 + tt * 4) * 2), (0) + (unsigned)OFF_VT, o);
            } else {
                int tok = brow + wr * 64 + i * 16 + fr;
                int col = bcol + wc * 64 + j * 16 + fg * 4;
                if (EPI == EPI_QKV) {
                    int cc = col & (HD - 1);
                    int seg = tok >> 5, tt = (tok >> 4) & 1, hh = cc >> 7, e = cc & 127;
                    u32x2 o = {pack2(a[0], a[1]), pack2(a[2], a[3])};
                    unsigned off = (unsigned)((((seg * NH + hh) * 8 + tt * 4 + (e >> 5)) * 512 + (((e >> 3) & 3) * 16 + (tok & 15)) * 8 + (e & 4)) * 2);
                    if (col >= HD) bst8_sc1(rW, off, (0) + (unsigned)OFF_K, o); else bst8_sc1(rW, off, (0) + (unsigned)OFF_Q, o);
                } else if (EPI == EPI_H) {
                    float r[4];
#pragma unroll
                    for (int q = 0; q < 4; ++q) {
                        float b = (col + q < FF) ? bf2f(bias[col + q]) : 0.f;
                        float v = rbf(rbf(a[q]) + b);
                        r[q] = (col + q < FF) ? gelu_tanh(v) : 0.f;
                    }
                    u32x2 o = {pack2(r[0], r[1]), pack2(r[2], r[3])};
                    bst8_sc1(rW, (unsigned)((tok * FFP + col) * 2), (0) + (unsigned)OFF_hmid, o);
                } else {
                    float r[4];
#pragma unroll
                    for (int q = 0; q < 4; ++q) {
                        float v = rbf(rbf(a[q]) + bf2f(bias[col + q]));
                        r[q] = v / (1.f + __expf(-v));
                    }
                    u32x2 o = {pack2(r[0], r[1]), pack2(r[2], r[3])};
                    bst8_sc1(rW, (unsigned)((tok * DM + col) * 2), (0) + (unsigned)OFF_g, o);
                }
            }
        }
}

__device__ __forceinline__ void attn_task(int s, int hg, const WS& ws, char* lds, const int tidx) {
    const int lane = tidx & 63, w = __builtin_amdgcn_readfirstlane(tidx >> 6);
    const int h = hg * 4 + w;
    const int fr = lane & 15, fg = lane >> 4, fr8 = lane & 7;
    char* sO = lds;
    const u16* KF = ws.K_() + (size_t)(s * NH + h) * 4096;
    const u16* QF = ws.Q_() + (size_t)(s * NH + h) * 4096;
    const u16* VF = ws.VT_() + (size_t)(s * NH + h) * 4096;
    bf16x8 kx[2][4], kmf[4];
#pragma unroll
    for (int ks = 0; ks < 4; ++ks) {
        kmf[ks] = *(const bf16x8*)(ws.km_() + ((size_t)(s * NH + h) * 8 + fr8) * DH + ks * 32 + fg * 8);
#pragma unroll
        for (int t = 0; t < 2; ++t) kx[t][ks] = *(const bf16x8*)(KF + (t * 4 + ks) * 512 + lane * 8);
    }
    u32x4 px[2], pm[2];
#pragma unroll
    for (int qt = 0; qt < 2; ++qt) {
        f32x4 sm = {0.f, 0.f, 0.f, 0.f}, sx0 = sm, sx1 = sm;
#pragma unroll
        for (int ks = 0; ks < 4; ++ks) {
            bf16x8 qf = *(const bf16x8*)(QF + (qt * 4 + ks) * 512 + lane * 8);
            sm = __builtin_amdgcn_mfma_f32_16x16x32_bf16(kmf[ks], qf, sm, 0, 0, 0);
            sx0 = __builtin_amdgcn_mfma_f32_16x16x32_bf16(kx[0][ks], qf, sx0, 0, 0, 0);
            sx1 = __builtin_amdgcn_mfma_f32_16x16x32_bf16(kx[1][ks], qf, sx1, 0, 0, 0);
        }
        const int qi = qt * 16 + fr;
        float sv[12];
#pragma unroll
        for (int q = 0; q < 4; ++q) {
            sv[q] = (fg < 2) ? rbf(rbf(sm[q]) * SCALE) : -3.0e38f;
            int k0 = fg * 4 + q, k1 = 16 + fg * 4 + q;
            sv[4 + q] = (k0 <= qi) ? rbf(rbf(sx0[q]) * SCALE) : -3.0e38f;
            sv[8 + q] = (k1 <= qi) ? rbf(rbf(sx1[q]) * SCALE) : -3.0e38f;
        }
        float mx = sv[0];
#pragma unroll
        for (int q = 1; q < 12; ++q) mx = fmaxf(mx, sv[q]);
        mx = xmax16(mx); mx = xmax32(mx);
        float sum = 0.f;
#pragma unroll
        for (int q = 0; q < 12; ++q) { sv[q] = __expf(sv[q] - mx); sum += sv[q]; }
        sum = xsum16(sum); sum = xsum32(sum);
        float inv = 1.f / sum;
        px[qt] = (u32x4){pack2(sv[4] * inv, sv[5] * inv), pack2(sv[6] * inv, sv[7] * inv), pack2(sv[8] * inv, sv[9] * inv), pack2(sv[10] * inv, sv[11] * inv)};
        pm[qt] = (u32x4){pack2(sv[0] * inv, sv[1] * inv), pack2(sv[2] * inv, sv[3] * inv), 0u, 0u};
    }
#pragma unroll
    for (int et = 0; et < 8; ++et) {
        u32x4 ax = *(const u32x4*)(VF + et * 512 + lane * 8);
        u32x2 m0 = {0u, 0u};
        if (fg < 2) m0 = *(const u32x2*)(ws.vmT_() + ((size_t)(s * NH + h) * DH + et * 16 + fr) * 8 + fg * 4);
        u32x4 am = {m0[0], m0[1], 0u, 0u};
#pragma unroll
        for (int qt = 0; qt < 2; ++qt) {
            f32x4 o = {0.f, 0.f, 0.f, 0.f};
            o = __builtin_amdgcn_mfma_f32_16x16x32_bf16(__builtin_bit_cast(bf16x8, ax), __builtin_bit_cast(bf16x8, px[qt]), o, 0, 0, 0);
            o = __builtin_amdgcn_mfma_f32_16x16x32_bf16(__builtin_bit_cast(bf16x8, am), __builtin_bit_cast(bf16x8, pm[qt]), o, 0, 0, 0);
            u32x2 ov = {pack2(o[0], o[1]), pack2(o[2], o[3])};
            *(u32x2*)(sO + (qt * 16 + fr) * 1040 + (w * DH + et * 16 + fg * 4) * 2) = ov;
        }
    }
    __syncthreads();
    f32x4 acc[8][2];
#pragma unroll
    for (int t = 0; t < 8; ++t) { acc[t][0] = (f32x4){0.f, 0.f, 0.f, 0.f}; acc[t][1] = acc[t][0]; }
    const u16* wp = ws.WoF_() + ((size_t)(w * 8 * 32 + hg * 4) * 4) * 512 + lane * 8;
    {
        char* ring = lds + 33280 + w * 24576;
        const rsrc_t rW2 = mk_rsrc(ws.base);
        const unsigned wbase = (unsigned)OFF_WoF + (unsigned)((w * 8 * 32 + hg * 4) * 4) * 1024u;
        auto issue = [&](int ks) {
#pragma unroll
            for (int t = 0; t < 8; ++t)
                __builtin_amdgcn_raw_ptr_buffer_load_lds(rW2, (LAS void*)(ring + ((ks % 3) * 8 + t) * 1024), 16, lane * 16, wbase + (unsigned)(t * 128 + ks) * 1024u, 0, 0);
        };
        issue(0); issue(1); issue(2);
#pragma unroll
        for (int ks = 0; ks < 16; ++ks) {
            if (ks <= 13) asm volatile("s_waitcnt vmcnt(16)" ::: "memory");
            else if (ks == 14) asm volatile("s_waitcnt vmcnt(8)" ::: "memory");
            else asm volatile("s_waitcnt vmcnt(0)" ::: "memory");
            bf16x8 b0 = *(const bf16x8*)(sO + fr * 1040 + (ks * 32 + fg * 8) * 2);
            bf16x8 b1 = *(const bf16x8*)(sO + (16 + fr) * 1040 + (ks * 32 + fg * 8) * 2);
            bf16x8 a[8];
#pragma unroll
            for (int t = 0; t < 8; ++t) a[t] = *(const bf16x8*)(ring + ((ks % 3) * 8 + t) * 1024 + lane * 16);
            if (ks + 3 < 16) {
                asm volatile("s_waitcnt lgkmcnt(0)" ::: "memory");
                issue(ks + 3);
            }
#pragma unroll
            for (int t = 0; t < 8; ++t) {
                acc[t][0] = __builtin_amdgcn_mfma_f32_16x16x32_bf16(a[t], b0, acc[t][0], 0, 0, 0);
                acc[t][1] = __builtin_amdgcn_mfma_f32_16x16x32_bf16(a[t], b1, acc[t][1], 0, 0, 0);
            }
        }
    }
    const rsrc_t rW = mk_rsrc(ws.base);
#pragma unroll
    for (int t = 0; t < 8; ++t)
#pragma unroll
        for (int qt = 0; qt < 2; ++qt) {
            u32x4 o = {__float_as_uint(acc[t][qt][0]), __float_as_uint(acc[t][qt][1]), __float_as_uint(acc[t][qt][2]), __float_as_uint(acc[t][qt][3])};
            unsigned off = (unsigned)((((s * 8 + hg) * 32 + qt * 16 + fr) * 512 + w * 128 + t * 16 + fg * 4) * 4);
            bst16_sc1(rW, off, (0) + (unsigned)OFF_part, o);
        }
}

__device__ __forceinline__ void reduce_task(int s, int r0, const u16* __restrict__ x, u16* __restrict__ out, const WS& ws, const int tidx) {
    const int i = tidx;
    {
        int row = r0 + (i >> 7), c = (i & 127) * 4;
        f32x4 t = {0.f, 0.f, 0.f, 0.f};
        f32x4 pv[8];
#pragma unroll
        for (int hg = 0; hg < 8; ++hg) pv[hg] = *(const f32x4*)(ws.part_() + ((size_t)(s * 8 + hg) * 32 + row) * 512 + c);
#pragma unroll
        for (int hg = 0; hg < 8; ++hg) t += pv[hg];
        int tok = s * SEG + row;
        u32x2 xv = *(const u32x2*)(x + (size_t)tok * DM + c);
        u32x2 gv = *(const u32x2*)(ws.g_() + (size_t)tok * DM + c);
        float xs[4] = {bf2f(xv[0] & 0xffff), bf2f(xv[0] >> 16), bf2f(xv[1] & 0xffff), bf2f(xv[1] >> 16)};
        float gs[4] = {bf2f(gv[0] & 0xffff), bf2f(gv[0] >> 16), bf2f(gv[1] & 0xffff), bf2f(gv[1] >> 16)};
        float r[4];
#pragma unroll
        for (int q = 0; q < 4; ++q) r[q] = xs[q] + rbf(rbf(t[q]) * gs[q]);
        u32x2 o = {pack2(r[0], r[1]), pack2(r[2], r[3])};
        *(u32x2*)(out + (size_t)tok * DM + c) = o;
    }
}

#define NWORK 224
#define T_KV 1024
#define T_H 176
#define T_Q 512
#define T_G 64
#define T_A 512
#define T_TOTAL (T_KV + T_H + T_Q + T_G + T_A)
#define LDS_BYTES 162816

__global__ void __launch_bounds__(256, 1) mega_k(const u16* __restrict__ x, const u16* __restrict__ mem0, const u16* __restrict__ b1, const u16* __restrict__ b2,
                                                 u16* __restrict__ out, WS ws) {
    extern __shared__ __attribute__((aligned(16))) char smem[];
    if (blockIdx.x < NH) {
        chain_body(mem0, ws, smem, blockIdx.x);
        return;
    }
    const int tid = threadIdx.x, lane = tid & 63, w = tid >> 6;
    for (int task = blockIdx.x - NH; task < T_TOTAL; task += NWORK) {
        int t = task;
        int tidx = threadIdx.x;
        asm volatile("" : "+v"(tidx));
        if (t < T_KV) {
            int tb = t >> 6, cb = t & 63;
            gemm_tile<EPI_QKV>(ws.xn_(), ws.WqkvT_(), DM, DM, DM, tb * 128, HD + cb * 128, ws, nullptr, smem, tidx);
            publish_done();
            if (tid == 0) __hip_atomic_store(ws.flagKV_() + tb * 64 + cb, 1u, __ATOMIC_RELAXED, __HIP_MEMORY_SCOPE_AGENT);
            continue;
        }
        t -= T_KV;
        if (t < T_H) {
            int tb = t / 11, cb = t % 11;
            gemm_tile<EPI_H>(ws.y_(), ws.W1T_(), DM, DM, DM, tb * 128, cb * 128, ws, b1, smem, tidx);
            publish_done();
            if (tid == 0) __hip_atomic_fetch_add(ws.cntH_() + tb, 1u, __ATOMIC_RELAXED, __HIP_MEMORY_SCOPE_AGENT);
            continue;
        }
        t -= T_H;
        if (t < T_Q) {
            int tb = t >> 5, cb = t & 31;
            gemm_tile<EPI_QKV>(ws.xn_(), ws.WqkvT_(), DM, DM, DM, tb * 128, cb * 128, ws, nullptr, smem, tidx);
            publish_done();
            if (tid == 0) __hip_atomic_store(ws.flagQ_() + tb * 32 + cb, 1u, __ATOMIC_RELAXED, __HIP_MEMORY_SCOPE_AGENT);
            continue;
        }
        t -= T_Q;
        if (t < T_G) {
            int tb = t >> 2, cb = t & 3;
            if (w == 0) wait_flag_ge(ws.cntH_() + tb, 11u);
            acquire_all();
            gemm_tile<EPI_G>(ws.hmid_(), ws.W2T_(), FFP, FFP, FFP, tb * 128, cb * 128, ws, b2, smem, tidx);
            publish_done();
            if (tid == 0) __hip_atomic_store(ws.flagG_() + tb * 4 + cb, 1u, __ATOMIC_RELAXED, __HIP_MEMORY_SCOPE_AGENT);
            continue;
        }
        t -= T_G;
        {
            int s = t >> 3, hg = t & 7, tb = s >> 2;
            if (w == 0) {
                const unsigned* f;
                int l = lane & 15, hh = hg * 4 + (l & 3);
                if (l < 4) f = ws.flagKV_() + tb * 64 + hh;
                else if (l < 8) f = ws.flagKV_() + tb * 64 + 32 + hh;
                else if (l < 12) f = ws.flagQ_() + tb * 32 + hh;
                else f = ws.flagM_() + s * NH + hh;
                wait_flag_ge(f, 1u);
            }
            acquire_all();
            attn_task(s, hg, ws, smem, tidx);
            publish_done();
            if (tid == 0) __hip_atomic_fetch_add(ws.cntO_() + s, 1u, __ATOMIC_RELAXED, __HIP_MEMORY_SCOPE_AGENT);
            if (w == 0) { wait_flag_ge(lane < 4 ? ws.flagG_() + tb * 4 + lane : ws.cntO_() + s, lane < 4 ? 1u : 8u); }
            acquire_all();
            reduce_task(s, hg * 4, x, out, ws, tidx);
            reduce_task(s, hg * 4 + 2, x, out, ws, tidx);
            __syncthreads();
        }
    }
}

extern "C" void kernel_launch(void* const* d_in, const int* in_sizes, int n_in, void* d_out, int out_size, void* d_ws, size_t ws_size,
                              hipStream_t stream) {
    const u16* x = (const u16*)d_in[0];
    const u16* mem0 = (const u16*)d_in[1];
    const u16* Wq = (const u16*)d_in[2];
    const u16* Wk = (const u16*)d_in[3];
    const u16* Wv = (const u16*)d_in[4];
    const u16* Wo = (const u16*)d_in[5];
    const u16* w1 = (const u16*)d_in[8];
    const u16* b1 = (const u16*)d_in[9];
    const u16* w2 = (const u16*)d_in[10];
    const u16* b2 = (const u16*)d_in[11];
    u16* out = (u16*)d_out;

    WS ws; ws.base = (char*)d_ws;

    const int nprep = PREP_ZERO + PREP_LN + PREP_TQKV + PREP_TWO + PREP_TW1 + PREP_TW2;
    prep_k<<<nprep, 256, 0, stream>>>(x, Wq, Wk, Wv, Wo, w1, w2, ws);
    (void)hipFuncSetAttribute((const void*)mega_k, hipFuncAttributeMaxDynamicSharedMemorySize, LDS_BYTES);
    mega_k<<<256, 256, LDS_BYTES, stream>>>(x, mem0, b1, b2, out, ws);
}
```

```cpp
#include <hip/hip_runtime.h>
#include <hip/hip_bf16.h>
#include <stdint.h>

typedef __attribute__((ext_vector_type(8))) short bf16x8;
typedef __attribute__((ext_vector_type(4))) float f32x4;
typedef __attribute__((ext_vector_type(4))) unsigned u32x4;
typedef __attribute__((ext_vector_type(2))) unsigned u32x2;
typedef unsigned short u16;

#define S_TOK 2048
#define DM 512
#define NH 32
#define DH 128
#define HD 4096
#define NMEM 8
#define SEG 32
#define NSEG 64
#define NSTEPS 64
#define FF 1376
#define FFP 1408

__device__ __forceinline__ u16 f2bf(float f) { return __builtin_bit_cast(u16, (__bf16)f); }
__device__ __forceinline__ float bf2f(u16 h) { return __builtin_bit_cast(float, (unsigned)h << 16); }
__device__ __forceinline__ float rbf(float f) { return bf2f(f2bf(f)); }
typedef __attribute__((ext_vector_type(2))) float f32x2;
typedef __attribute__((ext_vector_type(2))) __bf16 bf16x2v;
__device__ __forceinline__ unsigned pack2(float a, float b) { f32x2 v = {a, b}; return __builtin_bit_cast(unsigned, __builtin_convertvector(v, bf16x2v)); }
__device__ __forceinline__ f32x2 rbf2(f32x2 v) { unsigned u = __builtin_bit_cast(unsigned, __builtin_convertvector(v, bf16x2v)); f32x2 r = {__uint_as_float(u << 16), __uint_as_float(u & 0xffff0000u)}; return r; }

constexpr size_t OFF_flags = 0;
constexpr size_t OFF_exB = OFF_flags + (((size_t)65536) + 255) / 256 * 256;
constexpr size_t OFF_exA = OFF_exB + (((size_t)68 * 16384) + 255) / 256 * 256;
constexpr size_t OFF_xnF = OFF_exA + (((size_t)64 * 524288) + 255) / 256 * 256;
constexpr size_t OFF_yF = OFF_xnF + (((size_t)S_TOK * DM * 2) + 255) / 256 * 256;
constexpr size_t OFF_WqkvF = OFF_yF + (((size_t)S_TOK * DM * 2) + 255) / 256 * 256;
constexpr size_t OFF_WoF = OFF_WqkvF + (((size_t)3 * HD * DM * 2) + 255) / 256 * 256;
constexpr size_t OFF_W1F = OFF_WoF + (((size_t)DM * HD * 2) + 255) / 256 * 256;
constexpr size_t OFF_W2F = OFF_W1F + (((size_t)FFP * DM * 2) + 255) / 256 * 256;
constexpr size_t OFF_Q = OFF_W2F + (((size_t)DM * FFP * 2) + 255) / 256 * 256;
constexpr size_t OFF_K = OFF_Q + (((size_t)S_TOK * HD * 2) + 255) / 256 * 256;
constexpr size_t OFF_VT = OFF_K + (((size_t)S_TOK * HD * 2) + 255) / 256 * 256;
constexpr size_t OFF_hmidF = OFF_VT + (((size_t)S_TOK * HD * 2) + 255) / 256 * 256;
constexpr size_t OFF_g = OFF_hmidF + (((size_t)S_TOK * FFP * 2) + 255) / 256 * 256;
constexpr size_t OFF_km = OFF_g + (((size_t)S_TOK * DM * 2) + 255) / 256 * 256;
constexpr size_t OFF_vmT = OFF_km + (((size_t)NSEG * NH * 8 * DH * 2) + 255) / 256 * 256;
constexpr size_t OFF_part = OFF_vmT + (((size_t)NSEG * NH * 8 * DH * 2) + 255) / 256 * 256;
constexpr size_t OFF_dbg = (size_t)240 * 1024 * 1024;
struct WS {
    char* base;
    __host__ __device__ unsigned* flags_() const { return (unsigned*)(base + OFF_flags); }
    __host__ __device__ unsigned* exB_() const { return (unsigned*)(base + OFF_exB); }
    __host__ __device__ unsigned* exA_() const { return (unsigned*)(base + OFF_exA); }
    __host__ __device__ u16* xnF_() const { return (u16*)(base + OFF_xnF); }
    __host__ __device__ u16* yF_() const { return (u16*)(base + OFF_yF); }
    __host__ __device__ u16* WqkvF_() const { return (u16*)(base + OFF_WqkvF); }
    __host__ __device__ u16* WoF_() const { return (u16*)(base + OFF_WoF); }
    __host__ __device__ u16* W1F_() const { return (u16*)(base + OFF_W1F); }
    __host__ __device__ u16* W2F_() const { return (u16*)(base + OFF_W2F); }
    __host__ __device__ u16* Q_() const { return (u16*)(base + OFF_Q); }
    __host__ __device__ u16* K_() const { return (u16*)(base + OFF_K); }
    __host__ __device__ u16* VT_() const { return (u16*)(base + OFF_VT); }
    __host__ __device__ u16* hmidF_() const { return (u16*)(base + OFF_hmidF); }
    __host__ __device__ u16* g_() const { return (u16*)(base + OFF_g); }
    __host__ __device__ u16* km_() const { return (u16*)(base + OFF_km); }
    __host__ __device__ u16* vmT_() const { return (u16*)(base + OFF_vmT); }
    __host__ __device__ float* part_() const { return (float*)(base + OFF_part); }
    __host__ __device__ unsigned* flagKV_() const { return flags_(); }
    __host__ __device__ unsigned* flagQ_() const { return flags_() + 1024; }
    __host__ __device__ unsigned* cntH_() const { return flags_() + 1536; }
    __host__ __device__ unsigned* flagG_() const { return flags_() + 1600; }
    __host__ __device__ unsigned* flagZ_() const { return flags_() + 1792; }
    __host__ __device__ unsigned* flagM_() const { return flags_() + 2048; }
    __host__ __device__ unsigned* cntO_() const { return flags_() + 4096; }
    __host__ __device__ unsigned long long* dbg_() const { return (unsigned long long*)(base + OFF_dbg); }
};

__device__ __forceinline__ float wave_sum(float v) {
#pragma unroll
    for (int m = 32; m >= 1; m >>= 1) v += __shfl_xor(v, m);
    return v;
}
__device__ __forceinline__ void ln8(float (&v)[8]) {
    float s = 0.f;
#pragma unroll
    for (int i = 0; i < 8; ++i) s += v[i];
    s = wave_sum(s);
    float mean = s * (1.f / 512.f);
    float q = 0.f;
#pragma unroll
    for (int i = 0; i < 8; ++i) { float d = v[i] - mean; q += d * d; }
    q = wave_sum(q);
    float var = rbf(q * (1.f / 512.f));
    float mb = rbf(mean);
    float sd = rbf(sqrtf(rbf(var + 1e-5f)));
#pragma unroll
    for (int i = 0; i < 8; ++i) v[i] = rbf(rbf(v[i] - mb) / sd);
}
template <class F>
__device__ __forceinline__ void frag_tile(const u16* __restrict__ src, int R, int C, int bx, int by, u16 (*t)[72], F addr) {
    const int c0 = bx * 64, r0 = by * 64;
#pragma unroll
    for (int i = 0; i < 2; ++i) {
        int idx = threadIdx.x + i * 256, rr = idx >> 3, cc = (idx & 7) * 8;
        int r = r0 + rr, c = c0 + cc;
        u32x4 v = {0u, 0u, 0u, 0u};
        if (r < R && c < C) v = *(const u32x4*)(src + (size_t)r * C + c);
        *(u32x4*)(&t[rr][cc]) = v;
    }
    __syncthreads();
    int l = threadIdx.x & 63, q = threadIdx.x >> 6;
#pragma unroll
    for (int i = 0; i < 2; ++i) {
        int f = q * 2 + i, ksl = f >> 2, ct = f & 3;
        u16 v[8];
#pragma unroll
        for (int j = 0; j < 8; ++j) v[j] = t[ksl * 32 + (l >> 4) * 8 + j][ct * 16 + (l & 15)];
        u32x4 o = {(unsigned)v[0] | ((unsigned)v[1] << 16), (unsigned)v[2] | ((unsigned)v[3] << 16), (unsigned)v[4] | ((unsigned)v[5] << 16), (unsigned)v[6] | ((unsigned)v[7] << 16)};
        *(u32x4*)(addr(ksl, ct) + l * 8) = o;
    }
}
#define PREP_LN 512
#define PREP_TQKV 1536
#define PREP_TWO 512
#define PREP_TW1 176
#define PREP_TW2 176
#define ZERO_BYTES ((size_t)68 * 16384 + 65536)
#define PREP_ZERO ((int)(ZERO_BYTES / 65536))
__global__ void __launch_bounds__(256) prep_k(const u16* __restrict__ x, const u16* __restrict__ Wq, const u16* __restrict__ Wk, const u16* __restrict__ Wv,
                                              const u16* __restrict__ Wo, const u16* __restrict__ w1, const u16* __restrict__ w2, WS ws) {
    __shared__ __attribute__((aligned(16))) u16 t[64][72];
    int b = blockIdx.x;
    if (b < PREP_LN) {
        int row = b * 4 + (threadIdx.x >> 6);
        int lane = threadIdx.x & 63;
        u32x4 r = *(const u32x4*)(x + (size_t)row * DM + lane * 8);
        float v[8];
#pragma unroll
        for (int i = 0; i < 4; ++i) { v[2 * i] = bf2f(r[i] & 0xffff); v[2 * i + 1] = bf2f(r[i] >> 16); }
        ln8(v);
        u32x4 o;
#pragma unroll
        for (int i = 0; i < 4; ++i) o[i] = pack2(v[2 * i], v[2 * i + 1]);
        const size_t fo = ((size_t)((row >> 4) * 16 + (lane >> 2)) * 64 + (lane & 3) * 16 + (row & 15)) * 8;
        *(u32x4*)(ws.xnF_() + fo) = o;
        ln8(v);
#pragma unroll
        for (int i = 0; i < 4; ++i) o[i] = pack2(v[2 * i], v[2 * i + 1]);
        *(u32x4*)(ws.yF_() + fo) = o;
        return;
    }
    b -= PREP_LN;
    if (b < PREP_TQKV) {
        int m = b / 512, r = b % 512;
        const u16* src = m == 0 ? Wq : (m == 1 ? Wk : Wv);
        const int bx = r % 64, by = r / 64;
        frag_tile(src, DM, HD, bx, by, t, [&](int ksl, int ct) {
            int n = bx * 64 + ct * 16, ks = by * 2 + ksl;
            int hh = n >> 7, ww = (n >> 5) & 3, tt = (n >> 4) & 1;
            return ws.WqkvF_() + ((size_t)(((hh * 4 + ww) * 6 + m * 2 + tt) * 16 + ks)) * 512;
        });
        return;
    }
    b -= PREP_TQKV;
    if (b < PREP_TWO) {
        const int bx = b % 8, by = b / 8;
        frag_tile(Wo, HD, DM, bx, by, t, [&](int ksl, int ct) {
            int tile = bx * 4 + ct, k = by * 64 + ksl * 32;
            return ws.WoF_() + ((size_t)((tile * 32 + (k >> 7)) * 4 + ((k >> 5) & 3))) * 512;
        });
        return;
    }
    b -= PREP_TWO;
    if (b < PREP_TW1) {
        const int bx = b % 22, by = b / 22;
        frag_tile(w1, DM, FF, bx, by, t, [&](int ksl, int ct) { return ws.W1F_() + ((size_t)((bx * 4 + ct) * 16 + by * 2 + ksl)) * 512; });
        return;
    }
    b -= PREP_TW1;
    if (b < PREP_TW2) {
        const int bx = b % 8, by = b / 8;
        frag_tile(w2, FF, DM, bx, by, t, [&](int ksl, int ct) { return ws.W2F_() + ((size_t)((bx * 4 + ct) * 44 + by * 2 + ksl)) * 512; });
        return;
    }
    b -= PREP_TW2;
    {
        u32x4 z = {0u, 0u, 0u, 0u};
        u32x4* p = (u32x4*)(ws.base + (size_t)b * 65536) + threadIdx.x;
#pragma unroll
        for (int i = 0; i < 16; ++i) p[i * 256] = z;
    }
}

#define SCALE 0.08838834764831845f
#define MEM_STRIDE 1056
#define QK_STRIDE 288

typedef __amdgpu_buffer_rsrc_t rsrc_t;
__device__ __forceinline__ rsrc_t mk_rsrc(const void* p) { return __builtin_amdgcn_make_buffer_rsrc((void*)p, 0, 0x7fffffff, 0x00020000); }
__device__ __forceinline__ u32x4 bld16(rsrc_t r, unsigned vo, unsigned so) { return __builtin_amdgcn_raw_buffer_load_b128(r, vo, so, 0); }
__device__ __forceinline__ u32x4 bld16_sc1(rsrc_t r, unsigned vo, unsigned so) { return __builtin_amdgcn_raw_buffer_load_b128(r, vo, so, 16); }
__device__ __forceinline__ u32x2 bld8(rsrc_t r, unsigned vo, unsigned so) { return __builtin_amdgcn_raw_buffer_load_b64(r, vo, so, 0); }
__device__ __forceinline__ void bst16_sc1(rsrc_t r, unsigned vo, unsigned so, u32x4 v) { __builtin_amdgcn_raw_buffer_store_b128(v, r, vo, so, 16); }
__device__ __forceinline__ void bst8(rsrc_t r, unsigned vo, unsigned so, u32x2 v) { __builtin_amdgcn_raw_buffer_store_b64(v, r, vo, so, 0); }
__device__ __forceinline__ void bst8_sc1(rsrc_t r, unsigned vo, unsigned so, u32x2 v) { __builtin_amdgcn_raw_buffer_store_b64(v, r, vo, so, 16); }

#define STAMP(i)
#define MFMA0_A(acc, a, b) asm volatile("v_mfma_f32_16x16x32_bf16 %0, %1, %2, 0" : "=&v"(acc) : "a"(a), "v"(b))
#define MFMA_A(acc, a, b) asm volatile("v_mfma_f32_16x16x32_bf16 %0, %1, %2, %0" : "+v"(acc) : "a"(a), "v"(b))
#define MFMA0_V(acc, a, b) asm volatile("v_mfma_f32_16x16x32_bf16 %0, %1, %2, 0" : "=&v"(acc) : "v"(a), "v"(b))
#define MFMA_V(acc, a, b) asm volatile("v_mfma_f32_16x16x32_bf16 %0, %1, %2, %0" : "+v"(acc) : "v"(a), "v"(b))
#define BARRIER() asm volatile("s_waitcnt lgkmcnt(0)\n\ts_barrier" ::: "memory")
#define LAS __attribute__((address_space(3)))

__device__ __forceinline__ float xmax16(float x) {
    unsigned u = __float_as_uint(x);
    auto r = __builtin_amdgcn_permlane16_swap(u, u, false, false);
    return fmaxf(__uint_as_float(r[0]), __uint_as_float(r[1]));
}
__device__ __forceinline__ float xmax32(float x) {
    unsigned u = __float_as_uint(x);
    auto r = __builtin_amdgcn_permlane32_swap(u, u, false, false);
    return fmaxf(__uint_as_float(r[0]), __uint_as_float(r[1]));
}
__device__ __forceinline__ float xsum16(float x) {
    unsigned u = __float_as_uint(x);
    auto r = __builtin_amdgcn_permlane16_swap(u, u, false, false);
    return __uint_as_float(r[0]) + __uint_as_float(r[1]);
}
__device__ __forceinline__ float xsum32(float x) {
    unsigned u = __float_as_uint(x);
    auto r = __builtin_amdgcn_permlane32_swap(u, u, false, false);
    return __uint_as_float(r[0]) + __uint_as_float(r[1]);
}
__device__ __forceinline__ unsigned ror8u(unsigned x) { return __builtin_amdgcn_update_dpp(0u, x, 0x128, 0xf, 0xf, false); }
__device__ __forceinline__ float ror8(float x) { return __uint_as_float(ror8u(__float_as_uint(x))); }
__device__ __forceinline__ float xsum8(float x) {
    return x + __uint_as_float(__builtin_amdgcn_update_dpp(0u, __float_as_uint(x), 0x128, 0xf, 0xf, false));
}

#define DELAY_A 12
#define DELAY_B 12
#define EXA_STEP 524288u
#define EXB_STEP 16384u
__device__ __forceinline__ void chain_body(const u16* __restrict__ mem0, const WS& ws, char* smem, const int h) {
    char* sW = smem;
    char* sKV = smem + 131072;
    char* sMem = smem + 147456;
    char* sQK = sMem + 8 * MEM_STRIDE;
    char* sO = sQK + 16 * QK_STRIDE;
    const int tid = threadIdx.x, lane = tid & 63;
    const int w = __builtin_amdgcn_readfirstlane(tid >> 6);
    const int fr = lane & 15, fg = lane >> 4, fr8 = lane & 7;
    const unsigned l16 = lane * 16;
    char* lw = sW + w * 32768 + l16;

    bf16x8 wqk[3][16], wv[24], wo[24];
#pragma unroll
    for (int t = 0; t < 6; ++t) {
        const u16* p = ws.WqkvF_() + ((size_t)((h * 4 + w) * 6 + t) * 16) * 512 + lane * 8;
#pragma unroll
        for (int ks = 0; ks < 16; ++ks) {
            bf16x8 v = *(const bf16x8*)(p + ks * 512);
            if (t < 3) wqk[t][ks] = v;
            else if (t == 3) *(bf16x8*)(lw + ks * 1024) = v;
            else if (t == 4) wv[ks] = v;
            else if (ks < 8) wv[16 + ks] = v;
            else *(bf16x8*)(lw + (16 + ks - 8) * 1024) = v;
        }
    }
#pragma unroll
    for (int t = 0; t < 8; ++t)
#pragma unroll
        for (int ks = 0; ks < 4; ++ks) {
            bf16x8 v = *(const bf16x8*)(ws.WoF_() + ((size_t)(((w * 8 + t) * 32 + h) * 4 + ks)) * 512 + lane * 8);
            if (t < 6) wo[t * 4 + ks] = v;
            else *(bf16x8*)(lw + (24 + (t - 6) * 4 + ks) * 1024) = v;
        }
    for (int i = tid; i < 8 * 64; i += 256) {
        int row = i >> 6, c = (i & 63) * 8;
        *(u32x4*)(sMem + row * MEM_STRIDE + c * 2) = *(const u32x4*)(mem0 + row * DM + c);
    }
    const rsrc_t rW = mk_rsrc(ws.base);
    const unsigned stA_vo = (unsigned)fg * 128 + (fr & 7) * 16 + (fr >> 3) * 16384;
    const unsigned rdA_vo = (unsigned)(lane >> 3) * 512 + (lane & 7) * 16;
    const char* memrd = sMem + fr8 * MEM_STRIDE + fg * 16;
    const char* qrd = sQK + fr8 * QK_STRIDE + fg * 16;
    const char* ord_ = sO + fr8 * QK_STRIDE + fg * 16;
    char* memwr = sMem + (lane & 7) * MEM_STRIDE + ((w * 2 + (lane >> 5)) * 16 + ((lane >> 3) & 3) * 4) * 2;

    for (int s = 0; s < NSTEPS; ++s) {
        STAMP(0);
        if ((s & 3) == 0) {
            const unsigned* f = (lane & 4) ? (ws.flagZ_() + s + (lane & 3)) : (ws.flagKV_() + (s >> 2) * 64 + (lane & 1) * 32 + h);
            for (unsigned spin = 0; spin < (1u << 22); ++spin) {
                unsigned v = __hip_atomic_load(f, __ATOMIC_RELAXED, __HIP_MEMORY_SCOPE_AGENT);
                if (__all(v != 0)) break;
                __builtin_amdgcn_s_sleep(2);
            }
        }
        {
            const u16* gk = ws.K_() + (size_t)(s * NH + h) * 4096 + w * 1024 + lane * 8;
            const u16* gv = ws.VT_() + (size_t)(s * NH + h) * 4096 + w * 1024 + lane * 8;
            __builtin_amdgcn_global_load_lds((const unsigned*)gk, (LAS unsigned*)(sKV + w * 2048), 16, 0, 16);
            __builtin_amdgcn_global_load_lds((const unsigned*)(gk + 512), (LAS unsigned*)(sKV + w * 2048 + 1024), 16, 0, 16);
            __builtin_amdgcn_global_load_lds((const unsigned*)gv, (LAS unsigned*)(sKV + 8192 + w * 2048), 16, 0, 16);
            __builtin_amdgcn_global_load_lds((const unsigned*)(gv + 512), (LAS unsigned*)(sKV + 8192 + w * 2048 + 1024), 16, 0, 16);
        }
        if (s == 0) {
            asm volatile("s_waitcnt vmcnt(0)" ::: "memory");
        } else {
#pragma unroll 1
            for (int d = 0; d < DELAY_B; ++d) __builtin_amdgcn_s_sleep(1);
            u32x4 vv[4];
            for (unsigned spin = 0;; ++spin) {
                if (spin > (1u << 20)) break;
                asm volatile("" ::: "memory");
#pragma unroll
                for (int i = 0; i < 4; ++i) vv[i] = bld16_sc1(rW, l16, ((unsigned)(i * 4096 + w * 1024) + (unsigned)s * EXB_STEP) + (unsigned)OFF_exB);
                unsigned am_ = 1u;
#pragma unroll
                for (int i = 0; i < 4; ++i) am_ &= (vv[i][0] & vv[i][1]) & (vv[i][2] & vv[i][3]);
                if (__all(am_ != 0)) break;
            }
#pragma unroll
            for (int i = 0; i < 4; ++i) {
                u32x2 o = {(vv[i][0] >> 16) | (vv[i][1] & 0xffff0000u), (vv[i][2] >> 16) | (vv[i][3] & 0xffff0000u)};
                *(u32x2*)(memwr + i * 256) = o;
            }
        }
        bf16x8 kw[2], vw[2];
#pragma unroll
        for (int i = 0; i < 2; ++i) kw[i] = *(const bf16x8*)(lw + i * 1024);
        vw[0] = *(const bf16x8*)(lw + 16 * 1024); vw[1] = *(const bf16x8*)(lw + 17 * 1024);
        STAMP(1);
        BARRIER();
        STAMP(2);
        f32x4 acc[4], av[2];
        {
            bf16x8 mb[3];
#pragma unroll
            for (int i = 0; i < 3; ++i) mb[i] = *(const bf16x8*)(memrd + i * 64);
#pragma unroll
            for (int ks = 0; ks < 16; ++ks) {
                bf16x8 m = mb[ks % 3];
                bf16x8 k3 = kw[ks & 1];
                bf16x8 v5 = (ks < 8) ? wv[16 + ks] : vw[ks & 1];
                if (ks == 0) {
                    MFMA0_A(acc[0], wqk[0][ks], m); MFMA0_A(acc[1], wqk[1][ks], m); MFMA0_A(acc[2], wqk[2][ks], m); MFMA0_V(acc[3], k3, m);
                    MFMA0_V(av[0], m, wv[ks]); MFMA0_V(av[1], m, v5);
                } else {
                    MFMA_A(acc[0], wqk[0][ks], m); MFMA_A(acc[1], wqk[1][ks], m); MFMA_A(acc[2], wqk[2][ks], m); MFMA_V(acc[3], k3, m);
                    MFMA_V(av[0], m, wv[ks]); MFMA_V(av[1], m, v5);
                }
                if (ks + 3 < 16) mb[ks % 3] = *(const bf16x8*)(memrd + (ks + 3) * 64);
                if (ks + 2 < 16) kw[ks & 1] = *(const bf16x8*)(lw + (ks + 2) * 1024);
                if (ks >= 8 && ks + 2 < 16) vw[ks & 1] = *(const bf16x8*)(lw + (16 + ks + 2 - 8) * 1024);
            }
        }
        STAMP(3);
        asm volatile("s_nop 7" : "+v"(acc[0]), "+v"(acc[1]), "+v"(acc[2]), "+v"(acc[3]), "+v"(av[0]), "+v"(av[1]));
        u32x2 o[4];
        {
#pragma unroll
            for (int t = 0; t < 4; ++t) o[t] = (u32x2){pack2(acc[t][0], acc[t][1]), pack2(acc[t][2], acc[t][3])};
            if (fr < 8) {
#pragma unroll
                for (int t = 0; t < 4; ++t) {
                    int row = (t >> 1) * 8 + fr;
                    int c = w * 32 + (t & 1) * 16 + fg * 4;
                    *(u32x2*)(sQK + row * QK_STRIDE + c * 2) = o[t];
                }
            }
        }
        u32x4 am[2], ax[2];
#pragma unroll
        for (int t = 0; t < 2; ++t) {
            am[t] = (u32x4){pack2(av[t][0], av[t][1]), pack2(av[t][2], av[t][3]), 0u, 0u};
            if (fg >= 2) { am[t][0] = 0u; am[t][1] = 0u; }
        }
        bf16x8 kx[2][4];
#pragma unroll
        for (int ks = 0; ks < 4; ++ks) {
            kx[0][ks] = *(const bf16x8*)(sKV + ks * 1024 + l16);
            kx[1][ks] = *(const bf16x8*)(sKV + 4096 + ks * 1024 + l16);
        }
        ax[0] = *(const u32x4*)(sKV + 8192 + (w * 2) * 1024 + l16);
        ax[1] = *(const u32x4*)(sKV + 8192 + (w * 2 + 1) * 1024 + l16);
        STAMP(4);
        BARRIER();
        STAMP(5);
        f32x4 sm, sx0, sx1;
        {
            bf16x8 qf[4], kf[4];
#pragma unroll
            for (int ks = 0; ks < 4; ++ks) {
                qf[ks] = *(const bf16x8*)(qrd + ks * 64);
                kf[ks] = *(const bf16x8*)(qrd + 8 * QK_STRIDE + ks * 64);
            }
            MFMA0_V(sx0, kx[0][0], qf[0]); MFMA0_V(sx1, kx[1][0], qf[0]); MFMA0_V(sm, kf[0], qf[0]);
#pragma unroll
            for (int ks = 1; ks < 4; ++ks) { MFMA_V(sx0, kx[0][ks], qf[ks]); MFMA_V(sx1, kx[1][ks], qf[ks]); MFMA_V(sm, kf[ks], qf[ks]); }
        }
        STAMP(10);
        if (fr < 8) {
                bst8_sc1(rW, (unsigned)fr * 256 + fg * 8, ((unsigned)((s * NH + h) * 2048 + (w * 32) * 2)) + (unsigned)OFF_km, o[2]);
                bst8_sc1(rW, (unsigned)fr * 256 + fg * 8, ((unsigned)((s * NH + h) * 2048 + (w * 32 + 16) * 2)) + (unsigned)OFF_km, o[3]);
        }
        if (fg < 2) {
            bst8_sc1(rW, (unsigned)fr * 16 + fg * 8, ((unsigned)((s * NH + h) * 2048 + (w * 32) * 16)) + (unsigned)OFF_vmT, (u32x2){am[0][0], am[0][1]});
            bst8_sc1(rW, (unsigned)fr * 16 + fg * 8, ((unsigned)((s * NH + h) * 2048 + (w * 32 + 16) * 16)) + (unsigned)OFF_vmT, (u32x2){am[1][0], am[1][1]});
        }
        bf16x8 wf[8];
#pragma unroll
        for (int i = 0; i < 8; ++i) wf[i] = *(const bf16x8*)(lw + (24 + i) * 1024);
        asm volatile("s_nop 7" : "+v"(sm), "+v"(sx0), "+v"(sx1));
        const bool hi = fr >= 8;
        const f32x2 sc2 = {SCALE, SCALE};
        f32x2 p0 = {hi ? sx1[0] : sx0[0], hi ? sx1[1] : sx0[1]};
        f32x2 p1 = {hi ? sx1[2] : sx0[2], hi ? sx1[3] : sx0[3]};
        f32x2 p2 = {hi ? sm[2] : sm[0], hi ? sm[3] : sm[1]};
        p0 = rbf2(rbf2(p0) * sc2);
        p1 = rbf2(rbf2(p1) * sc2);
        p2 = rbf2(rbf2(p2) * sc2);
        if (fg >= 2) p2 = (f32x2){-3.0e38f, -3.0e38f};
        float mx = fmaxf(fmaxf(fmaxf(p0[0], p0[1]), fmaxf(p1[0], p1[1])), fmaxf(p2[0], p2[1]));
        mx = fmaxf(mx, ror8(mx));
        mx = xmax16(mx);
        mx = xmax32(mx);
        const float L2E = 1.4426950408889634f;
        const f32x2 l2 = {L2E, L2E};
        const f32x2 mxl = {mx, mx};
        p0 = (p0 - mxl) * l2; p1 = (p1 - mxl) * l2; p2 = (p2 - mxl) * l2;
        p0 = (f32x2){__builtin_amdgcn_exp2f(p0[0]), __builtin_amdgcn_exp2f(p0[1])};
        p1 = (f32x2){__builtin_amdgcn_exp2f(p1[0]), __builtin_amdgcn_exp2f(p1[1])};
        p2 = (f32x2){__builtin_amdgcn_exp2f(p2[0]), __builtin_amdgcn_exp2f(p2[1])};
        f32x2 s2 = (p0 + p1) + p2;
        float sum = s2[0] + s2[1];
        sum = sum + ror8(sum);
        sum = xsum16(sum);
        sum = xsum32(sum);
        float inv = __builtin_amdgcn_rcpf(sum);
        const f32x2 iv = {inv, inv};
        p0 *= iv; p1 *= iv; p2 *= iv;
        unsigned d0 = pack2(p0[0], p0[1]), d1 = pack2(p1[0], p1[1]), d2 = pack2(p2[0], p2[1]);
        unsigned e0 = ror8u(d0), e1 = ror8u(d1), e2 = ror8u(d2);
        u32x4 px = {d0, d1, e0, e1};
        u32x4 pm = {d2, e2, 0u, 0u};
        STAMP(12);
        f32x4 o0, o1;
        asm volatile("s_nop 1" : "+v"(px), "+v"(pm), "+v"(am[0]), "+v"(am[1]), "+v"(ax[0]), "+v"(ax[1]));
        MFMA0_V(o0, ax[0], px); MFMA0_V(o1, ax[1], px);
        MFMA_V(o0, am[0], pm); MFMA_V(o1, am[1], pm);
        asm volatile("s_nop 7" : "+v"(o0), "+v"(o1));
        if (fr < 8) {
            *(u32x2*)(sO + fr * QK_STRIDE + (w * 32 + fg * 4) * 2) = (u32x2){pack2(o0[0], o0[1]), pack2(o0[2], o0[3])};
            *(u32x2*)(sO + fr * QK_STRIDE + (w * 32 + 16 + fg * 4) * 2) = (u32x2){pack2(o1[0], o1[1]), pack2(o1[2], o1[3])};
        }
        STAMP(6);
        BARRIER();
        STAMP(7);
        bf16x8 of[4];
#pragma unroll
        for (int ks = 0; ks < 4; ++ks) of[ks] = *(const bf16x8*)(ord_ + ks * 64);
        {
            f32x4 a[4];
#pragma unroll
            for (int p = 0; p <= 4; ++p) {
                if (p < 4) {
#pragma unroll
                    for (int u = 0; u < 2; ++u) {
                        const int t = 2 * p + u;
                        f32x4& ac = a[(p & 1) * 2 + u];
#pragma unroll
                        for (int ks = 0; ks < 4; ++ks) {
                            if (t < 4) { if (ks == 0) MFMA0_A(ac, wo[t * 4 + ks], of[ks]); else MFMA_A(ac, wo[t * 4 + ks], of[ks]); }
                            else {
                                bf16x8 f = (t < 6) ? wo[t * 4 + ks] : wf[(t - 6) * 4 + ks];
                                if (ks == 0) MFMA0_V(ac, f, of[ks]); else MFMA_V(ac, f, of[ks]);
                            }
                        }
                    }
                }
                if (p >= 1) {
                    const int q0 = ((p - 1) & 1) * 2;
                    if (p == 4) asm volatile("s_nop 7" : "+v"(a[q0]), "+v"(a[q0 + 1]));
                    u32x4 o;
#pragma unroll
                    for (int q = 0; q < 4; ++q) o[q] = __float_as_uint(fr < 8 ? a[q0][q] : a[q0 + 1][q]) | 1u;
                    bst16_sc1(rW, stA_vo, ((unsigned)(((w * 8 + 2 * (p - 1)) * NH + h) * 512) + (unsigned)s * EXA_STEP) + (unsigned)OFF_exA, o);
                }
            }
        }
        STAMP(8);
        if (s > 0 && tid == 192) __hip_atomic_store(ws.flagM_() + (s - 1) * NH + h, 1u, __ATOMIC_RELAXED, __HIP_MEMORY_SCOPE_AGENT);
        {
#pragma unroll 1
            for (int d = 0; d < DELAY_A; ++d) __builtin_amdgcn_s_sleep(1);
            float tot[4];
            for (unsigned spin = 0;; ++spin) {
                if (spin > (1u << 20)) break;
                asm volatile("" ::: "memory");
                u32x4 v[4];
#pragma unroll
                for (int i = 0; i < 4; ++i) v[i] = bld16_sc1(rW, rdA_vo, ((unsigned)(h * NH * 512 + i * 4096 + w * 128) + (unsigned)s * EXA_STEP) + (unsigned)OFF_exA);
                bool ok = true;
#pragma unroll
                for (int q = 0; q < 4; ++q) {
                    ok &= ((v[0][q] & v[1][q] & v[2][q] & v[3][q] & 1u) != 0);
                    tot[q] = (__uint_as_float(v[0][q]) + __uint_as_float(v[1][q])) + (__uint_as_float(v[2][q]) + __uint_as_float(v[3][q]));
                }
                if (__all(ok)) break;
            }
#pragma unroll
            for (int q = 0; q < 4; ++q) {
                tot[q] = xsum8(tot[q]);
                tot[q] = xsum16(tot[q]);
                tot[q] = xsum32(tot[q]);
            }
            STAMP(9);
            if (lane < 8) {
                u32x4 o;
#pragma unroll
                for (int q = 0; q < 4; ++q) o[q] = ((unsigned)f2bf(tot[q]) << 16) | 1u;
                bst16_sc1(rW, l16, ((unsigned)(h * 512 + w * 128) + (unsigned)(s + 1) * EXB_STEP) + (unsigned)OFF_exB, o);
            }
        }
    }
    asm volatile("s_waitcnt vmcnt(0)" ::: "memory");
    __syncthreads();
    if (tid == 0) __hip_atomic_store(ws.flagM_() + (NSTEPS - 1) * NH + h, 1u, __ATOMIC_RELAXED, __HIP_MEMORY_SCOPE_AGENT);
}

enum { EPI_QKV = 0, EPI_H = 1, EPI_G = 2 };
__device__ __forceinline__ float gelu_tanh(float v) {
    float u = 0.7978845608028654f * (v + 0.044715f * v * v * v);
    return 0.5f * v * (1.f + tanhf(u));
}
__device__ __forceinline__ void wait_flag_ge(const unsigned* f, unsigned want) {
    for (unsigned spin = 0; spin < (1u << 22); ++spin) {
        unsigned v = __hip_atomic_load(f, __ATOMIC_RELAXED, __HIP_MEMORY_SCOPE_AGENT);
        if (__all(v >= want)) break;
        __builtin_amdgcn_s_sleep(32);
    }
}
__device__ __forceinline__ void acquire_all() {
    __builtin_amdgcn_fence(__ATOMIC_ACQUIRE, "agent");
    asm volatile("s_waitcnt vmcnt(0)" ::: "memory");
    __syncthreads();
}
__device__ __forceinline__ void publish_done() {
    asm volatile("s_waitcnt vmcnt(0)" ::: "memory");
    __syncthreads();
}

template <int EPI, int NKS>
__device__ __forceinline__ void gemm_tile(unsigned offA, int tb, int cb, const WS& ws, const u16* __restrict__ bias, const int tidx) {
    const int lane = tidx & 63, w = __builtin_amdgcn_readfirstlane(tidx >> 6);
    const int wr = w >> 1, wc = w & 1;
    const int fr = lane & 15, fg = lane >> 4;
    const rsrc_t rW = mk_rsrc(ws.base);
    const unsigned l16 = lane * 16;
    const bool vtile = (EPI == EPI_QKV) && (cb >= 64);
    unsigned ao[4], bo[4];
#pragma unroll
    for (int i = 0; i < 4; ++i) {
        ao[i] = offA + (unsigned)((tb * 8 + wr * 4 + i) * NKS) * 1024u;
        int ct = cb * 8 + wc * 4 + i;
        if (EPI == EPI_QKV) {
            int m = ct >> 8, r = ct & 255;
            bo[i] = (unsigned)OFF_WqkvF + (unsigned)((((r >> 3) * 4 + ((r >> 1) & 3)) * 6 + m * 2 + (r & 1)) * 16) * 1024u;
        } else if (EPI == EPI_H) bo[i] = (unsigned)OFF_W1F + (unsigned)(ct * 16) * 1024u;
        else bo[i] = (unsigned)OFF_W2F + (unsigned)(ct * 44) * 1024u;
    }
    f32x4 acc[4][4];
#pragma unroll
    for (int i = 0; i < 4; ++i)
#pragma unroll
        for (int j = 0; j < 4; ++j) acc[i][j] = (f32x4){0.f, 0.f, 0.f, 0.f};
    constexpr int D = 4;
    u32x4 fa[D][4], fb[D][4];
#pragma unroll
    for (int d = 0; d < D; ++d)
#pragma unroll
        for (int i = 0; i < 4; ++i) { fa[d][i] = bld16(rW, l16, ao[i] + d * 1024u); fb[d][i] = bld16(rW, l16, bo[i] + d * 1024u); }
    for (int k0 = 0; k0 < NKS; k0 += D) {
#pragma unroll
        for (int d = 0; d < D; ++d) {
            u32x4 a[4], bq[4];
#pragma unroll
            for (int i = 0; i < 4; ++i) { a[i] = fa[d][i]; bq[i] = fb[d][i]; }
            const int kn = k0 + d + D;
            if (kn < NKS) {
#pragma unroll
                for (int i = 0; i < 4; ++i) { fa[d][i] = bld16(rW, l16, ao[i] + (unsigned)kn * 1024u); fb[d][i] = bld16(rW, l16, bo[i] + (unsigned)kn * 1024u); }
            }
            if (vtile) {
#pragma unroll
                for (int i = 0; i < 4; ++i)
#pragma unroll
                    for (int j = 0; j < 4; ++j) acc[i][j] = __builtin_amdgcn_mfma_f32_16x16x32_bf16(__builtin_bit_cast(bf16x8, a[i]), __builtin_bit_cast(bf16x8, bq[j]), acc[i][j], 0, 0, 0);
            } else {
#pragma unroll
                for (int i = 0; i < 4; ++i)
#pragma unroll
                    for (int j = 0; j < 4; ++j) acc[i][j] = __builtin_amdgcn_mfma_f32_16x16x32_bf16(__builtin_bit_cast(bf16x8, bq[j]), __builtin_bit_cast(bf16x8, a[i]), acc[i][j], 0, 0, 0);
            }
        }
    }
    const int brow = tb * 128, bcol = cb * 128;
#pragma unroll
    for (int i = 0; i < 4; ++i)
#pragma unroll
        for (int j = 0; j < 4; ++j) {
            f32x4 a = acc[i][j];
            if (vtile) {
                int col = bcol + wc * 64 + j * 16 + fr - 2 * HD;
                int tok = brow + wr * 64 + i * 16 + fg * 4;
                int seg = tok >> 5, tt = (tok >> 4) & 1, hh = col >> 7, et = (col >> 4) & 7;
                u32x2 o = {pack2(a[0], a[1]), pack2(a[2], a[3])};
                bst8_sc1(rW, (unsigned)((((seg * NH + hh) * 8 + et) * 512 + (fg * 16 + fr) * 8 + tt * 4) * 2), (unsigned)OFF_VT, o);
            } else {
                int tok = brow + wr * 64 + i * 16 + fr;
                int col = bcol + wc * 64 + j * 16 + fg * 4;
                if (EPI == EPI_QKV) {
                    int cc = col & (HD - 1);
                    int seg = tok >> 5, tt = (tok >> 4) & 1, hh = cc >> 7, e = cc & 127;
                    u32x2 o = {pack2(a[0], a[1]), pack2(a[2], a[3])};
                    unsigned off = (unsigned)((((seg * NH + hh) * 8 + tt * 4 + (e >> 5)) * 512 + (((e >> 3) & 3) * 16 + (tok & 15)) * 8 + (e & 4)) * 2);
                    bst8_sc1(rW, off, (col >= HD) ? (unsigned)OFF_K : (unsigned)OFF_Q, o);
                } else if (EPI == EPI_H) {
                    float r[4];
#pragma unroll
                    for (int q = 0; q < 4; ++q) {
                        float b = (col + q < FF) ? bf2f(bias[col + q]) : 0.f;
                        float v = rbf(rbf(a[q]) + b);
                        r[q] = (col + q < FF) ? gelu_tanh(v) : 0.f;
                    }
                    u32x2 o = {pack2(r[0], r[1]), pack2(r[2], r[3])};
                    unsigned off = (unsigned)((((tok >> 4) * 44 + (col >> 5)) * 64 + ((col >> 3) & 3) * 16 + (tok & 15)) * 16 + (col & 4) * 2);
                    bst8_sc1(rW, off, (unsigned)OFF_hmidF, o);
                } else {
                    float r[4];
#pragma unroll
                    for (int q = 0; q < 4; ++q) {
                        float v = rbf(rbf(a[q]) + bf2f(bias[col + q]));
                        r[q] = v / (1.f + __expf(-v));
                    }
                    u32x2 o = {pack2(r[0], r[1]), pack2(r[2], r[3])};
                    bst8_sc1(rW, (unsigned)((tok * DM + col) * 2), (unsigned)OFF_g, o);
                }
            }
        }
}

__device__ __forceinline__ void attn_task(int s, int hg, const WS& ws, char* lds, const int tidx) {
    const int lane = tidx & 63, w = __builtin_amdgcn_readfirstlane(tidx >> 6);
    const int h = hg * 4 + w;
    const int fr = lane & 15, fg = lane >> 4, fr8 = lane & 7;
    char* sO = lds;
    const u16* KF = ws.K_() + (size_t)(s * NH + h) * 4096;
    const u16* QF = ws.Q_() + (size_t)(s * NH + h) * 4096;
    const u16* VF = ws.VT_() + (size_t)(s * NH + h) * 4096;
    bf16x8 kx[2][4], kmf[4];
#pragma unroll
    for (int ks = 0; ks < 4; ++ks) {
        kmf[ks] = *(const bf16x8*)(ws.km_() + ((size_t)(s * NH + h) * 8 + fr8) * DH + ks * 32 + fg * 8);
#pragma unroll
        for (int t = 0; t < 2; ++t) kx[t][ks] = *(const bf16x8*)(KF + (t * 4 + ks) * 512 + lane * 8);
    }
    u32x4 px[2], pm[2];
#pragma unroll
    for (int qt = 0; qt < 2; ++qt) {
        f32x4 sm = {0.f, 0.f, 0.f, 0.f}, sx0 = sm, sx1 = sm;
#pragma unroll
        for (int ks = 0; ks < 4; ++ks) {
            bf16x8 qf = *(const bf16x8*)(QF + (qt * 4 + ks) * 512 + lane * 8);
            sm = __builtin_amdgcn_mfma_f32_16x16x32_bf16(kmf[ks], qf, sm, 0, 0, 0);
            sx0 = __builtin_amdgcn_mfma_f32_16x16x32_bf16(kx[0][ks], qf, sx0, 0, 0, 0);
            sx1 = __builtin_amdgcn_mfma_f32_16x16x32_bf16(kx[1][ks], qf, sx1, 0, 0, 0);
        }
        const int qi = qt * 16 + fr;
        float sv[12];
#pragma unroll
        for (int q = 0; q < 4; ++q) {
            sv[q] = (fg < 2) ? rbf(rbf(sm[q]) * SCALE) : -3.0e38f;
            int k0 = fg * 4 + q, k1 = 16 + fg * 4 + q;
            sv[4 + q] = (k0 <= qi) ? rbf(rbf(sx0[q]) * SCALE) : -3.0e38f;
            sv[8 + q] = (k1 <= qi) ? rbf(rbf(sx1[q]) * SCALE) : -3.0e38f;
        }
        float mx = sv[0];
#pragma unroll
        for (int q = 1; q < 12; ++q) mx = fmaxf(mx, sv[q]);
        mx = xmax16(mx); mx = xmax32(mx);
        float sum = 0.f;
#pragma unroll
        for (int q = 0; q < 12; ++q) { sv[q] = __expf(sv[q] - mx); sum += sv[q]; }
        sum = xsum16(sum); sum = xsum32(sum);
        float inv = 1.f / sum;
        px[qt] = (u32x4){pack2(sv[4] * inv, sv[5] * inv), pack2(sv[6] * inv, sv[7] * inv), pack2(sv[8] * inv, sv[9] * inv), pack2(sv[10] * inv, sv[11] * inv)};
        pm[qt] = (u32x4){pack2(sv[0] * inv, sv[1] * inv), pack2(sv[2] * inv, sv[3] * inv), 0u, 0u};
    }
#pragma unroll
    for (int et = 0; et < 8; ++et) {
        u32x4 ax = *(const u32x4*)(VF + et * 512 + lane * 8);
        u32x2 m0 = {0u, 0u};
        if (fg < 2) m0 = *(const u32x2*)(ws.vmT_() + ((size_t)(s * NH + h) * DH + et * 16 + fr) * 8 + fg * 4);
        u32x4 am = {m0[0], m0[1], 0u, 0u};
#pragma unroll
        for (int qt = 0; qt < 2; ++qt) {
            f32x4 o = {0.f, 0.f, 0.f, 0.f};
            o = __builtin_amdgcn_mfma_f32_16x16x32_bf16(__builtin_bit_cast(bf16x8, ax), __builtin_bit_cast(bf16x8, px[qt]), o, 0, 0, 0);
            o = __builtin_amdgcn_mfma_f32_16x16x32_bf16(__builtin_bit_cast(bf16x8, am), __builtin_bit_cast(bf16x8, pm[qt]), o, 0, 0, 0);
            u32x2 ov = {pack2(o[0], o[1]), pack2(o[2], o[3])};
            *(u32x2*)(sO + (qt * 16 + fr) * 1040 + (w * DH + et * 16 + fg * 4) * 2) = ov;
        }
    }
    __syncthreads();
    f32x4 acc[8][2];
#pragma unroll
    for (int t = 0; t < 8; ++t) { acc[t][0] = (f32x4){0.f, 0.f, 0.f, 0.f}; acc[t][1] = acc[t][0]; }
    const u16* wp = ws.WoF_() + ((size_t)(w * 8 * 32 + hg * 4) * 4) * 512 + lane * 8;
    {
        char* ring = lds + 33280 + w * 24576;
        const rsrc_t rW2 = mk_rsrc(ws.base);
        const unsigned wbase = (unsigned)OFF_WoF + (unsigned)((w * 8 * 32 + hg * 4) * 4) * 1024u;
        auto issue = [&](int ks) {
#pragma unroll
            for (int t = 0; t < 8; ++t)
                __builtin_amdgcn_raw_ptr_buffer_load_lds(rW2, (LAS void*)(ring + ((ks % 3) * 8 + t) * 1024), 16, lane * 16, wbase + (unsigned)(t * 128 + ks) * 1024u, 0, 0);
        };
        issue(0); issue(1); issue(2);
#pragma unroll
        for (int ks = 0; ks < 16; ++ks) {
            if (ks <= 13) asm volatile("s_waitcnt vmcnt(16)" ::: "memory");
            else if (ks == 14) asm volatile("s_waitcnt vmcnt(8)" ::: "memory");
            else asm volatile("s_waitcnt vmcnt(0)" ::: "memory");
            bf16x8 b0 = *(const bf16x8*)(sO + fr * 1040 + (ks * 32 + fg * 8) * 2);
            bf16x8 b1 = *(const bf16x8*)(sO + (16 + fr) * 1040 + (ks * 32 + fg * 8) * 2);
            bf16x8 a[8];
#pragma unroll
            for (int t = 0; t < 8; ++t) a[t] = *(const bf16x8*)(ring + ((ks % 3) * 8 + t) * 1024 + lane * 16);
            if (ks + 3 < 16) {
                asm volatile("s_waitcnt lgkmcnt(0)" ::: "memory");
                issue(ks + 3);
            }
#pragma unroll
            for (int t = 0; t < 8; ++t) {
                acc[t][0] = __builtin_amdgcn_mfma_f32_16x16x32_bf16(a[t], b0, acc[t][0], 0, 0, 0);
                acc[t][1] = __builtin_amdgcn_mfma_f32_16x16x32_bf16(a[t], b1, acc[t][1], 0, 0, 0);
            }
        }
    }
    const rsrc_t rW = mk_rsrc(ws.base);
#pragma unroll
    for (int t = 0; t < 8; ++t)
#pragma unroll
        for (int qt = 0; qt < 2; ++qt) {
            u32x4 o = {__float_as_uint(acc[t][qt][0]), __float_as_uint(acc[t][qt][1]), __float_as_uint(acc[t][qt][2]), __float_as_uint(acc[t][qt][3])};
            unsigned off = (unsigned)((((s * 8 + hg) * 32 + qt * 16 + fr) * 512 + w * 128 + t * 16 + fg * 4) * 4);
            bst16_sc1(rW, off, (0) + (unsigned)OFF_part, o);
        }
}

__device__ __forceinline__ void reduce_task(int s, int r0, const u16* __restrict__ x, u16* __restrict__ out, const WS& ws, const int tidx) {
    const int i = tidx;
    {
        int row = r0 + (i >> 7), c = (i & 127) * 4;
        f32x4 t = {0.f, 0.f, 0.f, 0.f};
        f32x4 pv[8];
#pragma unroll
        for (int hg = 0; hg < 8; ++hg) pv[hg] = *(const f32x4*)(ws.part_() + ((size_t)(s * 8 + hg) * 32 + row) * 512 + c);
#pragma unroll
        for (int hg = 0; hg < 8; ++hg) t += pv[hg];
        int tok = s * SEG + row;
        u32x2 xv = *(const u32x2*)(x + (size_t)tok * DM + c);
        u32x2 gv = *(const u32x2*)(ws.g_() + (size_t)tok * DM + c);
        float xs[4] = {bf2f(xv[0] & 0xffff), bf2f(xv[0] >> 16), bf2f(xv[1] & 0xffff), bf2f(xv[1] >> 16)};
        float gs[4] = {bf2f(gv[0] & 0xffff), bf2f(gv[0] >> 16), bf2f(gv[1] & 0xffff), bf2f(gv[1] >> 16)};
        float r[4];
#pragma unroll
        for (int q = 0; q < 4; ++q) r[q] = xs[q] + rbf(rbf(t[q]) * gs[q]);
        u32x2 o = {pack2(r[0], r[1]), pack2(r[2], r[3])};
        *(u32x2*)(out + (size_t)tok * DM + c) = o;
    }
}

#define NWORK 224
#define T_KV0 64
#define T_Z 64
#define T_KV 960
#define T_H 176
#define T_Q 512
#define T_G 64
#define T_A 512
#define T_TOTAL (T_KV0 + T_Z + T_KV + T_H + T_Q + T_G + T_A)
#define LDS_BYTES 162816

__global__ void __launch_bounds__(256, 1) mega_k(const u16* __restrict__ x, const u16* __restrict__ mem0, const u16* __restrict__ b1, const u16* __restrict__ b2,
                                                 u16* __restrict__ out, WS ws) {
    extern __shared__ __attribute__((aligned(16))) char smem[];
    if (blockIdx.x < NH) {
        chain_body(mem0, ws, smem, blockIdx.x);
        return;
    }
    const int tid = threadIdx.x, lane = tid & 63, w = tid >> 6;
    for (int task = blockIdx.x - NH; task < T_TOTAL; task += NWORK) {
        int t = task;
        int tidx = threadIdx.x;
        asm volatile("" : "+v"(tidx));
        if (t < T_KV0 + T_Z + T_KV) {
            if (t >= T_KV0 && t < T_KV0 + T_Z) {
                const int z = t - T_KV0;
                const rsrc_t rW = mk_rsrc(ws.base);
                u32x4 zz = {0u, 0u, 0u, 0u};
#pragma unroll 8
                for (int i = 0; i < 128; ++i) bst16_sc1(rW, (unsigned)tidx * 16, (unsigned)OFF_exA + (unsigned)z * 524288u + (unsigned)i * 4096u, zz);
                publish_done();
                if (tid == 0) __hip_atomic_store(ws.flagZ_() + z, 1u, __ATOMIC_RELAXED, __HIP_MEMORY_SCOPE_AGENT);
                continue;
            }
            if (t >= T_KV0) t -= T_Z;
            int tb = t >> 6, cb = t & 63;
            gemm_tile<EPI_QKV, 16>((unsigned)OFF_xnF, tb, 32 + cb, ws, nullptr, tidx);
            publish_done();
            if (tid == 0) __hip_atomic_store(ws.flagKV_() + tb * 64 + cb, 1u, __ATOMIC_RELAXED, __HIP_MEMORY_SCOPE_AGENT);
            continue;
        }
        t -= T_KV0 + T_Z + T_KV;
        if (t < T_H) {
            int tb = t / 11, cb = t % 11;
            gemm_tile<EPI_H, 16>((unsigned)OFF_yF, tb, cb, ws, b1, tidx);
            publish_done();
            if (tid == 0) __hip_atomic_fetch_add(ws.cntH_() + tb, 1u, __ATOMIC_RELAXED, __HIP_MEMORY_SCOPE_AGENT);
            continue;
        }
        t -= T_H;
        if (t < T_Q) {
            int tb = t >> 5, cb = t & 31;
            gemm_tile<EPI_QKV, 16>((unsigned)OFF_xnF, tb, cb, ws, nullptr, tidx);
            publish_done();
            if (tid == 0) __hip_atomic_store(ws.flagQ_() + tb * 32 + cb, 1u, __ATOMIC_RELAXED, __HIP_MEMORY_SCOPE_AGENT);
            continue;
        }
        t -= T_Q;
        if (t < T_G) {
            int tb = t >> 2, cb = t & 3;
            if (w == 0) wait_flag_ge(ws.cntH_() + tb, 11u);
            acquire_all();
            gemm_tile<EPI_G, 44>((unsigned)OFF_hmidF, tb, cb, ws, b2, tidx);
            publish_done();
            if (tid == 0) __hip_atomic_store(ws.flagG_() + tb * 4 + cb, 1u, __ATOMIC_RELAXED, __HIP_MEMORY_SCOPE_AGENT);
            continue;
        }
        t -= T_G;
        {
            int s = t >> 3, hg = t & 7, tb = s >> 2;
            if (w == 0) {
                const unsigned* f;
                int l = lane & 15, hh = hg * 4 + (l & 3);
                if (l < 4) f = ws.flagKV_() + tb * 64 + hh;
                else if (l < 8) f = ws.flagKV_() + tb * 64 + 32 + hh;
                else if (l < 12) f = ws.flagQ_() + tb * 32 + hh;
                else f = ws.flagM_() + s * NH + hh;
                wait_flag_ge(f, 1u);
            }
            acquire_all();
            attn_task(s, hg, ws, smem, tidx);
            publish_done();
            if (tid == 0) __hip_atomic_fetch_add(ws.cntO_() + s, 1u, __ATOMIC_RELAXED, __HIP_MEMORY_SCOPE_AGENT);
            if (w == 0) { wait_flag_ge(lane < 4 ? ws.flagG_() + tb * 4 + lane : ws.cntO_() + s, lane < 4 ? 1u : 8u); }
            acquire_all();
            reduce_task(s, hg * 4, x, out, ws, tidx);
            reduce_task(s, hg * 4 + 2, x, out, ws, tidx);
            __syncthreads();
        }
    }
}

extern "C" void kernel_launch(void* const* d_in, const int* in_sizes, int n_in, void* d_out, int out_size, void* d_ws, size_t ws_size,
                              hipStream_t stream) {
    const u16* x = (const u16*)d_in[0];
    const u16* mem0 = (const u16*)d_in[1];
    const u16* Wq = (const u16*)d_in[2];
    const u16* Wk = (const u16*)d_in[3];
    const u16* Wv = (const u16*)d_in[4];
    const u16* Wo = (const u16*)d_in[5];
    const u16* w1 = (const u16*)d_in[8];
    const u16* b1 = (const u16*)d_in[9];
    const u16* w2 = (const u16*)d_in[10];
    const u16* b2 = (const u16*)d_in[11];
    u16* out = (u16*)d_out;

    WS ws; ws.base = (char*)d_ws;

    const int nprep = PREP_LN + PREP_TQKV + PREP_TWO + PREP_TW1 + PREP_TW2 + PREP_ZERO;
    prep_k<<<nprep, 256, 0, stream>>>(x, Wq, Wk, Wv, Wo, w1, w2, ws);
    (void)hipFuncSetAttribute((const void*)mega_k, hipFuncAttributeMaxDynamicSharedMemorySize, LDS_BYTES);
    mega_k<<<256, 256, LDS_BYTES, stream>>>(x, mem0, b1, b2, out, ws);
}
```

```cpp
#include <hip/hip_runtime.h>
#include <hip/hip_bf16.h>
#include <stdint.h>

typedef __attribute__((ext_vector_type(8))) short bf16x8;
typedef __attribute__((ext_vector_type(4))) float f32x4;
typedef __attribute__((ext_vector_type(4))) unsigned u32x4;
typedef __attribute__((ext_vector_type(2))) unsigned u32x2;
typedef unsigned short u16;

#define S_TOK 2048
#define DM 512
#define NH 32
#define DH 128
#define HD 4096
#define NMEM 8
#define SEG 32
#define NSEG 64
#define NSTEPS 64
#define FF 1376
#define FFP 1408

__device__ __forceinline__ u16 f2bf(float f) { return __builtin_bit_cast(u16, (__bf16)f); }
__device__ __forceinline__ float bf2f(u16 h) { return __builtin_bit_cast(float, (unsigned)h << 16); }
__device__ __forceinline__ float rbf(float f) { return bf2f(f2bf(f)); }
typedef __attribute__((ext_vector_type(2))) float f32x2;
typedef __attribute__((ext_vector_type(2))) __bf16 bf16x2v;
__device__ __forceinline__ unsigned pack2(float a, float b) { f32x2 v = {a, b}; return __builtin_bit_cast(unsigned, __builtin_convertvector(v, bf16x2v)); }
__device__ __forceinline__ f32x2 rbf2(f32x2 v) { unsigned u = __builtin_bit_cast(unsigned, __builtin_convertvector(v, bf16x2v)); f32x2 r = {__uint_as_float(u << 16), __uint_as_float(u & 0xffff0000u)}; return r; }

constexpr size_t OFF_flags = 0;
constexpr size_t OFF_exB = OFF_flags + (((size_t)65536) + 255) / 256 * 256;
constexpr size_t OFF_exA = OFF_exB + (((size_t)68 * 16384) + 255) / 256 * 256;
constexpr size_t OFF_xnF = OFF_exA + (((size_t)64 * 524288) + 255) / 256 * 256;
constexpr size_t OFF_yF = OFF_xnF + (((size_t)S_TOK * DM * 2) + 255) / 256 * 256;
constexpr size_t OFF_WqkvF = OFF_yF + (((size_t)S_TOK * DM * 2) + 255) / 256 * 256;
constexpr size_t OFF_WoF = OFF_WqkvF + (((size_t)3 * HD * DM * 2) + 255) / 256 * 256;
constexpr size_t OFF_W1F = OFF_WoF + (((size_t)DM * HD * 2) + 255) / 256 * 256;
constexpr size_t OFF_W2F = OFF_W1F + (((size_t)FFP * DM * 2) + 255) / 256 * 256;
constexpr size_t OFF_Q = OFF_W2F + (((size_t)DM * FFP * 2) + 255) / 256 * 256;
constexpr size_t OFF_K = OFF_Q + (((size_t)S_TOK * HD * 2) + 255) / 256 * 256;
constexpr size_t OFF_VT = OFF_K + (((size_t)S_TOK * HD * 2) + 255) / 256 * 256;
constexpr size_t OFF_hmidF = OFF_VT + (((size_t)S_TOK * HD * 2) + 255) / 256 * 256;
constexpr size_t OFF_g = OFF_hmidF + (((size_t)S_TOK * FFP * 2) + 255) / 256 * 256;
constexpr size_t OFF_km = OFF_g + (((size_t)S_TOK * DM * 2) + 255) / 256 * 256;
constexpr size_t OFF_vmT = OFF_km + (((size_t)NSEG * NH * 8 * DH * 2) + 255) / 256 * 256;
constexpr size_t OFF_part = OFF_vmT + (((size_t)NSEG * NH * 8 * DH * 2) + 255) / 256 * 256;
constexpr size_t OFF_dbg = (size_t)240 * 1024 * 1024;
struct WS {
    char* base;
    __host__ __device__ unsigned* flags_() const { return (unsigned*)(base + OFF_flags); }
    __host__ __device__ unsigned* exB_() const { return (unsigned*)(base + OFF_exB); }
    __host__ __device__ unsigned* exA_() const { return (unsigned*)(base + OFF_exA); }
    __host__ __device__ u16* xnF_() const { return (u16*)(base + OFF_xnF); }
    __host__ __device__ u16* yF_() const { return (u16*)(base + OFF_yF); }
    __host__ __device__ u16* WqkvF_() const { return (u16*)(base + OFF_WqkvF); }
    __host__ __device__ u16* WoF_() const { return (u16*)(base + OFF_WoF); }
    __host__ __device__ u16* W1F_() const { return (u16*)(base + OFF_W1F); }
    __host__ __device__ u16* W2F_() const { return (u16*)(base + OFF_W2F); }
    __host__ __device__ u16* Q_() const { return (u16*)(base + OFF_Q); }
    __host__ __device__ u16* K_() const { return (u16*)(base + OFF_K); }
    __host__ __device__ u16* VT_() const { return (u16*)(base + OFF_VT); }
    __host__ __device__ u16* hmidF_() const { return (u16*)(base + OFF_hmidF); }
    __host__ __device__ u16* g_() const { return (u16*)(base + OFF_g); }
    __host__ __device__ u16* km_() const { return (u16*)(base + OFF_km); }
    __host__ __device__ u16* vmT_() const { return (u16*)(base + OFF_vmT); }
    __host__ __device__ float* part_() const { return (float*)(base + OFF_part); }
    __host__ __device__ unsigned* flagKV_() const { return flags_(); }
    __host__ __device__ unsigned* flagQ_() const { return flags_() + 1024; }
    __host__ __device__ unsigned* cntH_() const { return flags_() + 1536; }
    __host__ __device__ unsigned* flagG_() const { return flags_() + 1600; }
    __host__ __device__ unsigned* flagZ_() const { return flags_() + 1792; }
    __host__ __device__ unsigned* flagM_() const { return flags_() + 2048; }
    __host__ __device__ unsigned* cntO_() const { return flags_() + 4096; }
    __host__ __device__ unsigned long long* dbg_() const { return (unsigned long long*)(base + OFF_dbg); }
};

__device__ __forceinline__ float wave_sum(float v) {
#pragma unroll
    for (int m = 32; m >= 1; m >>= 1) v += __shfl_xor(v, m);
    return v;
}
__device__ __forceinline__ void ln8(float (&v)[8]) {
    float s = 0.f;
#pragma unroll
    for (int i = 0; i < 8; ++i) s += v[i];
    s = wave_sum(s);
    float mean = s * (1.f / 512.f);
    float q = 0.f;
#pragma unroll
    for (int i = 0; i < 8; ++i) { float d = v[i] - mean; q += d * d; }
    q = wave_sum(q);
    float var = rbf(q * (1.f / 512.f));
    float mb = rbf(mean);
    float sd = rbf(sqrtf(rbf(var + 1e-5f)));
#pragma unroll
    for (int i = 0; i < 8; ++i) v[i] = rbf(rbf(v[i] - mb) / sd);
}
template <class F>
__device__ __forceinline__ void frag_tile(const u16* __restrict__ src, int R, int C, int bx, int by, u16 (*t)[72], F addr) {
    const int c0 = bx * 64, r0 = by * 64;
#pragma unroll
    for (int i = 0; i < 2; ++i) {
        int idx = threadIdx.x + i * 256, rr = idx >> 3, cc = (idx & 7) * 8;
        int r = r0 + rr, c = c0 + cc;
        u32x4 v = {0u, 0u, 0u, 0u};
        if (r < R && c < C) v = *(const u32x4*)(src + (size_t)r * C + c);
        *(u32x4*)(&t[rr][cc]) = v;
    }
    __syncthreads();
    int l = threadIdx.x & 63, q = threadIdx.x >> 6;
#pragma unroll
    for (int i = 0; i < 2; ++i) {
        int f = q * 2 + i, ksl = f >> 2, ct = f & 3;
        u16 v[8];
#pragma unroll
        for (int j = 0; j < 8; ++j) v[j] = t[ksl * 32 + (l >> 4) * 8 + j][ct * 16 + (l & 15)];
        u32x4 o = {(unsigned)v[0] | ((unsigned)v[1] << 16), (unsigned)v[2] | ((unsigned)v[3] << 16), (unsigned)v[4] | ((unsigned)v[5] << 16), (unsigned)v[6] | ((unsigned)v[7] << 16)};
        *(u32x4*)(addr(ksl, ct) + l * 8) = o;
    }
}
#define PREP_LN 512
#define PREP_TQKV 1536
#define PREP_TWO 512
#define PREP_TW1 176
#define PREP_TW2 176
#define ZERO_BYTES ((size_t)68 * 16384 + 65536)
#define PREP_ZERO ((int)(ZERO_BYTES / 65536))
__global__ void __launch_bounds__(256) prep_k(const u16* __restrict__ x, const u16* __restrict__ Wq, const u16* __restrict__ Wk, const u16* __restrict__ Wv,
                                              const u16* __restrict__ Wo, const u16* __restrict__ w1, const u16* __restrict__ w2, WS ws) {
    __shared__ __attribute__((aligned(16))) u16 t[64][72];
    int b = blockIdx.x;
    if (b < PREP_LN) {
        int row = b * 4 + (threadIdx.x >> 6);
        int lane = threadIdx.x & 63;
        u32x4 r = *(const u32x4*)(x + (size_t)row * DM + lane * 8);
        float v[8];
#pragma unroll
        for (int i = 0; i < 4; ++i) { v[2 * i] = bf2f(r[i] & 0xffff); v[2 * i + 1] = bf2f(r[i] >> 16); }
        ln8(v);
        u32x4 o;
#pragma unroll
        for (int i = 0; i < 4; ++i) o[i] = pack2(v[2 * i], v[2 * i + 1]);
        const size_t fo = ((size_t)((row >> 4) * 16 + (lane >> 2)) * 64 + (lane & 3) * 16 + (row & 15)) * 8;
        *(u32x4*)(ws.xnF_() + fo) = o;
        ln8(v);
#pragma unroll
        for (int i = 0; i < 4; ++i) o[i] = pack2(v[2 * i], v[2 * i + 1]);
        *(u32x4*)(ws.yF_() + fo) = o;
        return;
    }
    b -= PREP_LN;
    if (b < PREP_TQKV) {
        int m = b / 512, r = b % 512;
        const u16* src = m == 0 ? Wq : (m == 1 ? Wk : Wv);
        const int bx = r % 64, by = r / 64;
        frag_tile(src, DM, HD, bx, by, t, [&](int ksl, int ct) {
            int n = bx * 64 + ct * 16, ks = by * 2 + ksl;
            int hh = n >> 7, ww = (n >> 5) & 3, tt = (n >> 4) & 1;
            return ws.WqkvF_() + ((size_t)(((hh * 4 + ww) * 6 + m * 2 + tt) * 16 + ks)) * 512;
        });
        return;
    }
    b -= PREP_TQKV;
    if (b < PREP_TWO) {
        const int bx = b % 8, by = b / 8;
        frag_tile(Wo, HD, DM, bx, by, t, [&](int ksl, int ct) {
            int tile = bx * 4 + ct, k = by * 64 + ksl * 32;
            return ws.WoF_() + ((size_t)((tile * 32 + (k >> 7)) * 4 + ((k >> 5) & 3))) * 512;
        });
        return;
    }
    b -= PREP_TWO;
    if (b < PREP_TW1) {
        const int bx = b % 22, by = b / 22;
        frag_tile(w1, DM, FF, bx, by, t, [&](int ksl, int ct) { return ws.W1F_() + ((size_t)((bx * 4 + ct) * 16 + by * 2 + ksl)) * 512; });
        return;
    }
    b -= PREP_TW1;
    if (b < PREP_TW2) {
        const int bx = b % 8, by = b / 8;
        frag_tile(w2, FF, DM, bx, by, t, [&](int ksl, int ct) { return ws.W2F_() + ((size_t)((bx * 4 + ct) * 44 + by * 2 + ksl)) * 512; });
        return;
    }
    b -= PREP_TW2;
    {
        u32x4 z = {0u, 0u, 0u, 0u};
        u32x4* p = (u32x4*)(ws.base + (size_t)b * 65536) + threadIdx.x;
#pragma unroll
        for (int i = 0; i < 16; ++i) p[i * 256] = z;
    }
}

#define SCALE 0.08838834764831845f
#define MEM_STRIDE 1056
#define QK_STRIDE 288

typedef __amdgpu_buffer_rsrc_t rsrc_t;
__device__ __forceinline__ rsrc_t mk_rsrc(const void* p) { return __builtin_amdgcn_make_buffer_rsrc((void*)p, 0, 0x7fffffff, 0x00020000); }
__device__ __forceinline__ u32x4 bld16(rsrc_t r, unsigned vo, unsigned so) { return __builtin_amdgcn_raw_buffer_load_b128(r, vo, so, 0); }
__device__ __forceinline__ u32x4 bld16_sc1(rsrc_t r, unsigned vo, unsigned so) { return __builtin_amdgcn_raw_buffer_load_b128(r, vo, so, 16); }
__device__ __forceinline__ u32x2 bld8(rsrc_t r, unsigned vo, unsigned so) { return __builtin_amdgcn_raw_buffer_load_b64(r, vo, so, 0); }
__device__ __forceinline__ void bst16_sc1(rsrc_t r, unsigned vo, unsigned so, u32x4 v) { __builtin_amdgcn_raw_buffer_store_b128(v, r, vo, so, 16); }
__device__ __forceinline__ void bst8(rsrc_t r, unsigned vo, unsigned so, u32x2 v) { __builtin_amdgcn_raw_buffer_store_b64(v, r, vo, so, 0); }
__device__ __forceinline__ void bst8_sc1(rsrc_t r, unsigned vo, unsigned so, u32x2 v) { __builtin_amdgcn_raw_buffer_store_b64(v, r, vo, so, 16); }

#define STAMP(i)
#define MFMA0_A(acc, a, b) asm volatile("v_mfma_f32_16x16x32_bf16 %0, %1, %2, 0" : "=&v"(acc) : "a"(a), "v"(b))
#define MFMA_A(acc, a, b) asm volatile("v_mfma_f32_16x16x32_bf16 %0, %1, %2, %0" : "+v"(acc) : "a"(a), "v"(b))
#define MFMA0_V(acc, a, b) asm volatile("v_mfma_f32_16x16x32_bf16 %0, %1, %2, 0" : "=&v"(acc) : "v"(a), "v"(b))
#define MFMA_V(acc, a, b) asm volatile("v_mfma_f32_16x16x32_bf16 %0, %1, %2, %0" : "+v"(acc) : "v"(a), "v"(b))
#define BARRIER() asm volatile("s_waitcnt lgkmcnt(0)\n\ts_barrier" ::: "memory")
#define LAS __attribute__((address_space(3)))

__device__ __forceinline__ float xmax16(float x) {
    unsigned u = __float_as_uint(x);
    auto r = __builtin_amdgcn_permlane16_swap(u, u, false, false);
    return fmaxf(__uint_as_float(r[0]), __uint_as_float(r[1]));
}
__device__ __forceinline__ float xmax32(float x) {
    unsigned u = __float_as_uint(x);
    auto r = __builtin_amdgcn_permlane32_swap(u, u, false, false);
    return fmaxf(__uint_as_float(r[0]), __uint_as_float(r[1]));
}
__device__ __forceinline__ float xsum16(float x) {
    unsigned u = __float_as_uint(x);
    auto r = __builtin_amdgcn_permlane16_swap(u, u, false, false);
    return __uint_as_float(r[0]) + __uint_as_float(r[1]);
}
__device__ __forceinline__ float xsum32(float x) {
    unsigned u = __float_as_uint(x);
    auto r = __builtin_amdgcn_permlane32_swap(u, u, false, false);
    return __uint_as_float(r[0]) + __uint_as_float(r[1]);
}
__device__ __forceinline__ unsigned ror8u(unsigned x) { return __builtin_amdgcn_update_dpp(0u, x, 0x128, 0xf, 0xf, false); }
__device__ __forceinline__ float ror8(float x) { return __uint_as_float(ror8u(__float_as_uint(x))); }
__device__ __forceinline__ float xsum8(float x) {
    return x + __uint_as_float(__builtin_amdgcn_update_dpp(0u, __float_as_uint(x), 0x128, 0xf, 0xf, false));
}

#define DELAY_A 12
#define DELAY_B 12
#define EXA_STEP 524288u
#define EXB_STEP 16384u
__device__ __forceinline__ void chain_body(const u16* __restrict__ mem0, const WS& ws, char* smem, const int h) {
    char* sW = smem;
    char* sKV = smem + 131072;
    char* sMem = smem + 147456;
    char* sQK = sMem + 8 * MEM_STRIDE;
    char* sO = sQK + 16 * QK_STRIDE;
    const int tid = threadIdx.x, lane = tid & 63;
    const int w = __builtin_amdgcn_readfirstlane(tid >> 6);
    const int fr = lane & 15, fg = lane >> 4, fr8 = lane & 7;
    const unsigned l16 = lane * 16;
    char* lw = sW + w * 32768 + l16;

    bf16x8 wqk[3][16], wv[24], wo[24];
#pragma unroll
    for (int t = 0; t < 6; ++t) {
        const u16* p = ws.WqkvF_() + ((size_t)((h * 4 + w) * 6 + t) * 16) * 512 + lane * 8;
#pragma unroll
        for (int ks = 0; ks < 16; ++ks) {
            bf16x8 v = *(const bf16x8*)(p + ks * 512);
            if (t < 3) wqk[t][ks] = v;
            else if (t == 3) *(bf16x8*)(lw + ks * 1024) = v;
            else if (t == 4) wv[ks] = v;
            else if (ks < 8) wv[16 + ks] = v;
            else *(bf16x8*)(lw + (16 + ks - 8) * 1024) = v;
        }
    }
#pragma unroll
    for (int t = 0; t < 8; ++t)
#pragma unroll
        for (int ks = 0; ks < 4; ++ks) {
            bf16x8 v = *(const bf16x8*)(ws.WoF_() + ((size_t)(((w * 8 + t) * 32 + h) * 4 + ks)) * 512 + lane * 8);
            if (t < 6) wo[t * 4 + ks] = v;
            else *(bf16x8*)(lw + (24 + (t - 6) * 4 + ks) * 1024) = v;
        }
    for (int i = tid; i < 8 * 64; i += 256) {
        int row = i >> 6, c = (i & 63) * 8;
        *(u32x4*)(sMem + row * MEM_STRIDE + c * 2) = *(const u32x4*)(mem0 + row * DM + c);
    }
    const rsrc_t rW = mk_rsrc(ws.base);
    const unsigned stA_vo = (unsigned)fg * 128 + (fr & 7) * 16 + (fr >> 3) * 16384;
    const unsigned rdA_vo = (unsigned)(lane >> 3) * 512 + (lane & 7) * 16;
    const char* memrd = sMem + fr8 * MEM_STRIDE + fg * 16;
    const char* qrd = sQK + fr8 * QK_STRIDE + fg * 16;
    const char* ord_ = sO + fr8 * QK_STRIDE + fg * 16;
    char* memwr = sMem + (lane & 7) * MEM_STRIDE + ((w * 2 + (lane >> 5)) * 16 + ((lane >> 3) & 3) * 4) * 2;

    for (int s = 0; s < NSTEPS; ++s) {
        STAMP(0);
        if ((s & 3) == 0) {
            const unsigned* f = (lane & 4) ? (ws.flagZ_() + s + (lane & 3)) : (ws.flagKV_() + (s >> 2) * 64 + (lane & 1) * 32 + h);
            for (unsigned spin = 0; spin < (1u << 22); ++spin) {
                unsigned v = __hip_atomic_load(f, __ATOMIC_RELAXED, __HIP_MEMORY_SCOPE_AGENT);
                if (__all(v != 0)) break;
                __builtin_amdgcn_s_sleep(2);
            }
        }
        {
            const u16* gk = ws.K_() + (size_t)(s * NH + h) * 4096 + w * 1024 + lane * 8;
            const u16* gv = ws.VT_() + (size_t)(s * NH + h) * 4096 + w * 1024 + lane * 8;
            __builtin_amdgcn_global_load_lds((const unsigned*)gk, (LAS unsigned*)(sKV + w * 2048), 16, 0, 16);
            __builtin_amdgcn_global_load_lds((const unsigned*)(gk + 512), (LAS unsigned*)(sKV + w * 2048 + 1024), 16, 0, 16);
            __builtin_amdgcn_global_load_lds((const unsigned*)gv, (LAS unsigned*)(sKV + 8192 + w * 2048), 16, 0, 16);
            __builtin_amdgcn_global_load_lds((const unsigned*)(gv + 512), (LAS unsigned*)(sKV + 8192 + w * 2048 + 1024), 16, 0, 16);
        }
        if (s == 0) {
            asm volatile("s_waitcnt vmcnt(0)" ::: "memory");
        } else {
#pragma unroll 1
            for (int d = 0; d < DELAY_B; ++d) __builtin_amdgcn_s_sleep(1);
            u32x4 vv[4];
            for (unsigned spin = 0;; ++spin) {
                if (spin > (1u << 20)) break;
                asm volatile("" ::: "memory");
#pragma unroll
                for (int i = 0; i < 4; ++i) vv[i] = bld16_sc1(rW, l16, ((unsigned)(i * 4096 + w * 1024) + (unsigned)s * EXB_STEP) + (unsigned)OFF_exB);
                unsigned am_ = 1u;
#pragma unroll
                for (int i = 0; i < 4; ++i) am_ &= (vv[i][0] & vv[i][1]) & (vv[i][2] & vv[i][3]);
                if (__all(am_ != 0)) break;
            }
#pragma unroll
            for (int i = 0; i < 4; ++i) {
                u32x2 o = {(vv[i][0] >> 16) | (vv[i][1] & 0xffff0000u), (vv[i][2] >> 16) | (vv[i][3] & 0xffff0000u)};
                *(u32x2*)(memwr + i * 256) = o;
            }
        }
        bf16x8 kw[2], vw[2];
#pragma unroll
        for (int i = 0; i < 2; ++i) kw[i] = *(const bf16x8*)(lw + i * 1024);
        vw[0] = *(const bf16x8*)(lw + 16 * 1024); vw[1] = *(const bf16x8*)(lw + 17 * 1024);
        STAMP(1);
        BARRIER();
        STAMP(2);
        f32x4 acc[4], av[2];
        {
            bf16x8 mb[3];
#pragma unroll
            for (int i = 0; i < 3; ++i) mb[i] = *(const bf16x8*)(memrd + i * 64);
#pragma unroll
            for (int ks = 0; ks < 16; ++ks) {
                bf16x8 m = mb[ks % 3];
                bf16x8 k3 = kw[ks & 1];
                bf16x8 v5 = (ks < 8) ? wv[16 + ks] : vw[ks & 1];
                if (ks == 0) {
                    MFMA0_A(acc[0], wqk[0][ks], m); MFMA0_A(acc[1], wqk[1][ks], m); MFMA0_A(acc[2], wqk[2][ks], m); MFMA0_V(acc[3], k3, m);
                    MFMA0_V(av[0], m, wv[ks]); MFMA0_V(av[1], m, v5);
                } else {
                    MFMA_A(acc[0], wqk[0][ks], m); MFMA_A(acc[1], wqk[1][ks], m); MFMA_A(acc[2], wqk[2][ks], m); MFMA_V(acc[3], k3, m);
                    MFMA_V(av[0], m, wv[ks]); MFMA_V(av[1], m, v5);
                }
                if (ks + 3 < 16) mb[ks % 3] = *(const bf16x8*)(memrd + (ks + 3) * 64);
                if (ks + 2 < 16) kw[ks & 1] = *(const bf16x8*)(lw + (ks + 2) * 1024);
                if (ks >= 8 && ks + 2 < 16) vw[ks & 1] = *(const bf16x8*)(lw + (16 + ks + 2 - 8) * 1024);
            }
        }
        STAMP(3);
        asm volatile("s_nop 7" : "+v"(acc[0]), "+v"(acc[1]), "+v"(acc[2]), "+v"(acc[3]), "+v"(av[0]), "+v"(av[1]));
        u32x2 o[4];
        {
#pragma unroll
            for (int t = 0; t < 4; ++t) o[t] = (u32x2){pack2(acc[t][0], acc[t][1]), pack2(acc[t][2], acc[t][3])};
            if (fr < 8) {
#pragma unroll
                for (int t = 0; t < 4; ++t) {
                    int row = (t >> 1) * 8 + fr;
                    int c = w * 32 + (t & 1) * 16 + fg * 4;
                    *(u32x2*)(sQK + row * QK_STRIDE + c * 2) = o[t];
                }
            }
        }
        u32x4 am[2], ax[2];
#pragma unroll
        for (int t = 0; t < 2; ++t) {
            am[t] = (u32x4){pack2(av[t][0], av[t][1]), pack2(av[t][2], av[t][3]), 0u, 0u};
            if (fg >= 2) { am[t][0] = 0u; am[t][1] = 0u; }
        }
        bf16x8 kx[2][4];
#pragma unroll
        for (int ks = 0; ks < 4; ++ks) {
            kx[0][ks] = *(const bf16x8*)(sKV + ks * 1024 + l16);
            kx[1][ks] = *(const bf16x8*)(sKV + 4096 + ks * 1024 + l16);
        }
        ax[0] = *(const u32x4*)(sKV + 8192 + (w * 2) * 1024 + l16);
        ax[1] = *(const u32x4*)(sKV + 8192 + (w * 2 + 1) * 1024 + l16);
        STAMP(4);
        BARRIER();
        STAMP(5);
        f32x4 sm, sx0, sx1;
        {
            bf16x8 qf[4], kf[4];
#pragma unroll
            for (int ks = 0; ks < 4; ++ks) {
                qf[ks] = *(const bf16x8*)(qrd + ks * 64);
                kf[ks] = *(const bf16x8*)(qrd + 8 * QK_STRIDE + ks * 64);
            }
            MFMA0_V(sx0, kx[0][0], qf[0]); MFMA0_V(sx1, kx[1][0], qf[0]); MFMA0_V(sm, kf[0], qf[0]);
#pragma unroll
            for (int ks = 1; ks < 4; ++ks) { MFMA_V(sx0, kx[0][ks], qf[ks]); MFMA_V(sx1, kx[1][ks], qf[ks]); MFMA_V(sm, kf[ks], qf[ks]); }
        }
        STAMP(10);
        if (fr < 8) {
                bst8_sc1(rW, (unsigned)fr * 256 + fg * 8, ((unsigned)((s * NH + h) * 2048 + (w * 32) * 2)) + (unsigned)OFF_km, o[2]);
                bst8_sc1(rW, (unsigned)fr * 256 + fg * 8, ((unsigned)((s * NH + h) * 2048 + (w * 32 + 16) * 2)) + (unsigned)OFF_km, o[3]);
        }
        if (fg < 2) {
            bst8_sc1(rW, (unsigned)fr * 16 + fg * 8, ((unsigned)((s * NH + h) * 2048 + (w * 32) * 16)) + (unsigned)OFF_vmT, (u32x2){am[0][0], am[0][1]});
            bst8_sc1(rW, (unsigned)fr * 16 + fg * 8, ((unsigned)((s * NH + h) * 2048 + (w * 32 + 16) * 16)) + (unsigned)OFF_vmT, (u32x2){am[1][0], am[1][1]});
        }
        bf16x8 wf[8];
#pragma unroll
        for (int i = 0; i < 8; ++i) wf[i] = *(const bf16x8*)(lw + (24 + i) * 1024);
        asm volatile("s_nop 7" : "+v"(sm), "+v"(sx0), "+v"(sx1));
        const bool hi = fr >= 8;
        const f32x2 sc2 = {SCALE, SCALE};
        f32x2 p0 = {hi ? sx1[0] : sx0[0], hi ? sx1[1] : sx0[1]};
        f32x2 p1 = {hi ? sx1[2] : sx0[2], hi ? sx1[3] : sx0[3]};
        f32x2 p2 = {hi ? sm[2] : sm[0], hi ? sm[3] : sm[1]};
        p0 = rbf2(rbf2(p0) * sc2);
        p1 = rbf2(rbf2(p1) * sc2);
        p2 = rbf2(rbf2(p2) * sc2);
        if (fg >= 2) p2 = (f32x2){-3.0e38f, -3.0e38f};
        float mx = fmaxf(fmaxf(fmaxf(p0[0], p0[1]), fmaxf(p1[0], p1[1])), fmaxf(p2[0], p2[1]));
        mx = fmaxf(mx, ror8(mx));
        mx = xmax16(mx);
        mx = xmax32(mx);
        const float L2E = 1.4426950408889634f;
        const f32x2 l2 = {L2E, L2E};
        const f32x2 mxl = {mx, mx};
        p0 = (p0 - mxl) * l2; p1 = (p1 - mxl) * l2; p2 = (p2 - mxl) * l2;
        p0 = (f32x2){__builtin_amdgcn_exp2f(p0[0]), __builtin_amdgcn_exp2f(p0[1])};
        p1 = (f32x2){__builtin_amdgcn_exp2f(p1[0]), __builtin_amdgcn_exp2f(p1[1])};
        p2 = (f32x2){__builtin_amdgcn_exp2f(p2[0]), __builtin_amdgcn_exp2f(p2[1])};
        f32x2 s2 = (p0 + p1) + p2;
        float sum = s2[0] + s2[1];
        sum = sum + ror8(sum);
        sum = xsum16(sum);
        sum = xsum32(sum);
        float inv = __builtin_amdgcn_rcpf(sum);
        const f32x2 iv = {inv, inv};
        p0 *= iv; p1 *= iv; p2 *= iv;
        unsigned d0 = pack2(p0[0], p0[1]), d1 = pack2(p1[0], p1[1]), d2 = pack2(p2[0], p2[1]);
        unsigned e0 = ror8u(d0), e1 = ror8u(d1), e2 = ror8u(d2);
        u32x4 px = {d0, d1, e0, e1};
        u32x4 pm = {d2, e2, 0u, 0u};
        STAMP(12);
        f32x4 o0, o1;
        asm volatile("s_nop 1" : "+v"(px), "+v"(pm), "+v"(am[0]), "+v"(am[1]), "+v"(ax[0]), "+v"(ax[1]));
        MFMA0_V(o0, ax[0], px); MFMA0_V(o1, ax[1], px);
        MFMA_V(o0, am[0], pm); MFMA_V(o1, am[1], pm);
        asm volatile("s_nop 7" : "+v"(o0), "+v"(o1));
        if (fr < 8) {
            *(u32x2*)(sO + fr * QK_STRIDE + (w * 32 + fg * 4) * 2) = (u32x2){pack2(o0[0], o0[1]), pack2(o0[2], o0[3])};
            *(u32x2*)(sO + fr * QK_STRIDE + (w * 32 + 16 + fg * 4) * 2) = (u32x2){pack2(o1[0], o1[1]), pack2(o1[2], o1[3])};
        }
        STAMP(6);
        BARRIER();
        STAMP(7);
        bf16x8 of[4];
#pragma unroll
        for (int ks = 0; ks < 4; ++ks) of[ks] = *(const bf16x8*)(ord_ + ks * 64);
        {
            f32x4 a[4];
#pragma unroll
            for (int p = 0; p <= 4; ++p) {
                if (p < 4) {
#pragma unroll
                    for (int u = 0; u < 2; ++u) {
                        const int t = 2 * p + u;
                        f32x4& ac = a[(p & 1) * 2 + u];
#pragma unroll
                        for (int ks = 0; ks < 4; ++ks) {
                            if (t < 4) { if (ks == 0) MFMA0_A(ac, wo[t * 4 + ks], of[ks]); else MFMA_A(ac, wo[t * 4 + ks], of[ks]); }
                            else {
                                bf16x8 f = (t < 6) ? wo[t * 4 + ks] : wf[(t - 6) * 4 + ks];
                                if (ks == 0) MFMA0_V(ac, f, of[ks]); else MFMA_V(ac, f, of[ks]);
                            }
                        }
                    }
                }
                if (p >= 1) {
                    const int q0 = ((p - 1) & 1) * 2;
                    if (p == 4) asm volatile("s_nop 7" : "+v"(a[q0]), "+v"(a[q0 + 1]));
                    u32x4 o;
#pragma unroll
                    for (int q = 0; q < 4; ++q) o[q] = __float_as_uint(fr < 8 ? a[q0][q] : a[q0 + 1][q]) | 1u;
                    bst16_sc1(rW, stA_vo, ((unsigned)(((w * 8 + 2 * (p - 1)) * NH + h) * 512) + (unsigned)s * EXA_STEP) + (unsigned)OFF_exA, o);
                }
            }
        }
        STAMP(8);
        if (s > 0 && tid == 192) __hip_atomic_store(ws.flagM_() + (s - 1) * NH + h, 1u, __ATOMIC_RELAXED, __HIP_MEMORY_SCOPE_AGENT);
        {
#pragma unroll 1
            for (int d = 0; d < DELAY_A; ++d) __builtin_amdgcn_s_sleep(1);
            float tot[4];
            for (unsigned spin = 0;; ++spin) {
                if (spin > (1u << 20)) break;
                asm volatile("" ::: "memory");
                u32x4 v[4];
#pragma unroll
                for (int i = 0; i < 4; ++i) v[i] = bld16_sc1(rW, rdA_vo, ((unsigned)(h * NH * 512 + i * 4096 + w * 128) + (unsigned)s * EXA_STEP) + (unsigned)OFF_exA);
                bool ok = true;
#pragma unroll
                for (int q = 0; q < 4; ++q) {
                    ok &= ((v[0][q] & v[1][q] & v[2][q] & v[3][q] & 1u) != 0);
                    tot[q] = (__uint_as_float(v[0][q]) + __uint_as_float(v[1][q])) + (__uint_as_float(v[2][q]) + __uint_as_float(v[3][q]));
                }
                if (__all(ok)) break;
            }
#pragma unroll
            for (int q = 0; q < 4; ++q) {
                tot[q] = xsum8(tot[q]);
                tot[q] = xsum16(tot[q]);
                tot[q] = xsum32(tot[q]);
            }
            STAMP(9);
            if (lane < 8) {
                u32x4 o;
#pragma unroll
                for (int q = 0; q < 4; ++q) o[q] = ((unsigned)f2bf(tot[q]) << 16) | 1u;
                bst16_sc1(rW, l16, ((unsigned)(h * 512 + w * 128) + (unsigned)(s + 1) * EXB_STEP) + (unsigned)OFF_exB, o);
            }
        }
    }
    asm volatile("s_waitcnt vmcnt(0)" ::: "memory");
    __syncthreads();
    if (tid == 0) __hip_atomic_store(ws.flagM_() + (NSTEPS - 1) * NH + h, 1u, __ATOMIC_RELAXED, __HIP_MEMORY_SCOPE_AGENT);
}

enum { EPI_QKV = 0, EPI_H = 1, EPI_G = 2 };
__device__ __forceinline__ float gelu_tanh(float v) {
    float u = 0.7978845608028654f * (v + 0.044715f * v * v * v);
    return 0.5f * v * (1.f + tanhf(u));
}
__device__ __forceinline__ void wait_flag_ge(const unsigned* f, unsigned want) {
    for (unsigned spin = 0; spin < (1u << 22); ++spin) {
        unsigned v = __hip_atomic_load(f, __ATOMIC_RELAXED, __HIP_MEMORY_SCOPE_AGENT);
        if (__all(v >= want)) break;
        __builtin_amdgcn_s_sleep(64);
    }
}
__device__ __forceinline__ void acquire_all() {
    __builtin_amdgcn_fence(__ATOMIC_ACQUIRE, "agent");
    asm volatile("s_waitcnt vmcnt(0)" ::: "memory");
    __syncthreads();
}
__device__ __forceinline__ void publish_done() {
    asm volatile("s_waitcnt vmcnt(0)" ::: "memory");
    __syncthreads();
}

template <int EPI, int NKS>
__device__ __forceinline__ void gemm_tile(unsigned offA, int tb, int cb, const WS& ws, const u16* __restrict__ bias, const int tidx) {
    const int lane = tidx & 63, w = __builtin_amdgcn_readfirstlane(tidx >> 6);
    const int wr = w >> 1, wc = w & 1;
    const int fr = lane & 15, fg = lane >> 4;
    const rsrc_t rW = mk_rsrc(ws.base);
    const unsigned l16 = lane * 16;
    const bool vtile = (EPI == EPI_QKV) && (cb >= 64);
    unsigned ao[4], bo[4];
#pragma unroll
    for (int i = 0; i < 4; ++i) {
        ao[i] = offA + (unsigned)((tb * 8 + wr * 4 + i) * NKS) * 1024u;
        int ct = cb * 8 + wc * 4 + i;
        if (EPI == EPI_QKV) {
            int m = ct >> 8, r = ct & 255;
            bo[i] = (unsigned)OFF_WqkvF + (unsigned)((((r >> 3) * 4 + ((r >> 1) & 3)) * 6 + m * 2 + (r & 1)) * 16) * 1024u;
        } else if (EPI == EPI_H) bo[i] = (unsigned)OFF_W1F + (unsigned)(ct * 16) * 1024u;
        else bo[i] = (unsigned)OFF_W2F + (unsigned)(ct * 44) * 1024u;
    }
    f32x4 acc[4][4];
#pragma unroll
    for (int i = 0; i < 4; ++i)
#pragma unroll
        for (int j = 0; j < 4; ++j) acc[i][j] = (f32x4){0.f, 0.f, 0.f, 0.f};
    constexpr int D = 4;
    u32x4 fa[D][4], fb[D][4];
#pragma unroll
    for (int d = 0; d < D; ++d)
#pragma unroll
        for (int i = 0; i < 4; ++i) { fa[d][i] = bld16(rW, l16, ao[i] + d * 1024u); fb[d][i] = bld16(rW, l16, bo[i] + d * 1024u); }
    for (int k0 = 0; k0 < NKS; k0 += D) {
#pragma unroll
        for (int d = 0; d < D; ++d) {
            u32x4 a[4], bq[4];
#pragma unroll
            for (int i = 0; i < 4; ++i) { a[i] = fa[d][i]; bq[i] = fb[d][i]; }
            const int kn = k0 + d + D;
            if (kn < NKS) {
#pragma unroll
                for (int i = 0; i < 4; ++i) { fa[d][i] = bld16(rW, l16, ao[i] + (unsigned)kn * 1024u); fb[d][i] = bld16(rW, l16, bo[i] + (unsigned)kn * 1024u); }
            }
            if (vtile) {
#pragma unroll
                for (int i = 0; i < 4; ++i)
#pragma unroll
                    for (int j = 0; j < 4; ++j) acc[i][j] = __builtin_amdgcn_mfma_f32_16x16x32_bf16(__builtin_bit_cast(bf16x8, a[i]), __builtin_bit_cast(bf16x8, bq[j]), acc[i][j], 0, 0, 0);
            } else {
#pragma unroll
                for (int i = 0; i < 4; ++i)
#pragma unroll
                    for (int j = 0; j < 4; ++j) acc[i][j] = __builtin_amdgcn_mfma_f32_16x16x32_bf16(__builtin_bit_cast(bf16x8, bq[j]), __builtin_bit_cast(bf16x8, a[i]), acc[i][j], 0, 0, 0);
            }
        }
    }
    const int brow = tb * 128, bcol = cb * 128;
#pragma unroll
    for (int i = 0; i < 4; ++i)
#pragma unroll
        for (int j = 0; j < 4; ++j) {
            f32x4 a = acc[i][j];
            if (vtile) {
                int col = bcol + wc * 64 + j * 16 + fr - 2 * HD;
                int tok = brow + wr * 64 + i * 16 + fg * 4;
                int seg = tok >> 5, tt = (tok >> 4) & 1, hh = col >> 7, et = (col >> 4) & 7;
                u32x2 o = {pack2(a[0], a[1]), pack2(a[2], a[3])};
                bst8_sc1(rW, (unsigned)((((seg * NH + hh) * 8 + et) * 512 + (fg * 16 + fr) * 8 + tt * 4) * 2), (unsigned)OFF_VT, o);
            } else {
                int tok = brow + wr * 64 + i * 16 + fr;
                int col = bcol + wc * 64 + j * 16 + fg * 4;
                if (EPI == EPI_QKV) {
                    int cc = col & (HD - 1);
                    int seg = tok >> 5, tt = (tok >> 4) & 1, hh = cc >> 7, e = cc & 127;
                    u32x2 o = {pack2(a[0], a[1]), pack2(a[2], a[3])};
                    unsigned off = (unsigned)((((seg * NH + hh) * 8 + tt * 4 + (e >> 5)) * 512 + (((e >> 3) & 3) * 16 + (tok & 15)) * 8 + (e & 4)) * 2);
                    bst8_sc1(rW, off, (col >= HD) ? (unsigned)OFF_K : (unsigned)OFF_Q, o);
                } else if (EPI == EPI_H) {
                    float r[4];
#pragma unroll
                    for (int q = 0; q < 4; ++q) {
                        float b = (col + q < FF) ? bf2f(bias[col + q]) : 0.f;
                        float v = rbf(rbf(a[q]) + b);
                        r[q] = (col + q < FF) ? gelu_tanh(v) : 0.f;
                    }
                    u32x2 o = {pack2(r[0], r[1]), pack2(r[2], r[3])};
                    unsigned off = (unsigned)((((tok >> 4) * 44 + (col >> 5)) * 64 + ((col >> 3) & 3) * 16 + (tok & 15)) * 16 + (col & 4) * 2);
                    bst8_sc1(rW, off, (unsigned)OFF_hmidF, o);
                } else {
                    float r[4];
#pragma unroll
                    for (int q = 0; q < 4; ++q) {
                        float v = rbf(rbf(a[q]) + bf2f(bias[col + q]));
                        r[q] = v / (1.f + __expf(-v));
                    }
                    u32x2 o = {pack2(r[0], r[1]), pack2(r[2], r[3])};
                    bst8_sc1(rW, (unsigned)((tok * DM + col) * 2), (unsigned)OFF_g, o);
                }
            }
        }
}

__device__ __forceinline__ u32x2 bld8_sc1(rsrc_t r, unsigned vo, unsigned so) { return __builtin_amdgcn_raw_buffer_load_b64(r, vo, so, 16); }
__device__ __forceinline__ void attn_task(int s, int hg, const WS& ws, char* lds, const int tidx) {
    const int lane = tidx & 63, w = __builtin_amdgcn_readfirstlane(tidx >> 6);
    const int h = hg * 4 + w;
    const int fr = lane & 15, fg = lane >> 4, fr8 = lane & 7;
    char* sO = lds;
    const rsrc_t rW = mk_rsrc(ws.base);
    const unsigned l16 = lane * 16;
    const unsigned sh = (unsigned)(s * NH + h) * 8192u;
    char* ring = lds + 33280 + w * 24576;
    const unsigned wbase = (unsigned)OFF_WoF + (unsigned)((w * 8 * 32 + hg * 4) * 4) * 1024u;
    auto issue = [&](int ks) {
#pragma unroll
        for (int t = 0; t < 8; ++t)
            __builtin_amdgcn_raw_ptr_buffer_load_lds(rW, (LAS void*)(ring + ((ks % 3) * 8 + t) * 1024), 16, l16, wbase + (unsigned)(t * 128 + ks) * 1024u, 0, 0);
    };
    issue(0); issue(1); issue(2);
    u32x4 ax[8];
    f32x4 sx0[2], sx1[2];
    u32x4 qf[2][4];
    {
        u32x4 kx[2][4];
#pragma unroll
        for (int ks = 0; ks < 4; ++ks)
#pragma unroll
            for (int t = 0; t < 2; ++t) { kx[t][ks] = bld16(rW, l16, (unsigned)OFF_K + sh + (unsigned)(t * 4 + ks) * 1024u); qf[t][ks] = bld16(rW, l16, (unsigned)OFF_Q + sh + (unsigned)(t * 4 + ks) * 1024u); }
#pragma unroll
        for (int et = 0; et < 8; ++et) ax[et] = bld16(rW, l16, (unsigned)OFF_VT + sh + (unsigned)et * 1024u);
#pragma unroll
        for (int qt = 0; qt < 2; ++qt) {
            sx0[qt] = (f32x4){0.f, 0.f, 0.f, 0.f}; sx1[qt] = sx0[qt];
#pragma unroll
            for (int ks = 0; ks < 4; ++ks) {
                sx0[qt] = __builtin_amdgcn_mfma_f32_16x16x32_bf16(__builtin_bit_cast(bf16x8, kx[0][ks]), __builtin_bit_cast(bf16x8, qf[qt][ks]), sx0[qt], 0, 0, 0);
                sx1[qt] = __builtin_amdgcn_mfma_f32_16x16x32_bf16(__builtin_bit_cast(bf16x8, kx[1][ks]), __builtin_bit_cast(bf16x8, qf[qt][ks]), sx1[qt], 0, 0, 0);
            }
        }
    }
    if (w == 0) {
        const unsigned* f = ws.flagM_() + s * NH + hg * 4 + (lane & 3);
        for (unsigned spin = 0; spin < (1u << 22); ++spin) {
            unsigned v = __hip_atomic_load(f, __ATOMIC_RELAXED, __HIP_MEMORY_SCOPE_AGENT);
            if (__all(v != 0)) break;
            __builtin_amdgcn_s_sleep(64);
        }
    }
    asm volatile("" ::: "memory");
    __builtin_amdgcn_s_barrier();
    asm volatile("" ::: "memory");
    u32x4 kmf[4];
    u32x2 vm[8];
#pragma unroll
    for (int ks = 0; ks < 4; ++ks) kmf[ks] = bld16_sc1(rW, (unsigned)fr8 * 256 + fg * 16, (unsigned)OFF_km + (unsigned)(s * NH + h) * 2048u + ks * 64u);
#pragma unroll
    for (int et = 0; et < 8; ++et) {
        vm[et] = (u32x2){0u, 0u};
        if (fg < 2) vm[et] = bld8_sc1(rW, (unsigned)fr * 16 + fg * 8, (unsigned)OFF_vmT + (unsigned)(s * NH + h) * 2048u + et * 256u);
    }
    u32x4 px[2], pm[2];
#pragma unroll
    for (int qt = 0; qt < 2; ++qt) {
        f32x4 sm = {0.f, 0.f, 0.f, 0.f};
#pragma unroll
        for (int ks = 0; ks < 4; ++ks) sm = __builtin_amdgcn_mfma_f32_16x16x32_bf16(__builtin_bit_cast(bf16x8, kmf[ks]), __builtin_bit_cast(bf16x8, qf[qt][ks]), sm, 0, 0, 0);
        const int qi = qt * 16 + fr;
        float sv[12];
#pragma unroll
        for (int q = 0; q < 4; ++q) {
            sv[q] = (fg < 2) ? rbf(rbf(sm[q]) * SCALE) : -3.0e38f;
            int k0 = fg * 4 + q, k1 = 16 + fg * 4 + q;
            sv[4 + q] = (k0 <= qi) ? rbf(rbf(sx0[qt][q]) * SCALE) : -3.0e38f;
            sv[8 + q] = (k1 <= qi) ? rbf(rbf(sx1[qt][q]) * SCALE) : -3.0e38f;
        }
        float mx = sv[0];
#pragma unroll
        for (int q = 1; q < 12; ++q) mx = fmaxf(mx, sv[q]);
        mx = xmax16(mx); mx = xmax32(mx);
        float sum = 0.f;
#pragma unroll
        for (int q = 0; q < 12; ++q) { sv[q] = __expf(sv[q] - mx); sum += sv[q]; }
        sum = xsum16(sum); sum = xsum32(sum);
        float inv = 1.f / sum;
        px[qt] = (u32x4){pack2(sv[4] * inv, sv[5] * inv), pack2(sv[6] * inv, sv[7] * inv), pack2(sv[8] * inv, sv[9] * inv), pack2(sv[10] * inv, sv[11] * inv)};
        pm[qt] = (u32x4){pack2(sv[0] * inv, sv[1] * inv), pack2(sv[2] * inv, sv[3] * inv), 0u, 0u};
    }
#pragma unroll
    for (int et = 0; et < 8; ++et) {
        u32x4 am = {vm[et][0], vm[et][1], 0u, 0u};
#pragma unroll
        for (int qt = 0; qt < 2; ++qt) {
            f32x4 o = {0.f, 0.f, 0.f, 0.f};
            o = __builtin_amdgcn_mfma_f32_16x16x32_bf16(__builtin_bit_cast(bf16x8, ax[et]), __builtin_bit_cast(bf16x8, px[qt]), o, 0, 0, 0);
            o = __builtin_amdgcn_mfma_f32_16x16x32_bf16(__builtin_bit_cast(bf16x8, am), __builtin_bit_cast(bf16x8, pm[qt]), o, 0, 0, 0);
            u32x2 ov = {pack2(o[0], o[1]), pack2(o[2], o[3])};
            *(u32x2*)(sO + (qt * 16 + fr) * 1040 + (w * DH + et * 16 + fg * 4) * 2) = ov;
        }
    }
    asm volatile("s_waitcnt lgkmcnt(0)\n\ts_barrier" ::: "memory");
    f32x4 acc[8][2];
#pragma unroll
    for (int t = 0; t < 8; ++t) { acc[t][0] = (f32x4){0.f, 0.f, 0.f, 0.f}; acc[t][1] = acc[t][0]; }
    {
#pragma unroll
        for (int ks = 0; ks < 16; ++ks) {
            if (ks <= 13) asm volatile("s_waitcnt vmcnt(16)" ::: "memory");
            else if (ks == 14) asm volatile("s_waitcnt vmcnt(8)" ::: "memory");
            else asm volatile("s_waitcnt vmcnt(0)" ::: "memory");
            bf16x8 b0 = *(const bf16x8*)(sO + fr * 1040 + (ks * 32 + fg * 8) * 2);
            bf16x8 b1 = *(const bf16x8*)(sO + (16 + fr) * 1040 + (ks * 32 + fg * 8) * 2);
            bf16x8 a[8];
#pragma unroll
            for (int t = 0; t < 8; ++t) a[t] = *(const bf16x8*)(ring + ((ks % 3) * 8 + t) * 1024 + lane * 16);
            if (ks + 3 < 16) {
                asm volatile("s_waitcnt lgkmcnt(0)" ::: "memory");
                issue(ks + 3);
            }
#pragma unroll
            for (int t = 0; t < 8; ++t) {
                acc[t][0] = __builtin_amdgcn_mfma_f32_16x16x32_bf16(a[t], b0, acc[t][0], 0, 0, 0);
                acc[t][1] = __builtin_amdgcn_mfma_f32_16x16x32_bf16(a[t], b1, acc[t][1], 0, 0, 0);
            }
        }
    }
#pragma unroll
    for (int t = 0; t < 8; ++t)
#pragma unroll
        for (int qt = 0; qt < 2; ++qt) {
            u32x4 o = {__float_as_uint(acc[t][qt][0]), __float_as_uint(acc[t][qt][1]), __float_as_uint(acc[t][qt][2]), __float_as_uint(acc[t][qt][3])};
            unsigned off = (unsigned)((((s * 8 + hg) * 32 + qt * 16 + fr) * 512 + w * 128 + t * 16 + fg * 4) * 4);
            bst16_sc1(rW, off, (unsigned)OFF_part, o);
        }
}

__device__ __forceinline__ void reduce_task(int s, int r0, const u16* __restrict__ x, u16* __restrict__ out, const WS& ws, const int tidx) {
    const int i = tidx;
    {
        int row = r0 + (i >> 7), c = (i & 127) * 4;
        f32x4 t = {0.f, 0.f, 0.f, 0.f};
        f32x4 pv[8];
#pragma unroll
        for (int hg = 0; hg < 8; ++hg) pv[hg] = *(const f32x4*)(ws.part_() + ((size_t)(s * 8 + hg) * 32 + row) * 512 + c);
#pragma unroll
        for (int hg = 0; hg < 8; ++hg) t += pv[hg];
        int tok = s * SEG + row;
        u32x2 xv = *(const u32x2*)(x + (size_t)tok * DM + c);
        u32x2 gv = *(const u32x2*)(ws.g_() + (size_t)tok * DM + c);
        float xs[4] = {bf2f(xv[0] & 0xffff), bf2f(xv[0] >> 16), bf2f(xv[1] & 0xffff), bf2f(xv[1] >> 16)};
        float gs[4] = {bf2f(gv[0] & 0xffff), bf2f(gv[0] >> 16), bf2f(gv[1] & 0xffff), bf2f(gv[1] >> 16)};
        float r[4];
#pragma unroll
        for (int q = 0; q < 4; ++q) r[q] = xs[q] + rbf(rbf(t[q]) * gs[q]);
        u32x2 o = {pack2(r[0], r[1]), pack2(r[2], r[3])};
        *(u32x2*)(out + (size_t)tok * DM + c) = o;
    }
}

#define NWORK 224
#define T_KV0 64
#define T_Z 64
#define T_KV 960
#define T_H 176
#define T_Q 512
#define T_G 64
#define T_A 512
#define T_TOTAL (T_KV0 + T_Z + T_KV + T_H + T_Q + T_G + T_A)
#define LDS_BYTES 162816

__global__ void __launch_bounds__(256, 1) mega_k(const u16* __restrict__ x, const u16* __restrict__ mem0, const u16* __restrict__ b1, const u16* __restrict__ b2,
                                                 u16* __restrict__ out, WS ws) {
    extern __shared__ __attribute__((aligned(16))) char smem[];
    if (blockIdx.x < NH) {
        chain_body(mem0, ws, smem, blockIdx.x);
        return;
    }
    const int tid = threadIdx.x, lane = tid & 63, w = tid >> 6;
    for (int task = blockIdx.x - NH; task < T_TOTAL; task += NWORK) {
        int t = task;
        int tidx = threadIdx.x;
        asm volatile("" : "+v"(tidx));
        if (t < T_KV0 + T_Z + T_KV) {
            if (t >= T_KV0 && t < T_KV0 + T_Z) {
                const int z = t - T_KV0;
                const rsrc_t rW = mk_rsrc(ws.base);
                u32x4 zz = {0u, 0u, 0u, 0u};
#pragma unroll 8
                for (int i = 0; i < 128; ++i) bst16_sc1(rW, (unsigned)tidx * 16, (unsigned)OFF_exA + (unsigned)z * 524288u + (unsigned)i * 4096u, zz);
                publish_done();
                if (tid == 0) __hip_atomic_store(ws.flagZ_() + z, 1u, __ATOMIC_RELAXED, __HIP_MEMORY_SCOPE_AGENT);
                continue;
            }
            if (t >= T_KV0) t -= T_Z;
            int tb = t >> 6, cb = t & 63;
            gemm_tile<EPI_QKV, 16>((unsigned)OFF_xnF, tb, 32 + cb, ws, nullptr, tidx);
            publish_done();
            if (tid == 0) __hip_atomic_store(ws.flagKV_() + tb * 64 + cb, 1u, __ATOMIC_RELAXED, __HIP_MEMORY_SCOPE_AGENT);
            continue;
        }
        t -= T_KV0 + T_Z + T_KV;
        if (t < T_H) {
            int tb = t / 11, cb = t % 11;
            gemm_tile<EPI_H, 16>((unsigned)OFF_yF, tb, cb, ws, b1, tidx);
            publish_done();
            if (tid == 0) __hip_atomic_fetch_add(ws.cntH_() + tb, 1u, __ATOMIC_RELAXED, __HIP_MEMORY_SCOPE_AGENT);
            continue;
        }
        t -= T_H;
        if (t < T_Q) {
            int tb = t >> 5, cb = t & 31;
            gemm_tile<EPI_QKV, 16>((unsigned)OFF_xnF, tb, cb, ws, nullptr, tidx);
            publish_done();
            if (tid == 0) __hip_atomic_store(ws.flagQ_() + tb * 32 + cb, 1u, __ATOMIC_RELAXED, __HIP_MEMORY_SCOPE_AGENT);
            continue;
        }
        t -= T_Q;
        if (t < T_G) {
            int tb = t >> 2, cb = t & 3;
            if (w == 0) wait_flag_ge(ws.cntH_() + tb, 11u);
            acquire_all();
            gemm_tile<EPI_G, 44>((unsigned)OFF_hmidF, tb, cb, ws, b2, tidx);
            publish_done();
            if (tid == 0) __hip_atomic_store(ws.flagG_() + tb * 4 + cb, 1u, __ATOMIC_RELAXED, __HIP_MEMORY_SCOPE_AGENT);
            continue;
        }
        t -= T_G;
        {
            int s = t >> 3, hg = t & 7, tb = s >> 2;
            if (w == 0) {
                const unsigned* f;
                int l = lane & 15, hh = hg * 4 + (l & 3);
                if (l < 4) f = ws.flagKV_() + tb * 64 + hh;
                else if (l < 8) f = ws.flagKV_() + tb * 64 + 32 + hh;
                else f = ws.flagQ_() + tb * 32 + hh;
                wait_flag_ge(f, 1u);
            }
            acquire_all();
            attn_task(s, hg, ws, smem, tidx);
            publish_done();
            if (tid == 0) __hip_atomic_fetch_add(ws.cntO_() + s, 1u, __ATOMIC_RELAXED, __HIP_MEMORY_SCOPE_AGENT);
            if (w == 0) { wait_flag_ge(lane < 4 ? ws.flagG_() + tb * 4 + lane : ws.cntO_() + s, lane < 4 ? 1u : 8u); }
            acquire_all();
            reduce_task(s, hg * 4, x, out, ws, tidx);
            reduce_task(s, hg * 4 + 2, x, out, ws, tidx);
            __syncthreads();
        }
    }
}

extern "C" void kernel_launch(void* const* d_in, const int* in_sizes, int n_in, void* d_out, int out_size, void* d_ws, size_t ws_size,
                              hipStream_t stream) {
    const u16* x = (const u16*)d_in[0];
    const u16* mem0 = (const u16*)d_in[1];
    const u16* Wq = (const u16*)d_in[2];
    const u16* Wk = (const u16*)d_in[3];
    const u16* Wv = (const u16*)d_in[4];
    const u16* Wo = (const u16*)d_in[5];
    const u16* w1 = (const u16*)d_in[8];
    const u16* b1 = (const u16*)d_in[9];
    const u16* w2 = (const u16*)d_in[10];
    const u16* b2 = (const u16*)d_in[11];
    u16* out = (u16*)d_out;

    WS ws; ws.base = (char*)d_ws;

    const int nprep = PREP_LN + PREP_TQKV + PREP_TWO + PREP_TW1 + PREP_TW2 + PREP_ZERO;
    prep_k<<<nprep, 256, 0, stream>>>(x, Wq, Wk, Wv, Wo, w1, w2, ws);
    (void)hipFuncSetAttribute((const void*)mega_k, hipFuncAttributeMaxDynamicSharedMemorySize, LDS_BYTES);
    mega_k<<<256, 256, LDS_BYTES, stream>>>(x, mem0, b1, b2, out, ws);
}
```

```cpp
#include <hip/hip_runtime.h>
#include <hip/hip_bf16.h>
#include <stdint.h>

typedef __attribute__((ext_vector_type(8))) short bf16x8;
typedef __attribute__((ext_vector_type(4))) float f32x4;
typedef __attribute__((ext_vector_type(4))) unsigned u32x4;
typedef __attribute__((ext_vector_type(2))) unsigned u32x2;
typedef unsigned short u16;

#define S_TOK 2048
#define DM 512
#define NH 32
#define DH 128
#define HD 4096
#define NMEM 8
#define SEG 32
#define NSEG 64
#define NSTEPS 64
#define FF 1376
#define FFP 1408

__device__ __forceinline__ u16 f2bf(float f) { return __builtin_bit_cast(u16, (__bf16)f); }
__device__ __forceinline__ float bf2f(u16 h) { return __builtin_bit_cast(float, (unsigned)h << 16); }
__device__ __forceinline__ float rbf(float f) { return bf2f(f2bf(f)); }
typedef __attribute__((ext_vector_type(2))) float f32x2;
typedef __attribute__((ext_vector_type(2))) __bf16 bf16x2v;
__device__ __forceinline__ unsigned pack2(float a, float b) { f32x2 v = {a, b}; return __builtin_bit_cast(unsigned, __builtin_convertvector(v, bf16x2v)); }
__device__ __forceinline__ f32x2 rbf2(f32x2 v) { unsigned u = __builtin_bit_cast(unsigned, __builtin_convertvector(v, bf16x2v)); f32x2 r = {__uint_as_float(u << 16), __uint_as_float(u & 0xffff0000u)}; return r; }

constexpr size_t OFF_flags = 0;
constexpr size_t OFF_flagMr = OFF_flags + 65536;
constexpr size_t OFF_exB = OFF_flagMr + 65536;
constexpr size_t OFF_exA = OFF_exB + (((size_t)68 * 16384) + 255) / 256 * 256;
constexpr size_t OFF_xnF = OFF_exA + (((size_t)64 * 524288) + 255) / 256 * 256;
constexpr size_t OFF_yF = OFF_xnF + (((size_t)S_TOK * DM * 2) + 255) / 256 * 256;
constexpr size_t OFF_WqkvF = OFF_yF + (((size_t)S_TOK * DM * 2) + 255) / 256 * 256;
constexpr size_t OFF_WoF = OFF_WqkvF + (((size_t)3 * HD * DM * 2) + 255) / 256 * 256;
constexpr size_t OFF_W1F = OFF_WoF + (((size_t)DM * HD * 2) + 255) / 256 * 256;
constexpr size_t OFF_W2F = OFF_W1F + (((size_t)FFP * DM * 2) + 255) / 256 * 256;
constexpr size_t OFF_Q = OFF_W2F + (((size_t)DM * FFP * 2) + 255) / 256 * 256;
constexpr size_t OFF_K = OFF_Q + (((size_t)S_TOK * HD * 2) + 255) / 256 * 256;
constexpr size_t OFF_VT = OFF_K + (((size_t)S_TOK * HD * 2) + 255) / 256 * 256;
constexpr size_t OFF_hmidF = OFF_VT + (((size_t)S_TOK * HD * 2) + 255) / 256 * 256;
constexpr size_t OFF_g = OFF_hmidF + (((size_t)S_TOK * FFP * 2) + 255) / 256 * 256;
constexpr size_t OFF_km = OFF_g + (((size_t)S_TOK * DM * 2) + 255) / 256 * 256;
constexpr size_t OFF_vmT = OFF_km + (((size_t)NSEG * NH * 8 * DH * 2) + 255) / 256 * 256;
constexpr size_t OFF_part = OFF_vmT + (((size_t)NSEG * NH * 8 * DH * 2) + 255) / 256 * 256;
constexpr size_t OFF_dbg = (size_t)240 * 1024 * 1024;
struct WS {
    char* base;
    __host__ __device__ unsigned* flags_() const { return (unsigned*)(base + OFF_flags); }
    __host__ __device__ unsigned* exB_() const { return (unsigned*)(base + OFF_exB); }
    __host__ __device__ unsigned* exA_() const { return (unsigned*)(base + OFF_exA); }
    __host__ __device__ u16* xnF_() const { return (u16*)(base + OFF_xnF); }
    __host__ __device__ u16* yF_() const { return (u16*)(base + OFF_yF); }
    __host__ __device__ u16* WqkvF_() const { return (u16*)(base + OFF_WqkvF); }
    __host__ __device__ u16* WoF_() const { return (u16*)(base + OFF_WoF); }
    __host__ __device__ u16* W1F_() const { return (u16*)(base + OFF_W1F); }
    __host__ __device__ u16* W2F_() const { return (u16*)(base + OFF_W2F); }
    __host__ __device__ u16* Q_() const { return (u16*)(base + OFF_Q); }
    __host__ __device__ u16* K_() const { return (u16*)(base + OFF_K); }
    __host__ __device__ u16* VT_() const { return (u16*)(base + OFF_VT); }
    __host__ __device__ u16* hmidF_() const { return (u16*)(base + OFF_hmidF); }
    __host__ __device__ u16* g_() const { return (u16*)(base + OFF_g); }
    __host__ __device__ u16* km_() const { return (u16*)(base + OFF_km); }
    __host__ __device__ u16* vmT_() const { return (u16*)(base + OFF_vmT); }
    __host__ __device__ float* part_() const { return (float*)(base + OFF_part); }
    __host__ __device__ unsigned* flagKV_() const { return flags_(); }
    __host__ __device__ unsigned* flagQ_() const { return flags_() + 1024; }
    __host__ __device__ unsigned* cntH_() const { return flags_() + 1536; }
    __host__ __device__ unsigned* flagG_() const { return flags_() + 1600; }
    __host__ __device__ unsigned* flagZ_() const { return flags_() + 1792; }
    __host__ __device__ unsigned* flagM_() const { return (unsigned*)(base + OFF_flagMr); }
    __host__ __device__ unsigned* cntO_() const { return flags_() + 4096; }
    __host__ __device__ unsigned long long* dbg_() const { return (unsigned long long*)(base + OFF_dbg); }
};

__device__ __forceinline__ float wave_sum(float v) {
#pragma unroll
    for (int m = 32; m >= 1; m >>= 1) v += __shfl_xor(v, m);
    return v;
}
__device__ __forceinline__ void ln8(float (&v)[8]) {
    float s = 0.f;
#pragma unroll
    for (int i = 0; i < 8; ++i) s += v[i];
    s = wave_sum(s);
    float mean = s * (1.f / 512.f);
    float q = 0.f;
#pragma unroll
    for (int i = 0; i < 8; ++i) { float d = v[i] - mean; q += d * d; }
    q = wave_sum(q);
    float var = rbf(q * (1.f / 512.f));
    float mb = rbf(mean);
    float sd = rbf(sqrtf(rbf(var + 1e-5f)));
#pragma unroll
    for (int i = 0; i < 8; ++i) v[i] = rbf(rbf(v[i] - mb) / sd);
}
template <class F>
__device__ __forceinline__ void frag_tile(const u16* __restrict__ src, int R, int C, int bx, int by, u16 (*t)[72], F addr) {
    const int c0 = bx * 64, r0 = by * 64;
#pragma unroll
    for (int i = 0; i < 2; ++i) {
        int idx = threadIdx.x + i * 256, rr = idx >> 3, cc = (idx & 7) * 8;
        int r = r0 + rr, c = c0 + cc;
        u32x4 v = {0u, 0u, 0u, 0u};
        if (r < R && c < C) v = *(const u32x4*)(src + (size_t)r * C + c);
        *(u32x4*)(&t[rr][cc]) = v;
    }
    __syncthreads();
    int l = threadIdx.x & 63, q = threadIdx.x >> 6;
#pragma unroll
    for (int i = 0; i < 2; ++i) {
        int f = q * 2 + i, ksl = f >> 2, ct = f & 3;
        u16 v[8];
#pragma unroll
        for (int j = 0; j < 8; ++j) v[j] = t[ksl * 32 + (l >> 4) * 8 + j][ct * 16 + (l & 15)];
        u32x4 o = {(unsigned)v[0] | ((unsigned)v[1] << 16), (unsigned)v[2] | ((unsigned)v[3] << 16), (unsigned)v[4] | ((unsigned)v[5] << 16), (unsigned)v[6] | ((unsigned)v[7] << 16)};
        *(u32x4*)(addr(ksl, ct) + l * 8) = o;
    }
}
#define PREP_LN 512
#define PREP_TQKV 1536
#define PREP_TWO 512
#define PREP_TW1 176
#define PREP_TW2 176
#define ZERO_BYTES ((size_t)68 * 16384 + 2 * 65536)
#define PREP_ZERO ((int)(ZERO_BYTES / 65536))
__global__ void __launch_bounds__(256) prep_k(const u16* __restrict__ x, const u16* __restrict__ Wq, const u16* __restrict__ Wk, const u16* __restrict__ Wv,
                                              const u16* __restrict__ Wo, const u16* __restrict__ w1, const u16* __restrict__ w2, WS ws) {
    __shared__ __attribute__((aligned(16))) u16 t[64][72];
    int b = blockIdx.x;
    if (b < PREP_LN) {
        int row = b * 4 + (threadIdx.x >> 6);
        int lane = threadIdx.x & 63;
        u32x4 r = *(const u32x4*)(x + (size_t)row * DM + lane * 8);
        float v[8];
#pragma unroll
        for (int i = 0; i < 4; ++i) { v[2 * i] = bf2f(r[i] & 0xffff); v[2 * i + 1] = bf2f(r[i] >> 16); }
        ln8(v);
        u32x4 o;
#pragma unroll
        for (int i = 0; i < 4; ++i) o[i] = pack2(v[2 * i], v[2 * i + 1]);
        const size_t fo = ((size_t)((row >> 4) * 16 + (lane >> 2)) * 64 + (lane & 3) * 16 + (row & 15)) * 8;
        *(u32x4*)(ws.xnF_() + fo) = o;
        ln8(v);
#pragma unroll
        for (int i = 0; i < 4; ++i) o[i] = pack2(v[2 * i], v[2 * i + 1]);
        *(u32x4*)(ws.yF_() + fo) = o;
        return;
    }
    b -= PREP_LN;
    if (b < PREP_TQKV) {
        int m = b / 512, r = b % 512;
        const u16* src = m == 0 ? Wq : (m == 1 ? Wk : Wv);
        const int bx = r % 64, by = r / 64;
        frag_tile(src, DM, HD, bx, by, t, [&](int ksl, int ct) {
            int n = bx * 64 + ct * 16, ks = by * 2 + ksl;
            int hh = n >> 7, ww = (n >> 5) & 3, tt = (n >> 4) & 1;
            return ws.WqkvF_() + ((size_t)(((hh * 4 + ww) * 6 + m * 2 + tt) * 16 + ks)) * 512;
        });
        return;
    }
    b -= PREP_TQKV;
    if (b < PREP_TWO) {
        const int bx = b % 8, by = b / 8;
        frag_tile(Wo, HD, DM, bx, by, t, [&](int ksl, int ct) {
            int tile = bx * 4 + ct, k = by * 64 + ksl * 32;
            return ws.WoF_() + ((size_t)((tile * 32 + (k >> 7)) * 4 + ((k >> 5) & 3))) * 512;
        });
        return;
    }
    b -= PREP_TWO;
    if (b < PREP_TW1) {
        const int bx = b % 22, by = b / 22;
        frag_tile(w1, DM, FF, bx, by, t, [&](int ksl, int ct) { return ws.W1F_() + ((size_t)((bx * 4 + ct) * 16 + by * 2 + ksl)) * 512; });
        return;
    }
    b -= PREP_TW1;
    if (b < PREP_TW2) {
        const int bx = b % 8, by = b / 8;
        frag_tile(w2, FF, DM, bx, by, t, [&](int ksl, int ct) { return ws.W2F_() + ((size_t)((bx * 4 + ct) * 44 + by * 2 + ksl)) * 512; });
        return;
    }
    b -= PREP_TW2;
    {
        u32x4 z = {0u, 0u, 0u, 0u};
        u32x4* p = (u32x4*)(ws.base + (size_t)b * 65536) + threadIdx.x;
#pragma unroll
        for (int i = 0; i < 16; ++i) p[i * 256] = z;
    }
}

#define SCALE 0.08838834764831845f
#define MEM_STRIDE 1056
#define QK_STRIDE 288

typedef __amdgpu_buffer_rsrc_t rsrc_t;
__device__ __forceinline__ rsrc_t mk_rsrc(const void* p) { return __builtin_amdgcn_make_buffer_rsrc((void*)p, 0, 0x7fffffff, 0x00020000); }
__device__ __forceinline__ u32x4 bld16(rsrc_t r, unsigned vo, unsigned so) { return __builtin_amdgcn_raw_buffer_load_b128(r, vo, so, 0); }
__device__ __forceinline__ u32x4 bld16_sc1(rsrc_t r, unsigned vo, unsigned so) { return __builtin_amdgcn_raw_buffer_load_b128(r, vo, so, 16); }
__device__ __forceinline__ u32x2 bld8(rsrc_t r, unsigned vo, unsigned so) { return __builtin_amdgcn_raw_buffer_load_b64(r, vo, so, 0); }
__device__ __forceinline__ void bst16_sc1(rsrc_t r, unsigned vo, unsigned so, u32x4 v) { __builtin_amdgcn_raw_buffer_store_b128(v, r, vo, so, 16); }
__device__ __forceinline__ void bst8(rsrc_t r, unsigned vo, unsigned so, u32x2 v) { __builtin_amdgcn_raw_buffer_store_b64(v, r, vo, so, 0); }
__device__ __forceinline__ void bst8_sc1(rsrc_t r, unsigned vo, unsigned so, u32x2 v) { __builtin_amdgcn_raw_buffer_store_b64(v, r, vo, so, 16); }

#define STAMP(i)
#define MFMA0_A(acc, a, b) asm volatile("v_mfma_f32_16x16x32_bf16 %0, %1, %2, 0" : "=&v"(acc) : "a"(a), "v"(b))
#define MFMA_A(acc, a, b) asm volatile("v_mfma_f32_16x16x32_bf16 %0, %1, %2, %0" : "+v"(acc) : "a"(a), "v"(b))
#define MFMA0_V(acc, a, b) asm volatile("v_mfma_f32_16x16x32_bf16 %0, %1, %2, 0" : "=&v"(acc) : "v"(a), "v"(b))
#define MFMA_V(acc, a, b) asm volatile("v_mfma_f32_16x16x32_bf16 %0, %1, %2, %0" : "+v"(acc) : "v"(a), "v"(b))
#define BARRIER() asm volatile("s_waitcnt lgkmcnt(0)\n\ts_barrier" ::: "memory")
#define LAS __attribute__((address_space(3)))

__device__ __forceinline__ float xmax16(float x) {
    unsigned u = __float_as_uint(x);
    auto r = __builtin_amdgcn_permlane16_swap(u, u, false, false);
    return fmaxf(__uint_as_float(r[0]), __uint_as_float(r[1]));
}
__device__ __forceinline__ float xmax32(float x) {
    unsigned u = __float_as_uint(x);
    auto r = __builtin_amdgcn_permlane32_swap(u, u, false, false);
    return fmaxf(__uint_as_float(r[0]), __uint_as_float(r[1]));
}
__device__ __forceinline__ float xsum16(float x) {
    unsigned u = __float_as_uint(x);
    auto r = __builtin_amdgcn_permlane16_swap(u, u, false, false);
    return __uint_as_float(r[0]) + __uint_as_float(r[1]);
}
__device__ __forceinline__ float xsum32(float x) {
    unsigned u = __float_as_uint(x);
    auto r = __builtin_amdgcn_permlane32_swap(u, u, false, false);
    return __uint_as_float(r[0]) + __uint_as_float(r[1]);
}
__device__ __forceinline__ unsigned ror8u(unsigned x) { return __builtin_amdgcn_update_dpp(0u, x, 0x128, 0xf, 0xf, false); }
__device__ __forceinline__ float ror8(float x) { return __uint_as_float(ror8u(__float_as_uint(x))); }
__device__ __forceinline__ float xsum8(float x) {
    return x + __uint_as_float(__builtin_amdgcn_update_dpp(0u, __float_as_uint(x), 0x128, 0xf, 0xf, false));
}

#define DELAY_A 12
#define DELAY_B 12
#define EXA_STEP 524288u
#define EXB_STEP 16384u
__device__ __forceinline__ void chain_body(const u16* __restrict__ mem0, const WS& ws, char* smem, const int h) {
    char* sW = smem;
    char* sKV = smem + 131072;
    char* sMem = smem + 147456;
    char* sQK = sMem + 8 * MEM_STRIDE;
    char* sO = sQK + 16 * QK_STRIDE;
    const int tid = threadIdx.x, lane = tid & 63;
    const int w = __builtin_amdgcn_readfirstlane(tid >> 6);
    const int fr = lane & 15, fg = lane >> 4, fr8 = lane & 7;
    const unsigned l16 = lane * 16;
    char* lw = sW + w * 32768 + l16;

    bf16x8 wqk[3][16], wv[24], wo[24];
#pragma unroll
    for (int t = 0; t < 6; ++t) {
        const u16* p = ws.WqkvF_() + ((size_t)((h * 4 + w) * 6 + t) * 16) * 512 + lane * 8;
#pragma unroll
        for (int ks = 0; ks < 16; ++ks) {
            bf16x8 v = *(const bf16x8*)(p + ks * 512);
            if (t < 3) wqk[t][ks] = v;
            else if (t == 3) *(bf16x8*)(lw + ks * 1024) = v;
            else if (t == 4) wv[ks] = v;
            else if (ks < 8) wv[16 + ks] = v;
            else *(bf16x8*)(lw + (16 + ks - 8) * 1024) = v;
        }
    }
#pragma unroll
    for (int t = 0; t < 8; ++t)
#pragma unroll
        for (int ks = 0; ks < 4; ++ks) {
            bf16x8 v = *(const bf16x8*)(ws.WoF_() + ((size_t)(((w * 8 + t) * 32 + h) * 4 + ks)) * 512 + lane * 8);
            if (t < 6) wo[t * 4 + ks] = v;
            else *(bf16x8*)(lw + (24 + (t - 6) * 4 + ks) * 1024) = v;
        }
    for (int i = tid; i < 8 * 64; i += 256) {
        int row = i >> 6, c = (i & 63) * 8;
        *(u32x4*)(sMem + row * MEM_STRIDE + c * 2) = *(const u32x4*)(mem0 + row * DM + c);
    }
    const rsrc_t rW = mk_rsrc(ws.base);
    const unsigned stA_vo = (unsigned)fg * 128 + (fr & 7) * 16 + (fr >> 3) * 16384;
    const unsigned rdA_vo = (unsigned)(lane >> 3) * 512 + (lane & 7) * 16;
    const char* memrd = sMem + fr8 * MEM_STRIDE + fg * 16;
    const char* qrd = sQK + fr8 * QK_STRIDE + fg * 16;
    const char* ord_ = sO + fr8 * QK_STRIDE + fg * 16;
    char* memwr = sMem + (lane & 7) * MEM_STRIDE + ((w * 2 + (lane >> 5)) * 16 + ((lane >> 3) & 3) * 4) * 2;

    for (int s = 0; s < NSTEPS; ++s) {
        STAMP(0);
        if ((s & 3) == 0) {
            const unsigned* f = (lane & 4) ? (ws.flagZ_() + s + (lane & 3)) : (ws.flagKV_() + (s >> 2) * 64 + (lane & 1) * 32 + h);
            for (unsigned spin = 0; spin < (1u << 22); ++spin) {
                unsigned v = __hip_atomic_load(f, __ATOMIC_RELAXED, __HIP_MEMORY_SCOPE_AGENT);
                if (__all(v != 0)) break;
                __builtin_amdgcn_s_sleep(2);
            }
        }
        {
            const u16* gk = ws.K_() + (size_t)(s * NH + h) * 4096 + w * 1024 + lane * 8;
            const u16* gv = ws.VT_() + (size_t)(s * NH + h) * 4096 + w * 1024 + lane * 8;
            __builtin_amdgcn_global_load_lds((const unsigned*)gk, (LAS unsigned*)(sKV + w * 2048), 16, 0, 16);
            __builtin_amdgcn_global_load_lds((const unsigned*)(gk + 512), (LAS unsigned*)(sKV + w * 2048 + 1024), 16, 0, 16);
            __builtin_amdgcn_global_load_lds((const unsigned*)gv, (LAS unsigned*)(sKV + 8192 + w * 2048), 16, 0, 16);
            __builtin_amdgcn_global_load_lds((const unsigned*)(gv + 512), (LAS unsigned*)(sKV + 8192 + w * 2048 + 1024), 16, 0, 16);
        }
        if (s == 0) {
            asm volatile("s_waitcnt vmcnt(0)" ::: "memory");
        } else {
#pragma unroll 1
            for (int d = 0; d < DELAY_B; ++d) __builtin_amdgcn_s_sleep(1);
            u32x4 vv[4];
            for (unsigned spin = 0;; ++spin) {
                if (spin > (1u << 20)) break;
                asm volatile("" ::: "memory");
#pragma unroll
                for (int i = 0; i < 4; ++i) vv[i] = bld16_sc1(rW, l16, ((unsigned)(i * 4096 + w * 1024) + (unsigned)s * EXB_STEP) + (unsigned)OFF_exB);
                unsigned am_ = 1u;
#pragma unroll
                for (int i = 0; i < 4; ++i) am_ &= (vv[i][0] & vv[i][1]) & (vv[i][2] & vv[i][3]);
                if (__all(am_ != 0)) break;
            }
#pragma unroll
            for (int i = 0; i < 4; ++i) {
                u32x2 o = {(vv[i][0] >> 16) | (vv[i][1] & 0xffff0000u), (vv[i][2] >> 16) | (vv[i][3] & 0xffff0000u)};
                *(u32x2*)(memwr + i * 256) = o;
            }
        }
        bf16x8 kw[2], vw[2];
#pragma unroll
        for (int i = 0; i < 2; ++i) kw[i] = *(const bf16x8*)(lw + i * 1024);
        vw[0] = *(const bf16x8*)(lw + 16 * 1024); vw[1] = *(const bf16x8*)(lw + 17 * 1024);
        STAMP(1);
        BARRIER();
        STAMP(2);

        f32x4 acc[4], av[2];
        {
            bf16x8 mb[3];
#pragma unroll
            for (int i = 0; i < 3; ++i) mb[i] = *(const bf16x8*)(memrd + i * 64);
#pragma unroll
            for (int ks = 0; ks < 16; ++ks) {
                bf16x8 m = mb[ks % 3];
                bf16x8 k3 = kw[ks & 1];
                bf16x8 v5 = (ks < 8) ? wv[16 + ks] : vw[ks & 1];
                if (ks == 0) {
                    MFMA0_A(acc[0], wqk[0][ks], m); MFMA0_A(acc[1], wqk[1][ks], m); MFMA0_A(acc[2], wqk[2][ks], m); MFMA0_V(acc[3], k3, m);
                    MFMA0_V(av[0], m, wv[ks]); MFMA0_V(av[1], m, v5);
                } else {
                    MFMA_A(acc[0], wqk[0][ks], m); MFMA_A(acc[1], wqk[1][ks], m); MFMA_A(acc[2], wqk[2][ks], m); MFMA_V(acc[3], k3, m);
                    MFMA_V(av[0], m, wv[ks]); MFMA_V(av[1], m, v5);
                }
                if (ks + 3 < 16) mb[ks % 3] = *(const bf16x8*)(memrd + (ks + 3) * 64);
                if (ks + 2 < 16) kw[ks & 1] = *(const bf16x8*)(lw + (ks + 2) * 1024);
                if (ks >= 8 && ks + 2 < 16) vw[ks & 1] = *(const bf16x8*)(lw + (16 + ks + 2 - 8) * 1024);
            }
        }
        STAMP(3);
        asm volatile("s_nop 7" : "+v"(acc[0]), "+v"(acc[1]), "+v"(acc[2]), "+v"(acc[3]), "+v"(av[0]), "+v"(av[1]));
        u32x2 o[4];
        {
#pragma unroll
            for (int t = 0; t < 4; ++t) o[t] = (u32x2){pack2(acc[t][0], acc[t][1]), pack2(acc[t][2], acc[t][3])};
            if (fr < 8) {
#pragma unroll
                for (int t = 0; t < 4; ++t) {
                    int row = (t >> 1) * 8 + fr;
                    int c = w * 32 + (t & 1) * 16 + fg * 4;
                    *(u32x2*)(sQK + row * QK_STRIDE + c * 2) = o[t];
                }
            }
        }
        u32x4 am[2], ax[2];
#pragma unroll
        for (int t = 0; t < 2; ++t) {
            am[t] = (u32x4){pack2(av[t][0], av[t][1]), pack2(av[t][2], av[t][3]), 0u, 0u};
            if (fg >= 2) { am[t][0] = 0u; am[t][1] = 0u; }
        }
        bf16x8 kx[2][4];
#pragma unroll
        for (int ks = 0; ks < 4; ++ks) {
            kx[0][ks] = *(const bf16x8*)(sKV + ks * 1024 + l16);
            kx[1][ks] = *(const bf16x8*)(sKV + 4096 + ks * 1024 + l16);
        }
        ax[0] = *(const u32x4*)(sKV + 8192 + (w * 2) * 1024 + l16);
        ax[1] = *(const u32x4*)(sKV + 8192 + (w * 2 + 1) * 1024 + l16);
        STAMP(4);
        BARRIER();
        STAMP(5);
        f32x4 sm, sx0, sx1;
        {
            bf16x8 qf[4], kf[4];
#pragma unroll
            for (int ks = 0; ks < 4; ++ks) {
                qf[ks] = *(const bf16x8*)(qrd + ks * 64);
                kf[ks] = *(const bf16x8*)(qrd + 8 * QK_STRIDE + ks * 64);
            }
            MFMA0_V(sx0, kx[0][0], qf[0]); MFMA0_V(sx1, kx[1][0], qf[0]); MFMA0_V(sm, kf[0], qf[0]);
#pragma unroll
            for (int ks = 1; ks < 4; ++ks) { MFMA_V(sx0, kx[0][ks], qf[ks]); MFMA_V(sx1, kx[1][ks], qf[ks]); MFMA_V(sm, kf[ks], qf[ks]); }
        }
        STAMP(10);
        if (fr < 8) {
                bst8_sc1(rW, (unsigned)fr * 256 + fg * 8, ((unsigned)((s * NH + h) * 2048 + (w * 32) * 2)) + (unsigned)OFF_km, o[2]);
                bst8_sc1(rW, (unsigned)fr * 256 + fg * 8, ((unsigned)((s * NH + h) * 2048 + (w * 32 + 16) * 2)) + (unsigned)OFF_km, o[3]);
        }
        if (fg < 2) {
            bst8_sc1(rW, (unsigned)fr * 16 + fg * 8, ((unsigned)((s * NH + h) * 2048 + (w * 32) * 16)) + (unsigned)OFF_vmT, (u32x2){am[0][0], am[0][1]});
            bst8_sc1(rW, (unsigned)fr * 16 + fg * 8, ((unsigned)((s * NH + h) * 2048 + (w * 32 + 16) * 16)) + (unsigned)OFF_vmT, (u32x2){am[1][0], am[1][1]});
        }
        bf16x8 wf[8];
#pragma unroll
        for (int i = 0; i < 8; ++i) wf[i] = *(const bf16x8*)(lw + (24 + i) * 1024);
        asm volatile("s_nop 7" : "+v"(sm), "+v"(sx0), "+v"(sx1));
        const bool hi = fr >= 8;
        const f32x2 sc2 = {SCALE, SCALE};
        f32x2 p0 = {hi ? sx1[0] : sx0[0], hi ? sx1[1] : sx0[1]};
        f32x2 p1 = {hi ? sx1[2] : sx0[2], hi ? sx1[3] : sx0[3]};
        f32x2 p2 = {hi ? sm[2] : sm[0], hi ? sm[3] : sm[1]};
        p0 = rbf2(rbf2(p0) * sc2);
        p1 = rbf2(rbf2(p1) * sc2);
        p2 = rbf2(rbf2(p2) * sc2);
        if (fg >= 2) p2 = (f32x2){-3.0e38f, -3.0e38f};
        float mx = fmaxf(fmaxf(fmaxf(p0[0], p0[1]), fmaxf(p1[0], p1[1])), fmaxf(p2[0], p2[1]));
        mx = fmaxf(mx, ror8(mx));
        mx = xmax16(mx);
        mx = xmax32(mx);
        const float L2E = 1.4426950408889634f;
        const f32x2 l2 = {L2E, L2E};
        const f32x2 mxl = {mx, mx};
        p0 = (p0 - mxl) * l2; p1 = (p1 - mxl) * l2; p2 = (p2 - mxl) * l2;
        p0 = (f32x2){__builtin_amdgcn_exp2f(p0[0]), __builtin_amdgcn_exp2f(p0[1])};
        p1 = (f32x2){__builtin_amdgcn_exp2f(p1[0]), __builtin_amdgcn_exp2f(p1[1])};
        p2 = (f32x2){__builtin_amdgcn_exp2f(p2[0]), __builtin_amdgcn_exp2f(p2[1])};
        f32x2 s2 = (p0 + p1) + p2;
        float sum = s2[0] + s2[1];
        sum = sum + ror8(sum);
        sum = xsum16(sum);
        sum = xsum32(sum);
        float inv = __builtin_amdgcn_rcpf(sum);
        const f32x2 iv = {inv, inv};
        p0 *= iv; p1 *= iv; p2 *= iv;
        unsigned d0 = pack2(p0[0], p0[1]), d1 = pack2(p1[0], p1[1]), d2 = pack2(p2[0], p2[1]);
        unsigned e0 = ror8u(d0), e1 = ror8u(d1), e2 = ror8u(d2);
        u32x4 px = {d0, d1, e0, e1};
        u32x4 pm = {d2, e2, 0u, 0u};
        STAMP(12);
        f32x4 o0, o1;
        asm volatile("s_nop 1" : "+v"(px), "+v"(pm), "+v"(am[0]), "+v"(am[1]), "+v"(ax[0]), "+v"(ax[1]));
        MFMA0_V(o0, ax[0], px); MFMA0_V(o1, ax[1], px);
        MFMA_V(o0, am[0], pm); MFMA_V(o1, am[1], pm);
        asm volatile("s_nop 7" : "+v"(o0), "+v"(o1));
        if (fr < 8) {
            *(u32x2*)(sO + fr * QK_STRIDE + (w * 32 + fg * 4) * 2) = (u32x2){pack2(o0[0], o0[1]), pack2(o0[2], o0[3])};
            *(u32x2*)(sO + fr * QK_STRIDE + (w * 32 + 16 + fg * 4) * 2) = (u32x2){pack2(o1[0], o1[1]), pack2(o1[2], o1[3])};
        }
        STAMP(6);
        BARRIER();
        STAMP(7);
        bf16x8 of[4];
#pragma unroll
        for (int ks = 0; ks < 4; ++ks) of[ks] = *(const bf16x8*)(ord_ + ks * 64);
        {
            f32x4 a[4];
#pragma unroll
            for (int p = 0; p <= 4; ++p) {
                if (p < 4) {
#pragma unroll
                    for (int u = 0; u < 2; ++u) {
                        const int t = 2 * p + u;
                        f32x4& ac = a[(p & 1) * 2 + u];
#pragma unroll
                        for (int ks = 0; ks < 4; ++ks) {
                            if (t < 4) { if (ks == 0) MFMA0_A(ac, wo[t * 4 + ks], of[ks]); else MFMA_A(ac, wo[t * 4 + ks], of[ks]); }
                            else {
                                bf16x8 f = (t < 6) ? wo[t * 4 + ks] : wf[(t - 6) * 4 + ks];
                                if (ks == 0) MFMA0_V(ac, f, of[ks]); else MFMA_V(ac, f, of[ks]);
                            }
                        }
                    }
                }
                if (p >= 1) {
                    const int q0 = ((p - 1) & 1) * 2;
                    if (p == 4) asm volatile("s_nop 7" : "+v"(a[q0]), "+v"(a[q0 + 1]));
                    u32x4 o;
#pragma unroll
                    for (int q = 0; q < 4; ++q) o[q] = __float_as_uint(fr < 8 ? a[q0][q] : a[q0 + 1][q]) | 1u;
                    bst16_sc1(rW, stA_vo, ((unsigned)(((w * 8 + 2 * (p - 1)) * NH + h) * 512) + (unsigned)s * EXA_STEP) + (unsigned)OFF_exA, o);
                }
            }
        }
        STAMP(8);
                if (s > 0 && tid == 192) __builtin_amdgcn_raw_buffer_store_b32(1u, rW, 0, (unsigned)OFF_flagMr + (unsigned)(((s - 1) * 8 + (h >> 2)) * 32 + (h & 3)) * 4u, 16);
        {
#pragma unroll 1
            for (int d = 0; d < DELAY_A; ++d) __builtin_amdgcn_s_sleep(1);
            float tot[4];
            for (unsigned spin = 0;; ++spin) {
                if (spin > (1u << 20)) break;
                asm volatile("" ::: "memory");
                u32x4 v[4];
#pragma unroll
                for (int i = 0; i < 4; ++i) v[i] = bld16_sc1(rW, rdA_vo, ((unsigned)(h * NH * 512 + i * 4096 + w * 128) + (unsigned)s * EXA_STEP) + (unsigned)OFF_exA);
                bool ok = true;
#pragma unroll
                for (int q = 0; q < 4; ++q) {
                    ok &= ((v[0][q] & v[1][q] & v[2][q] & v[3][q] & 1u) != 0);
                    tot[q] = (__uint_as_float(v[0][q]) + __uint_as_float(v[1][q])) + (__uint_as_float(v[2][q]) + __uint_as_float(v[3][q]));
                }
                if (__all(ok)) break;
            }
#pragma unroll
            for (int q = 0; q < 4; ++q) {
                tot[q] = xsum8(tot[q]);
                tot[q] = xsum16(tot[q]);
                tot[q] = xsum32(tot[q]);
            }
            STAMP(9);
            if (lane < 8) {
                u32x4 o;
#pragma unroll
                for (int q = 0; q < 4; ++q) o[q] = ((unsigned)f2bf(tot[q]) << 16) | 1u;
                bst16_sc1(rW, l16, ((unsigned)(h * 512 + w * 128) + (unsigned)(s + 1) * EXB_STEP) + (unsigned)OFF_exB, o);
            }
        }
    }
    asm volatile("s_waitcnt vmcnt(0)" ::: "memory");
    __syncthreads();
    if (tid == 0) __builtin_amdgcn_raw_buffer_store_b32(1u, rW, 0, (unsigned)OFF_flagMr + (unsigned)(((NSTEPS - 1) * 8 + (h >> 2)) * 32 + (h & 3)) * 4u, 16);
}

enum { EPI_QKV = 0, EPI_H = 1, EPI_G = 2 };
__device__ __forceinline__ float gelu_tanh(float v) {
    float u = 0.7978845608028654f * (v + 0.044715f * v * v * v);
    return 0.5f * v * (1.f + tanhf(u));
}
__device__ __forceinline__ void wait_flag_ge(const unsigned* f, unsigned want) {
    for (unsigned spin = 0; spin < (1u << 22); ++spin) {
        unsigned v = __hip_atomic_load(f, __ATOMIC_RELAXED, __HIP_MEMORY_SCOPE_AGENT);
        if (__all(v >= want)) break;
        __builtin_amdgcn_s_sleep(64);
    }
}
__device__ __forceinline__ void acquire_all() {
    __builtin_amdgcn_fence(__ATOMIC_ACQUIRE, "agent");
    asm volatile("s_waitcnt vmcnt(0)" ::: "memory");
    __syncthreads();
}
__device__ __forceinline__ void publish_done() {
    asm volatile("s_waitcnt vmcnt(0)" ::: "memory");
    __syncthreads();
}

template <int EPI, int NKS>
__device__ __forceinline__ void gemm_tile(unsigned offA, int tb, int cb, const WS& ws, const u16* __restrict__ bias, const int tidx) {
    const int lane = tidx & 63, w = __builtin_amdgcn_readfirstlane(tidx >> 6);
    const int wr = w >> 1, wc = w & 1;
    const int fr = lane & 15, fg = lane >> 4;
    const rsrc_t rW = mk_rsrc(ws.base);
    const unsigned l16 = lane * 16;
    const bool vtile = (EPI == EPI_QKV) && (cb >= 64);
    unsigned ao[4], bo[4];
#pragma unroll
    for (int i = 0; i < 4; ++i) {
        ao[i] = offA + (unsigned)((tb * 8 + wr * 4 + i) * NKS) * 1024u;
        int ct = cb * 8 + wc * 4 + i;
        if (EPI == EPI_QKV) {
            int m = ct >> 8, r = ct & 255;
            bo[i] = (unsigned)OFF_WqkvF + (unsigned)((((r >> 3) * 4 + ((r >> 1) & 3)) * 6 + m * 2 + (r & 1)) * 16) * 1024u;
        } else if (EPI == EPI_H) bo[i] = (unsigned)OFF_W1F + (unsigned)(ct * 16) * 1024u;
        else bo[i] = (unsigned)OFF_W2F + (unsigned)(ct * 44) * 1024u;
    }
    f32x4 acc[4][4];
#pragma unroll
    for (int i = 0; i < 4; ++i)
#pragma unroll
        for (int j = 0; j < 4; ++j) acc[i][j] = (f32x4){0.f, 0.f, 0.f, 0.f};
    constexpr int D = 4;
    u32x4 fa[D][4], fb[D][4];
#pragma unroll
    for (int d = 0; d < D; ++d)
#pragma unroll
        for (int i = 0; i < 4; ++i) { fa[d][i] = bld16(rW, l16, ao[i] + d * 1024u); fb[d][i] = bld16(rW, l16, bo[i] + d * 1024u); }
    for (int k0 = 0; k0 < NKS; k0 += D) {
#pragma unroll
        for (int d = 0; d < D; ++d) {
            u32x4 a[4], bq[4];
#pragma unroll
            for (int i = 0; i < 4; ++i) { a[i] = fa[d][i]; bq[i] = fb[d][i]; }
            const int kn = k0 + d + D;
            if (kn < NKS) {
#pragma unroll
                for (int i = 0; i < 4; ++i) { fa[d][i] = bld16(rW, l16, ao[i] + (unsigned)kn * 1024u); fb[d][i] = bld16(rW, l16, bo[i] + (unsigned)kn * 1024u); }
            }
            if (vtile) {
#pragma unroll
                for (int i = 0; i < 4; ++i)
#pragma unroll
                    for (int j = 0; j < 4; ++j) acc[i][j] = __builtin_amdgcn_mfma_f32_16x16x32_bf16(__builtin_bit_cast(bf16x8, a[i]), __builtin_bit_cast(bf16x8, bq[j]), acc[i][j], 0, 0, 0);
            } else {
#pragma unroll
                for (int i = 0; i < 4; ++i)
#pragma unroll
                    for (int j = 0; j < 4; ++j) acc[i][j] = __builtin_amdgcn_mfma_f32_16x16x32_bf16(__builtin_bit_cast(bf16x8, bq[j]), __builtin_bit_cast(bf16x8, a[i]), acc[i][j], 0, 0, 0);
            }
        }
    }
    const int brow = tb * 128, bcol = cb * 128;
#pragma unroll
    for (int i = 0; i < 4; ++i)
#pragma unroll
        for (int j = 0; j < 4; ++j) {
            f32x4 a = acc[i][j];
            if (vtile) {
                int col = bcol + wc * 64 + j * 16 + fr - 2 * HD;
                int tok = brow + wr * 64 + i * 16 + fg * 4;
                int seg = tok >> 5, tt = (tok >> 4) & 1, hh = col >> 7, et = (col >> 4) & 7;
                u32x2 o = {pack2(a[0], a[1]), pack2(a[2], a[3])};
                bst8_sc1(rW, (unsigned)((((seg * NH + hh) * 8 + et) * 512 + (fg * 16 + fr) * 8 + tt * 4) * 2), (unsigned)OFF_VT, o);
            } else {
                int tok = brow + wr * 64 + i * 16 + fr;
                int col = bcol + wc * 64 + j * 16 + fg * 4;
                if (EPI == EPI_QKV) {
                    int cc = col & (HD - 1);
                    int seg = tok >> 5, tt = (tok >> 4) & 1, hh = cc >> 7, e = cc & 127;
                    u32x2 o = {pack2(a[0], a[1]), pack2(a[2], a[3])};
                    unsigned off = (unsigned)((((seg * NH + hh) * 8 + tt * 4 + (e >> 5)) * 512 + (((e >> 3) & 3) * 16 + (tok & 15)) * 8 + (e & 4)) * 2);
                    bst8_sc1(rW, off, (col >= HD) ? (unsigned)OFF_K : (unsigned)OFF_Q, o);
                } else if (EPI == EPI_H) {
                    float r[4];
#pragma unroll
                    for (int q = 0; q < 4; ++q) {
                        float b = (col + q < FF) ? bf2f(bias[col + q]) : 0.f;
                        float v = rbf(rbf(a[q]) + b);
                        r[q] = (col + q < FF) ? gelu_tanh(v) : 0.f;
                    }
                    u32x2 o = {pack2(r[0], r[1]), pack2(r[2], r[3])};
                    unsigned off = (unsigned)((((tok >> 4) * 44 + (col >> 5)) * 64 + ((col >> 3) & 3) * 16 + (tok & 15)) * 16 + (col & 4) * 2);
                    bst8_sc1(rW, off, (unsigned)OFF_hmidF, o);
                } else {
                    float r[4];
#pragma unroll
                    for (int q = 0; q < 4; ++q) {
                        float v = rbf(rbf(a[q]) + bf2f(bias[col + q]));
                        r[q] = v / (1.f + __expf(-v));
                    }
                    u32x2 o = {pack2(r[0], r[1]), pack2(r[2], r[3])};
                    bst8_sc1(rW, (unsigned)((tok * DM + col) * 2), (unsigned)OFF_g, o);
                }
            }
        }
}

__device__ __forceinline__ u32x2 bld8_sc1(rsrc_t r, unsigned vo, unsigned so) { return __builtin_amdgcn_raw_buffer_load_b64(r, vo, so, 16); }
__device__ __forceinline__ void attn_task(int s, int hg, const WS& ws, char* lds, const int tidx) {
    const int lane = tidx & 63, w = __builtin_amdgcn_readfirstlane(tidx >> 6);
    const int h = hg * 4 + w;
    const int fr = lane & 15, fg = lane >> 4, fr8 = lane & 7;
    char* sO = lds;
    const rsrc_t rW = mk_rsrc(ws.base);
    const unsigned l16 = lane * 16;
    const unsigned sh = (unsigned)(s * NH + h) * 8192u;
    char* ring = lds + 33280 + w * 24576;
    const unsigned wbase = (unsigned)OFF_WoF + (unsigned)((w * 8 * 32 + hg * 4) * 4) * 1024u;
    auto issue = [&](int ks) {
#pragma unroll
        for (int t = 0; t < 8; ++t)
            __builtin_amdgcn_raw_ptr_buffer_load_lds(rW, (LAS void*)(ring + ((ks % 3) * 8 + t) * 1024), 16, l16, wbase + (unsigned)(t * 128 + ks) * 1024u, 0, 0);
    };
    issue(0); issue(1); issue(2);
    u32x4 ax[8];
    f32x4 sx0[2], sx1[2];
    u32x4 qf[2][4];
    {
        u32x4 kx[2][4];
#pragma unroll
        for (int ks = 0; ks < 4; ++ks)
#pragma unroll
            for (int t = 0; t < 2; ++t) { kx[t][ks] = bld16(rW, l16, (unsigned)OFF_K + sh + (unsigned)(t * 4 + ks) * 1024u); qf[t][ks] = bld16(rW, l16, (unsigned)OFF_Q + sh + (unsigned)(t * 4 + ks) * 1024u); }
#pragma unroll
        for (int et = 0; et < 8; ++et) ax[et] = bld16(rW, l16, (unsigned)OFF_VT + sh + (unsigned)et * 1024u);
#pragma unroll
        for (int qt = 0; qt < 2; ++qt) {
            sx0[qt] = (f32x4){0.f, 0.f, 0.f, 0.f}; sx1[qt] = sx0[qt];
#pragma unroll
            for (int ks = 0; ks < 4; ++ks) {
                sx0[qt] = __builtin_amdgcn_mfma_f32_16x16x32_bf16(__builtin_bit_cast(bf16x8, kx[0][ks]), __builtin_bit_cast(bf16x8, qf[qt][ks]), sx0[qt], 0, 0, 0);
                sx1[qt] = __builtin_amdgcn_mfma_f32_16x16x32_bf16(__builtin_bit_cast(bf16x8, kx[1][ks]), __builtin_bit_cast(bf16x8, qf[qt][ks]), sx1[qt], 0, 0, 0);
            }
        }
    }
    if (w == 0) {
        const unsigned* f = ws.flagM_() + (s * 8 + hg) * 32 + (lane & 3);
        for (unsigned spin = 0; spin < (1u << 22); ++spin) {
            unsigned v = __hip_atomic_load(f, __ATOMIC_RELAXED, __HIP_MEMORY_SCOPE_AGENT);
            if (__all(v != 0)) break;
            __builtin_amdgcn_s_sleep(16);
        }
    }
    asm volatile("" ::: "memory");
    __builtin_amdgcn_s_barrier();
    asm volatile("" ::: "memory");
    u32x4 kmf[4];
    u32x2 vm[8];
#pragma unroll
    for (int ks = 0; ks < 4; ++ks) kmf[ks] = bld16_sc1(rW, (unsigned)fr8 * 256 + fg * 16, (unsigned)OFF_km + (unsigned)(s * NH + h) * 2048u + ks * 64u);
#pragma unroll
    for (int et = 0; et < 8; ++et) {
        vm[et] = (u32x2){0u, 0u};
        if (fg < 2) vm[et] = bld8_sc1(rW, (unsigned)fr * 16 + fg * 8, (unsigned)OFF_vmT + (unsigned)(s * NH + h) * 2048u + et * 256u);
    }
    u32x4 px[2], pm[2];
#pragma unroll
    for (int qt = 0; qt < 2; ++qt) {
        f32x4 sm = {0.f, 0.f, 0.f, 0.f};
#pragma unroll
        for (int ks = 0; ks < 4; ++ks) sm = __builtin_amdgcn_mfma_f32_16x16x32_bf16(__builtin_bit_cast(bf16x8, kmf[ks]), __builtin_bit_cast(bf16x8, qf[qt][ks]), sm, 0, 0, 0);
        const int qi = qt * 16 + fr;
        float sv[12];
#pragma unroll
        for (int q = 0; q < 4; ++q) {
            sv[q] = (fg < 2) ? rbf(rbf(sm[q]) * SCALE) : -3.0e38f;
            int k0 = fg * 4 + q, k1 = 16 + fg * 4 + q;
            sv[4 + q] = (k0 <= qi) ? rbf(rbf(sx0[qt][q]) * SCALE) : -3.0e38f;
            sv[8 + q] = (k1 <= qi) ? rbf(rbf(sx1[qt][q]) * SCALE) : -3.0e38f;
        }
        float mx = sv[0];
#pragma unroll
        for (int q = 1; q < 12; ++q) mx = fmaxf(mx, sv[q]);
        mx = xmax16(mx); mx = xmax32(mx);
        float sum = 0.f;
#pragma unroll
        for (int q = 0; q < 12; ++q) { sv[q] = __expf(sv[q] - mx); sum += sv[q]; }
        sum = xsum16(sum); sum = xsum32(sum);
        float inv = 1.f / sum;
        px[qt] = (u32x4){pack2(sv[4] * inv, sv[5] * inv), pack2(sv[6] * inv, sv[7] * inv), pack2(sv[8] * inv, sv[9] * inv), pack2(sv[10] * inv, sv[11] * inv)};
        pm[qt] = (u32x4){pack2(sv[0] * inv, sv[1] * inv), pack2(sv[2] * inv, sv[3] * inv), 0u, 0u};
    }
#pragma unroll
    for (int et = 0; et < 8; ++et) {
        u32x4 am = {vm[et][0], vm[et][1], 0u, 0u};
#pragma unroll
        for (int qt = 0; qt < 2; ++qt) {
            f32x4 o = {0.f, 0.f, 0.f, 0.f};
            o = __builtin_amdgcn_mfma_f32_16x16x32_bf16(__builtin_bit_cast(bf16x8, ax[et]), __builtin_bit_cast(bf16x8, px[qt]), o, 0, 0, 0);
            o = __builtin_amdgcn_mfma_f32_16x16x32_bf16(__builtin_bit_cast(bf16x8, am), __builtin_bit_cast(bf16x8, pm[qt]), o, 0, 0, 0);
            u32x2 ov = {pack2(o[0], o[1]), pack2(o[2], o[3])};
            *(u32x2*)(sO + (qt * 16 + fr) * 1040 + (w * DH + et * 16 + fg * 4) * 2) = ov;
        }
    }
    asm volatile("s_waitcnt lgkmcnt(0)\n\ts_barrier" ::: "memory");
    f32x4 acc[8][2];
#pragma unroll
    for (int t = 0; t < 8; ++t) { acc[t][0] = (f32x4){0.f, 0.f, 0.f, 0.f}; acc[t][1] = acc[t][0]; }
    {
#pragma unroll
        for (int ks = 0; ks < 16; ++ks) {
            if (ks <= 13) asm volatile("s_waitcnt vmcnt(16)" ::: "memory");
            else if (ks == 14) asm volatile("s_waitcnt vmcnt(8)" ::: "memory");
            else asm volatile("s_waitcnt vmcnt(0)" ::: "memory");
            bf16x8 b0 = *(const bf16x8*)(sO + fr * 1040 + (ks * 32 + fg * 8) * 2);
            bf16x8 b1 = *(const bf16x8*)(sO + (16 + fr) * 1040 + (ks * 32 + fg * 8) * 2);
            bf16x8 a[8];
#pragma unroll
            for (int t = 0; t < 8; ++t) a[t] = *(const bf16x8*)(ring + ((ks % 3) * 8 + t) * 1024 + lane * 16);
            if (ks + 3 < 16) {
                asm volatile("s_waitcnt lgkmcnt(0)" ::: "memory");
                issue(ks + 3);
            }
#pragma unroll
            for (int t = 0; t < 8; ++t) {
                acc[t][0] = __builtin_amdgcn_mfma_f32_16x16x32_bf16(a[t], b0, acc[t][0], 0, 0, 0);
                acc[t][1] = __builtin_amdgcn_mfma_f32_16x16x32_bf16(a[t], b1, acc[t][1], 0, 0, 0);
            }
        }
    }
#pragma unroll
    for (int t = 0; t < 8; ++t)
#pragma unroll
        for (int qt = 0; qt < 2; ++qt) {
            u32x4 o = {__float_as_uint(acc[t][qt][0]), __float_as_uint(acc[t][qt][1]), __float_as_uint(acc[t][qt][2]), __float_as_uint(acc[t][qt][3])};
            unsigned off = (unsigned)((((s * 8 + hg) * 32 + qt * 16 + fr) * 512 + w * 128 + t * 16 + fg * 4) * 4);
            bst16_sc1(rW, off, (unsigned)OFF_part, o);
        }
}

__device__ __forceinline__ void reduce_task(int s, int r0, const u16* __restrict__ x, u16* __restrict__ out, const WS& ws, const int tidx) {
    const int i = tidx;
    {
        int row = r0 + (i >> 7), c = (i & 127) * 4;
        f32x4 t = {0.f, 0.f, 0.f, 0.f};
        f32x4 pv[8];
#pragma unroll
        for (int hg = 0; hg < 8; ++hg) pv[hg] = *(const f32x4*)(ws.part_() + ((size_t)(s * 8 + hg) * 32 + row) * 512 + c);
#pragma unroll
        for (int hg = 0; hg < 8; ++hg) t += pv[hg];
        int tok = s * SEG + row;
        u32x2 xv = *(const u32x2*)(x + (size_t)tok * DM + c);
        u32x2 gv = *(const u32x2*)(ws.g_() + (size_t)tok * DM + c);
        float xs[4] = {bf2f(xv[0] & 0xffff), bf2f(xv[0] >> 16), bf2f(xv[1] & 0xffff), bf2f(xv[1] >> 16)};
        float gs[4] = {bf2f(gv[0] & 0xffff), bf2f(gv[0] >> 16), bf2f(gv[1] & 0xffff), bf2f(gv[1] >> 16)};
        float r[4];
#pragma unroll
        for (int q = 0; q < 4; ++q) r[q] = xs[q] + rbf(rbf(t[q]) * gs[q]);
        u32x2 o = {pack2(r[0], r[1]), pack2(r[2], r[3])};
        *(u32x2*)(out + (size_t)tok * DM + c) = o;
    }
}

#define NWORK 224
#define T_KV0 64
#define T_Z 64
#define T_KV 960
#define T_H 176
#define T_Q 512
#define T_G 64
#define T_A 512
#define T_TOTAL (T_KV0 + T_Z + T_KV + T_H + T_Q + T_G + T_A)
#define LDS_BYTES 162816

__global__ void __launch_bounds__(256, 1) mega_k(const u16* __restrict__ x, const u16* __restrict__ mem0, const u16* __restrict__ b1, const u16* __restrict__ b2,
                                                 u16* __restrict__ out, WS ws) {
    extern __shared__ __attribute__((aligned(16))) char smem[];
    if (blockIdx.x < NH) {
        chain_body(mem0, ws, smem, blockIdx.x);
        return;
    }
    const int tid = threadIdx.x, lane = tid & 63, w = tid >> 6;
    for (int task = blockIdx.x - NH; task < T_TOTAL; task += NWORK) {
        int t = task;
        int tidx = threadIdx.x;
        asm volatile("" : "+v"(tidx));
        if (t < T_KV0 + T_Z + T_KV) {
            if (t >= T_KV0 && t < T_KV0 + T_Z) {
                const int z = t - T_KV0;
                const rsrc_t rW = mk_rsrc(ws.base);
                u32x4 zz = {0u, 0u, 0u, 0u};
#pragma unroll 8
                for (int i = 0; i < 128; ++i) bst16_sc1(rW, (unsigned)tidx * 16, (unsigned)OFF_exA + (unsigned)z * 524288u + (unsigned)i * 4096u, zz);
                publish_done();
                if (tid == 0) __hip_atomic_store(ws.flagZ_() + z, 1u, __ATOMIC_RELAXED, __HIP_MEMORY_SCOPE_AGENT);
                continue;
            }
            if (t >= T_KV0) t -= T_Z;
            int tb = t >> 6, cb = t & 63;
            gemm_tile<EPI_QKV, 16>((unsigned)OFF_xnF, tb, 32 + cb, ws, nullptr, tidx);
            publish_done();
            if (tid == 0) __hip_atomic_store(ws.flagKV_() + tb * 64 + cb, 1u, __ATOMIC_RELAXED, __HIP_MEMORY_SCOPE_AGENT);
            continue;
        }
        t -= T_KV0 + T_Z + T_KV;
        if (t < T_H) {
            int tb = t / 11, cb = t % 11;
            gemm_tile<EPI_H, 16>((unsigned)OFF_yF, tb, cb, ws, b1, tidx);
            publish_done();
            if (tid == 0) __hip_atomic_fetch_add(ws.cntH_() + tb, 1u, __ATOMIC_RELAXED, __HIP_MEMORY_SCOPE_AGENT);
            continue;
        }
        t -= T_H;
        if (t < T_Q) {
            int tb = t >> 5, cb = t & 31;
            gemm_tile<EPI_QKV, 16>((unsigned)OFF_xnF, tb, cb, ws, nullptr, tidx);
            publish_done();
            if (tid == 0) __hip_atomic_store(ws.flagQ_() + tb * 32 + cb, 1u, __ATOMIC_RELAXED, __HIP_MEMORY_SCOPE_AGENT);
            continue;
        }
        t -= T_Q;
        if (t < T_G) {
            int tb = t >> 2, cb = t & 3;
            if (w == 0) wait_flag_ge(ws.cntH_() + tb, 11u);
            acquire_all();
            gemm_tile<EPI_G, 44>((unsigned)OFF_hmidF, tb, cb, ws, b2, tidx);
            publish_done();
            if (tid == 0) __hip_atomic_store(ws.flagG_() + tb * 4 + cb, 1u, __ATOMIC_RELAXED, __HIP_MEMORY_SCOPE_AGENT);
            continue;
        }
        t -= T_G;
        {
            int s = t >> 3, hg = t & 7, tb = s >> 2;
            if (w == 0) {
                const unsigned* f;
                int l = lane & 15, hh = hg * 4 + (l & 3);
                if (l < 4) f = ws.flagKV_() + tb * 64 + hh;
                else if (l < 8) f = ws.flagKV_() + tb * 64 + 32 + hh;
                else f = ws.flagQ_() + tb * 32 + hh;
                wait_flag_ge(f, 1u);
            }
            acquire_all();
            attn_task(s, hg, ws, smem, tidx);
            publish_done();
            if (tid == 0) __hip_atomic_fetch_add(ws.cntO_() + s, 1u, __ATOMIC_RELAXED, __HIP_MEMORY_SCOPE_AGENT);
            if (w == 0) { wait_flag_ge(lane < 4 ? ws.flagG_() + tb * 4 + lane : ws.cntO_() + s, lane < 4 ? 1u : 8u); }
            acquire_all();
            reduce_task(s, hg * 4, x, out, ws, tidx);
            reduce_task(s, hg * 4 + 2, x, out, ws, tidx);
            __syncthreads();
        }
    }
}

extern "C" void kernel_launch(void* const* d_in, const int* in_sizes, int n_in, void* d_out, int out_size, void* d_ws, size_t ws_size,
                              hipStream_t stream) {
    const u16* x = (const u16*)d_in[0];
    const u16* mem0 = (const u16*)d_in[1];
    const u16* Wq = (const u16*)d_in[2];
    const u16* Wk = (const u16*)d_in[3];
    const u16* Wv = (const u16*)d_in[4];
    const u16* Wo = (const u16*)d_in[5];
    const u16* w1 = (const u16*)d_in[8];
    const u16* b1 = (const u16*)d_in[9];
    const u16* w2 = (const u16*)d_in[10];
    const u16* b2 = (const u16*)d_in[11];
    u16* out = (u16*)d_out;

    WS ws; ws.base = (char*)d_ws;

    const int nprep = PREP_LN + PREP_TQKV + PREP_TWO + PREP_TW1 + PREP_TW2 + PREP_ZERO;
    prep_k<<<nprep, 256, 0, stream>>>(x, Wq, Wk, Wv, Wo, w1, w2, ws);
    (void)hipFuncSetAttribute((const void*)mega_k, hipFuncAttributeMaxDynamicSharedMemorySize, LDS_BYTES);
    mega_k<<<256, 256, LDS_BYTES, stream>>>(x, mem0, b1, b2, out, ws);
}
```

```cpp
#include <hip/hip_runtime.h>
#include <hip/hip_bf16.h>
#include <stdint.h>

typedef __attribute__((ext_vector_type(8))) short bf16x8;
typedef __attribute__((ext_vector_type(4))) float f32x4;
typedef __attribute__((ext_vector_type(4))) unsigned u32x4;
typedef __attribute__((ext_vector_type(2))) unsigned u32x2;
typedef unsigned short u16;

#define S_TOK 2048
#define DM 512
#define NH 32
#define DH 128
#define HD 4096
#define NMEM 8
#define SEG 32
#define NSEG 64
#define NSTEPS 64
#define FF 1376
#define FFP 1408

__device__ __forceinline__ u16 f2bf(float f) { return __builtin_bit_cast(u16, (__bf16)f); }
__device__ __forceinline__ float bf2f(u16 h) { return __builtin_bit_cast(float, (unsigned)h << 16); }
__device__ __forceinline__ float rbf(float f) { return bf2f(f2bf(f)); }
typedef __attribute__((ext_vector_type(2))) float f32x2;
typedef __attribute__((ext_vector_type(2))) __bf16 bf16x2v;
__device__ __forceinline__ unsigned pack2(float a, float b) { f32x2 v = {a, b}; return __builtin_bit_cast(unsigned, __builtin_convertvector(v, bf16x2v)); }
__device__ __forceinline__ f32x2 rbf2(f32x2 v) { unsigned u = __builtin_bit_cast(unsigned, __builtin_convertvector(v, bf16x2v)); f32x2 r = {__uint_as_float(u << 16), __uint_as_float(u & 0xffff0000u)}; return r; }

constexpr size_t OFF_flags = 0;
constexpr size_t OFF_flagMr = OFF_flags + 65536;
constexpr size_t OFF_exB = OFF_flagMr + 65536;
constexpr size_t OFF_exA = OFF_exB + (((size_t)68 * 16384) + 255) / 256 * 256;
constexpr size_t OFF_xnF = OFF_exA + (((size_t)64 * 524288) + 255) / 256 * 256;
constexpr size_t OFF_yF = OFF_xnF + (((size_t)S_TOK * DM * 2) + 255) / 256 * 256;
constexpr size_t OFF_WqkvF = OFF_yF + (((size_t)S_TOK * DM * 2) + 255) / 256 * 256;
constexpr size_t OFF_WoF = OFF_WqkvF + (((size_t)3 * HD * DM * 2) + 255) / 256 * 256;
constexpr size_t OFF_W1F = OFF_WoF + (((size_t)DM * HD * 2) + 255) / 256 * 256;
constexpr size_t OFF_W2F = OFF_W1F + (((size_t)FFP * DM * 2) + 255) / 256 * 256;
constexpr size_t OFF_Q = OFF_W2F + (((size_t)DM * FFP * 2) + 255) / 256 * 256;
constexpr size_t OFF_K = OFF_Q + (((size_t)S_TOK * HD * 2) + 255) / 256 * 256;
constexpr size_t OFF_VT = OFF_K + (((size_t)S_TOK * HD * 2) + 255) / 256 * 256;
constexpr size_t OFF_hmidF = OFF_VT + (((size_t)S_TOK * HD * 2) + 255) / 256 * 256;
constexpr size_t OFF_g = OFF_hmidF + (((size_t)S_TOK * FFP * 2) + 255) / 256 * 256;
constexpr size_t OFF_km = OFF_g + (((size_t)S_TOK * DM * 2) + 255) / 256 * 256;
constexpr size_t OFF_vmT = OFF_km + (((size_t)NSEG * NH * 8 * DH * 2) + 255) / 256 * 256;
constexpr size_t OFF_part = OFF_vmT + (((size_t)NSEG * NH * 8 * DH * 2) + 255) / 256 * 256;
constexpr size_t OFF_dbg = (size_t)240 * 1024 * 1024;
struct WS {
    char* base;
    __host__ __device__ unsigned* flags_() const { return (unsigned*)(base + OFF_flags); }
    __host__ __device__ unsigned* exB_() const { return (unsigned*)(base + OFF_exB); }
    __host__ __device__ unsigned* exA_() const { return (unsigned*)(base + OFF_exA); }
    __host__ __device__ u16* xnF_() const { return (u16*)(base + OFF_xnF); }
    __host__ __device__ u16* yF_() const { return (u16*)(base + OFF_yF); }
    __host__ __device__ u16* WqkvF_() const { return (u16*)(base + OFF_WqkvF); }
    __host__ __device__ u16* WoF_() const { return (u16*)(base + OFF_WoF); }
    __host__ __device__ u16* W1F_() const { return (u16*)(base + OFF_W1F); }
    __host__ __device__ u16* W2F_() const { return (u16*)(base + OFF_W2F); }
    __host__ __device__ u16* Q_() const { return (u16*)(base + OFF_Q); }
    __host__ __device__ u16* K_() const { return (u16*)(base + OFF_K); }
    __host__ __device__ u16* VT_() const { return (u16*)(base + OFF_VT); }
    __host__ __device__ u16* hmidF_() const { return (u16*)(base + OFF_hmidF); }
    __host__ __device__ u16* g_() const { return (u16*)(base + OFF_g); }
    __host__ __device__ u16* km_() const { return (u16*)(base + OFF_km); }
    __host__ __device__ u16* vmT_() const { return (u16*)(base + OFF_vmT); }
    __host__ __device__ float* part_() const { return (float*)(base + OFF_part); }
    __host__ __device__ unsigned* flagKV_() const { return flags_(); }
    __host__ __device__ unsigned* flagQ_() const { return flags_() + 1024; }
    __host__ __device__ unsigned* cntH_() const { return flags_() + 1536; }
    __host__ __device__ unsigned* flagG_() const { return flags_() + 1600; }
    __host__ __device__ unsigned* flagZ_() const { return flags_() + 1792; }
    __host__ __device__ unsigned* flagM_() const { return (unsigned*)(base + OFF_flagMr); }
    __host__ __device__ unsigned* cntO_() const { return flags_() + 4096; }
    __host__ __device__ unsigned long long* dbg_() const { return (unsigned long long*)(base + OFF_dbg); }
};

__device__ __forceinline__ float wave_sum(float v) {
#pragma unroll
    for (int m = 32; m >= 1; m >>= 1) v += __shfl_xor(v, m);
    return v;
}
__device__ __forceinline__ void ln8(float (&v)[8]) {
    float s = 0.f;
#pragma unroll
    for (int i = 0; i < 8; ++i) s += v[i];
    s = wave_sum(s);
    float mean = s * (1.f / 512.f);
    float q = 0.f;
#pragma unroll
    for (int i = 0; i < 8; ++i) { float d = v[i] - mean; q += d * d; }
    q = wave_sum(q);
    float var = rbf(q * (1.f / 512.f));
    float mb = rbf(mean);
    float sd = rbf(sqrtf(rbf(var + 1e-5f)));
#pragma unroll
    for (int i = 0; i < 8; ++i) v[i] = rbf(rbf(v[i] - mb) / sd);
}
template <class F>
__device__ __forceinline__ void frag_tile(const u16* __restrict__ src, int R, int C, int bx, int by, u16 (*t)[72], F addr) {
    const int c0 = bx * 64, r0 = by * 64;
#pragma unroll
    for (int i = 0; i < 2; ++i) {
        int idx = threadIdx.x + i * 256, rr = idx >> 3, cc = (idx & 7) * 8;
        int r = r0 + rr, c = c0 + cc;
        u32x4 v = {0u, 0u, 0u, 0u};
        if (r < R && c < C) v = *(const u32x4*)(src + (size_t)r * C + c);
        *(u32x4*)(&t[rr][cc]) = v;
    }
    __syncthreads();
    int l = threadIdx.x & 63, q = threadIdx.x >> 6;
#pragma unroll
    for (int i = 0; i < 2; ++i) {
        int f = q * 2 + i, ksl = f >> 2, ct = f & 3;
        u16 v[8];
#pragma unroll
        for (int j = 0; j < 8; ++j) v[j] = t[ksl * 32 + (l >> 4) * 8 + j][ct * 16 + (l & 15)];
        u32x4 o = {(unsigned)v[0] | ((unsigned)v[1] << 16), (unsigned)v[2] | ((unsigned)v[3] << 16), (unsigned)v[4] | ((unsigned)v[5] << 16), (unsigned)v[6] | ((unsigned)v[7] << 16)};
        *(u32x4*)(addr(ksl, ct) + l * 8) = o;
    }
}
#define PREP_LN 512
#define PREP_TQKV 1536
#define PREP_TWO 512
#define PREP_TW1 176
#define PREP_TW2 176
#define ZERO_BYTES ((size_t)68 * 16384 + 2 * 65536)
#define PREP_ZERO ((int)(ZERO_BYTES / 65536))
__global__ void __launch_bounds__(256) prep_k(const u16* __restrict__ x, const u16* __restrict__ Wq, const u16* __restrict__ Wk, const u16* __restrict__ Wv,
                                              const u16* __restrict__ Wo, const u16* __restrict__ w1, const u16* __restrict__ w2, WS ws) {
    __shared__ __attribute__((aligned(16))) u16 t[64][72];
    int b = blockIdx.x;
    if (b < PREP_LN) {
        int row = b * 4 + (threadIdx.x >> 6);
        int lane = threadIdx.x & 63;
        u32x4 r = *(const u32x4*)(x + (size_t)row * DM + lane * 8);
        float v[8];
#pragma unroll
        for (int i = 0; i < 4; ++i) { v[2 * i] = bf2f(r[i] & 0xffff); v[2 * i + 1] = bf2f(r[i] >> 16); }
        ln8(v);
        u32x4 o;
#pragma unroll
        for (int i = 0; i < 4; ++i) o[i] = pack2(v[2 * i], v[2 * i + 1]);
        const size_t fo = ((size_t)((row >> 4) * 16 + (lane >> 2)) * 64 + (lane & 3) * 16 + (row & 15)) * 8;
        *(u32x4*)(ws.xnF_() + fo) = o;
        ln8(v);
#pragma unroll
        for (int i = 0; i < 4; ++i) o[i] = pack2(v[2 * i], v[2 * i + 1]);
        *(u32x4*)(ws.yF_() + fo) = o;
        return;
    }
    b -= PREP_LN;
    if (b < PREP_TQKV) {
        int m = b / 512, r = b % 512;
        const u16* src = m == 0 ? Wq : (m == 1 ? Wk : Wv);
        const int bx = r % 64, by = r / 64;
        frag_tile(src, DM, HD, bx, by, t, [&](int ksl, int ct) {
            int n = bx * 64 + ct * 16, ks = by * 2 + ksl;
            int hh = n >> 7, ww = (n >> 5) & 3, tt = (n >> 4) & 1;
            return ws.WqkvF_() + ((size_t)(((hh * 4 + ww) * 6 + m * 2 + tt) * 16 + ks)) * 512;
        });
        return;
    }
    b -= PREP_TQKV;
    if (b < PREP_TWO) {
        const int bx = b % 8, by = b / 8;
        frag_tile(Wo, HD, DM, bx, by, t, [&](int ksl, int ct) {
            int tile = bx * 4 + ct, k = by * 64 + ksl * 32;
            return ws.WoF_() + ((size_t)((tile * 32 + (k >> 7)) * 4 + ((k >> 5) & 3))) * 512;
        });
        return;
    }
    b -= PREP_TWO;
    if (b < PREP_TW1) {
        const int bx = b % 22, by = b / 22;
        frag_tile(w1, DM, FF, bx, by, t, [&](int ksl, int ct) { return ws.W1F_() + ((size_t)((bx * 4 + ct) * 16 + by * 2 + ksl)) * 512; });
        return;
    }
    b -= PREP_TW1;
    if (b < PREP_TW2) {
        const int bx = b % 8, by = b / 8;
        frag_tile(w2, FF, DM, bx, by, t, [&](int ksl, int ct) { return ws.W2F_() + ((size_t)((bx * 4 + ct) * 44 + by * 2 + ksl)) * 512; });
        return;
    }
    b -= PREP_TW2;
    {
        u32x4 z = {0u, 0u, 0u, 0u};
        u32x4* p = (u32x4*)(ws.base + (size_t)b * 65536) + threadIdx.x;
#pragma unroll
        for (int i = 0; i < 16; ++i) p[i * 256] = z;
    }
}

#define SCALE 0.08838834764831845f
#define MEM_STRIDE 1056
#define QK_STRIDE 288

typedef __amdgpu_buffer_rsrc_t rsrc_t;
__device__ __forceinline__ rsrc_t mk_rsrc(const void* p) { return __builtin_amdgcn_make_buffer_rsrc((void*)p, 0, 0x7fffffff, 0x00020000); }
__device__ __forceinline__ u32x4 bld16(rsrc_t r, unsigned vo, unsigned so) { return __builtin_amdgcn_raw_buffer_load_b128(r, vo, so, 0); }
__device__ __forceinline__ u32x4 bld16_sc1(rsrc_t r, unsigned vo, unsigned so) { return __builtin_amdgcn_raw_buffer_load_b128(r, vo, so, 16); }
__device__ __forceinline__ u32x2 bld8(rsrc_t r, unsigned vo, unsigned so) { return __builtin_amdgcn_raw_buffer_load_b64(r, vo, so, 0); }
__device__ __forceinline__ void bst16_sc1(rsrc_t r, unsigned vo, unsigned so, u32x4 v) { __builtin_amdgcn_raw_buffer_store_b128(v, r, vo, so, 16); }
__device__ __forceinline__ void bst8(rsrc_t r, unsigned vo, unsigned so, u32x2 v) { __builtin_amdgcn_raw_buffer_store_b64(v, r, vo, so, 0); }
__device__ __forceinline__ void bst8_sc1(rsrc_t r, unsigned vo, unsigned so, u32x2 v) { __builtin_amdgcn_raw_buffer_store_b64(v, r, vo, so, 16); }

#define STAMP(i)
#define MFMA0_A(acc, a, b) asm volatile("v_mfma_f32_16x16x32_bf16 %0, %1, %2, 0" : "=&v"(acc) : "a"(a), "v"(b))
#define MFMA_A(acc, a, b) asm volatile("v_mfma_f32_16x16x32_bf16 %0, %1, %2, %0" : "+v"(acc) : "a"(a), "v"(b))
#define MFMA0_V(acc, a, b) asm volatile("v_mfma_f32_16x16x32_bf16 %0, %1, %2, 0" : "=&v"(acc) : "v"(a), "v"(b))
#define MFMA_V(acc, a, b) asm volatile("v_mfma_f32_16x16x32_bf16 %0, %1, %2, %0" : "+v"(acc) : "v"(a), "v"(b))
#define BARRIER() asm volatile("s_waitcnt lgkmcnt(0)\n\ts_barrier" ::: "memory")
#define LAS __attribute__((address_space(3)))

__device__ __forceinline__ float xmax16(float x) {
    unsigned u = __float_as_uint(x);
    auto r = __builtin_amdgcn_permlane16_swap(u, u, false, false);
    return fmaxf(__uint_as_float(r[0]), __uint_as_float(r[1]));
}
__device__ __forceinline__ float xmax32(float x) {
    unsigned u = __float_as_uint(x);
    auto r = __builtin_amdgcn_permlane32_swap(u, u, false, false);
    return fmaxf(__uint_as_float(r[0]), __uint_as_float(r[1]));
}
__device__ __forceinline__ float xsum16(float x) {
    unsigned u = __float_as_uint(x);
    auto r = __builtin_amdgcn_permlane16_swap(u, u, false, false);
    return __uint_as_float(r[0]) + __uint_as_float(r[1]);
}
__device__ __forceinline__ float xsum32(float x) {
    unsigned u = __float_as_uint(x);
    auto r = __builtin_amdgcn_permlane32_swap(u, u, false, false);
    return __uint_as_float(r[0]) + __uint_as_float(r[1]);
}
__device__ __forceinline__ unsigned ror8u(unsigned x) { return __builtin_amdgcn_update_dpp(0u, x, 0x128, 0xf, 0xf, false); }
__device__ __forceinline__ float ror8(float x) { return __uint_as_float(ror8u(__float_as_uint(x))); }
__device__ __forceinline__ float xsum8(float x) {
    return x + __uint_as_float(__builtin_amdgcn_update_dpp(0u, __float_as_uint(x), 0x128, 0xf, 0xf, false));
}

#define DELAY_A 10
#define DELAY_B 10
#define EXA_STEP 524288u
#define EXB_STEP 16384u
__device__ __forceinline__ void chain_body(const u16* __restrict__ mem0, const WS& ws, char* smem, const int h) {
    char* sW = smem;
    char* sKV = smem + 131072;
    char* sMem = smem + 147456;
    char* sQK = sMem + 8 * MEM_STRIDE;
    char* sO = sQK + 16 * QK_STRIDE;
    const int tid = threadIdx.x, lane = tid & 63;
    const int w = __builtin_amdgcn_readfirstlane(tid >> 6);
    const int fr = lane & 15, fg = lane >> 4, fr8 = lane & 7;
    const unsigned l16 = lane * 16;
    char* lw = sW + w * 32768 + l16;

    bf16x8 wqk[3][16], wv[24], wo[24];
#pragma unroll
    for (int t = 0; t < 6; ++t) {
        const u16* p = ws.WqkvF_() + ((size_t)((h * 4 + w) * 6 + t) * 16) * 512 + lane * 8;
#pragma unroll
        for (int ks = 0; ks < 16; ++ks) {
            bf16x8 v = *(const bf16x8*)(p + ks * 512);
            if (t < 3) wqk[t][ks] = v;
            else if (t == 3) *(bf16x8*)(lw + ks * 1024) = v;
            else if (t == 4) wv[ks] = v;
            else if (ks < 8) wv[16 + ks] = v;
            else *(bf16x8*)(lw + (16 + ks - 8) * 1024) = v;
        }
    }
#pragma unroll
    for (int t = 0; t < 8; ++t)
#pragma unroll
        for (int ks = 0; ks < 4; ++ks) {
            bf16x8 v = *(const bf16x8*)(ws.WoF_() + ((size_t)(((w * 8 + t) * 32 + h) * 4 + ks)) * 512 + lane * 8);
            if (t < 6) wo[t * 4 + ks] = v;
            else *(bf16x8*)(lw + (24 + (t - 6) * 4 + ks) * 1024) = v;
        }
    for (int i = tid; i < 8 * 64; i += 256) {
        int row = i >> 6, c = (i & 63) * 8;
        *(u32x4*)(sMem + row * MEM_STRIDE + c * 2) = *(const u32x4*)(mem0 + row * DM + c);
    }
    const rsrc_t rW = mk_rsrc(ws.base);
    const unsigned stA_vo = (unsigned)fg * 128 + (fr & 7) * 16 + (fr >> 3) * 16384;
    const unsigned rdA_vo = (unsigned)(lane >> 3) * 512 + (lane & 7) * 16;
    const char* memrd = sMem + fr8 * MEM_STRIDE + fg * 16;
    const char* qrd = sQK + fr8 * QK_STRIDE + fg * 16;
    const char* ord_ = sO + fr8 * QK_STRIDE + fg * 16;
    char* memwr = sMem + (lane & 7) * MEM_STRIDE + ((w * 2 + (lane >> 5)) * 16 + ((lane >> 3) & 3) * 4) * 2;

    for (int s = 0; s < NSTEPS; ++s) {
        STAMP(0);
        if ((s & 3) == 0) {
            const unsigned* f = (lane & 4) ? (ws.flagZ_() + s + (lane & 3)) : (ws.flagKV_() + (s >> 2) * 64 + (lane & 1) * 32 + h);
            for (unsigned spin = 0; spin < (1u << 22); ++spin) {
                unsigned v = __hip_atomic_load(f, __ATOMIC_RELAXED, __HIP_MEMORY_SCOPE_AGENT);
                if (__all(v != 0)) break;
                __builtin_amdgcn_s_sleep(2);
            }
        }
        {
            const u16* gk = ws.K_() + (size_t)(s * NH + h) * 4096 + w * 1024 + lane * 8;
            const u16* gv = ws.VT_() + (size_t)(s * NH + h) * 4096 + w * 1024 + lane * 8;
            __builtin_amdgcn_global_load_lds((const unsigned*)gk, (LAS unsigned*)(sKV + w * 2048), 16, 0, 16);
            __builtin_amdgcn_global_load_lds((const unsigned*)(gk + 512), (LAS unsigned*)(sKV + w * 2048 + 1024), 16, 0, 16);
            __builtin_amdgcn_global_load_lds((const unsigned*)gv, (LAS unsigned*)(sKV + 8192 + w * 2048), 16, 0, 16);
            __builtin_amdgcn_global_load_lds((const unsigned*)(gv + 512), (LAS unsigned*)(sKV + 8192 + w * 2048 + 1024), 16, 0, 16);
        }
        if (s == 0) {
            asm volatile("s_waitcnt vmcnt(0)" ::: "memory");
        } else {
#pragma unroll 1
            for (int d = 0; d < DELAY_B; ++d) __builtin_amdgcn_s_sleep(1);
            u32x4 vv[4];
            for (unsigned spin = 0;; ++spin) {
                if (spin > (1u << 20)) break;
                asm volatile("" ::: "memory");
#pragma unroll
                for (int i = 0; i < 4; ++i) vv[i] = bld16_sc1(rW, l16, ((unsigned)(i * 4096 + w * 1024) + (unsigned)s * EXB_STEP) + (unsigned)OFF_exB);
                unsigned am_ = 1u;
#pragma unroll
                for (int i = 0; i < 4; ++i) am_ &= (vv[i][0] & vv[i][1]) & (vv[i][2] & vv[i][3]);
                if (__all(am_ != 0)) break;
            }
#pragma unroll
            for (int i = 0; i < 4; ++i) {
                u32x2 o = {(vv[i][0] >> 16) | (vv[i][1] & 0xffff0000u), (vv[i][2] >> 16) | (vv[i][3] & 0xffff0000u)};
                *(u32x2*)(memwr + i * 256) = o;
            }
        }
        bf16x8 kw[2], vw[2];
#pragma unroll
        for (int i = 0; i < 2; ++i) kw[i] = *(const bf16x8*)(lw + i * 1024);
        vw[0] = *(const bf16x8*)(lw + 16 * 1024); vw[1] = *(const bf16x8*)(lw + 17 * 1024);
        STAMP(1);
        BARRIER();
        STAMP(2);

        f32x4 acc[4], av[2];
        {
            bf16x8 mb[3];
#pragma unroll
            for (int i = 0; i < 3; ++i) mb[i] = *(const bf16x8*)(memrd + i * 64);
#pragma unroll
            for (int ks = 0; ks < 16; ++ks) {
                bf16x8 m = mb[ks % 3];
                bf16x8 k3 = kw[ks & 1];
                bf16x8 v5 = (ks < 8) ? wv[16 + ks] : vw[ks & 1];
                if (ks == 0) {
                    MFMA0_A(acc[0], wqk[0][ks], m); MFMA0_A(acc[1], wqk[1][ks], m); MFMA0_A(acc[2], wqk[2][ks], m); MFMA0_V(acc[3], k3, m);
                    MFMA0_V(av[0], m, wv[ks]); MFMA0_V(av[1], m, v5);
                } else {
                    MFMA_A(acc[0], wqk[0][ks], m); MFMA_A(acc[1], wqk[1][ks], m); MFMA_A(acc[2], wqk[2][ks], m); MFMA_V(acc[3], k3, m);
                    MFMA_V(av[0], m, wv[ks]); MFMA_V(av[1], m, v5);
                }
                if (ks + 3 < 16) mb[ks % 3] = *(const bf16x8*)(memrd + (ks + 3) * 64);
                if (ks + 2 < 16) kw[ks & 1] = *(const bf16x8*)(lw + (ks + 2) * 1024);
                if (ks >= 8 && ks + 2 < 16) vw[ks & 1] = *(const bf16x8*)(lw + (16 + ks + 2 - 8) * 1024);
            }
        }
        STAMP(3);
        asm volatile("s_nop 7" : "+v"(acc[0]), "+v"(acc[1]), "+v"(acc[2]), "+v"(acc[3]), "+v"(av[0]), "+v"(av[1]));
        u32x2 o[4];
        {
#pragma unroll
            for (int t = 0; t < 4; ++t) o[t] = (u32x2){pack2(acc[t][0], acc[t][1]), pack2(acc[t][2], acc[t][3])};
            if (fr < 8) {
#pragma unroll
                for (int t = 0; t < 4; ++t) {
                    int row = (t >> 1) * 8 + fr;
                    int c = w * 32 + (t & 1) * 16 + fg * 4;
                    *(u32x2*)(sQK + row * QK_STRIDE + c * 2) = o[t];
                }
            }
        }
        u32x4 am[2], ax[2];
#pragma unroll
        for (int t = 0; t < 2; ++t) {
            am[t] = (u32x4){pack2(av[t][0], av[t][1]), pack2(av[t][2], av[t][3]), 0u, 0u};
            if (fg >= 2) { am[t][0] = 0u; am[t][1] = 0u; }
        }
        bf16x8 kx[2][4];
#pragma unroll
        for (int ks = 0; ks < 4; ++ks) {
            kx[0][ks] = *(const bf16x8*)(sKV + ks * 1024 + l16);
            kx[1][ks] = *(const bf16x8*)(sKV + 4096 + ks * 1024 + l16);
        }
        ax[0] = *(const u32x4*)(sKV + 8192 + (w * 2) * 1024 + l16);
        ax[1] = *(const u32x4*)(sKV + 8192 + (w * 2 + 1) * 1024 + l16);
        STAMP(4);
        BARRIER();
        STAMP(5);
        f32x4 sm, sx0, sx1;
        {
            bf16x8 qf[4], kf[4];
#pragma unroll
            for (int ks = 0; ks < 4; ++ks) {
                qf[ks] = *(const bf16x8*)(qrd + ks * 64);
                kf[ks] = *(const bf16x8*)(qrd + 8 * QK_STRIDE + ks * 64);
            }
            MFMA0_V(sx0, kx[0][0], qf[0]); MFMA0_V(sx1, kx[1][0], qf[0]); MFMA0_V(sm, kf[0], qf[0]);
#pragma unroll
            for (int ks = 1; ks < 4; ++ks) { MFMA_V(sx0, kx[0][ks], qf[ks]); MFMA_V(sx1, kx[1][ks], qf[ks]); MFMA_V(sm, kf[ks], qf[ks]); }
        }
        STAMP(10);
        if (fr < 8) {
                bst8_sc1(rW, (unsigned)fr * 256 + fg * 8, ((unsigned)((s * NH + h) * 2048 + (w * 32) * 2)) + (unsigned)OFF_km, o[2]);
                bst8_sc1(rW, (unsigned)fr * 256 + fg * 8, ((unsigned)((s * NH + h) * 2048 + (w * 32 + 16) * 2)) + (unsigned)OFF_km, o[3]);
        }
        if (fg < 2) {
            bst8_sc1(rW, (unsigned)fr * 16 + fg * 8, ((unsigned)((s * NH + h) * 2048 + (w * 32) * 16)) + (unsigned)OFF_vmT, (u32x2){am[0][0], am[0][1]});
            bst8_sc1(rW, (unsigned)fr * 16 + fg * 8, ((unsigned)((s * NH + h) * 2048 + (w * 32 + 16) * 16)) + (unsigned)OFF_vmT, (u32x2){am[1][0], am[1][1]});
        }
        bf16x8 wf[8];
#pragma unroll
        for (int i = 0; i < 8; ++i) wf[i] = *(const bf16x8*)(lw + (24 + i) * 1024);
        asm volatile("s_nop 7" : "+v"(sm), "+v"(sx0), "+v"(sx1));
        const bool hi = fr >= 8;
        const f32x2 sc2 = {SCALE, SCALE};
        f32x2 p0 = {hi ? sx1[0] : sx0[0], hi ? sx1[1] : sx0[1]};
        f32x2 p1 = {hi ? sx1[2] : sx0[2], hi ? sx1[3] : sx0[3]};
        f32x2 p2 = {hi ? sm[2] : sm[0], hi ? sm[3] : sm[1]};
        p0 = rbf2(rbf2(p0) * sc2);
        p1 = rbf2(rbf2(p1) * sc2);
        p2 = rbf2(rbf2(p2) * sc2);
        if (fg >= 2) p2 = (f32x2){-3.0e38f, -3.0e38f};
        float mx = fmaxf(fmaxf(fmaxf(p0[0], p0[1]), fmaxf(p1[0], p1[1])), fmaxf(p2[0], p2[1]));
        mx = fmaxf(mx, ror8(mx));
        mx = xmax16(mx);
        mx = xmax32(mx);
        const float L2E = 1.4426950408889634f;
        const f32x2 l2 = {L2E, L2E};
        const f32x2 mxl = {mx, mx};
        p0 = (p0 - mxl) * l2; p1 = (p1 - mxl) * l2; p2 = (p2 - mxl) * l2;
        p0 = (f32x2){__builtin_amdgcn_exp2f(p0[0]), __builtin_amdgcn_exp2f(p0[1])};
        p1 = (f32x2){__builtin_amdgcn_exp2f(p1[0]), __builtin_amdgcn_exp2f(p1[1])};
        p2 = (f32x2){__builtin_amdgcn_exp2f(p2[0]), __builtin_amdgcn_exp2f(p2[1])};
        f32x2 s2 = (p0 + p1) + p2;
        float sum = s2[0] + s2[1];
        sum = sum + ror8(sum);
        sum = xsum16(sum);
        sum = xsum32(sum);
        float inv = __builtin_amdgcn_rcpf(sum);
        const f32x2 iv = {inv, inv};
        p0 *= iv; p1 *= iv; p2 *= iv;
        unsigned d0 = pack2(p0[0], p0[1]), d1 = pack2(p1[0], p1[1]), d2 = pack2(p2[0], p2[1]);
        unsigned e0 = ror8u(d0), e1 = ror8u(d1), e2 = ror8u(d2);
        u32x4 px = {d0, d1, e0, e1};
        u32x4 pm = {d2, e2, 0u, 0u};
        STAMP(12);
        f32x4 o0, o1;
        asm volatile("s_nop 1" : "+v"(px), "+v"(pm), "+v"(am[0]), "+v"(am[1]), "+v"(ax[0]), "+v"(ax[1]));
        MFMA0_V(o0, ax[0], px); MFMA0_V(o1, ax[1], px);
        MFMA_V(o0, am[0], pm); MFMA_V(o1, am[1], pm);
        asm volatile("s_nop 7" : "+v"(o0), "+v"(o1));
        if (fr < 8) {
            *(u32x2*)(sO + fr * QK_STRIDE + (w * 32 + fg * 4) * 2) = (u32x2){pack2(o0[0], o0[1]), pack2(o0[2], o0[3])};
            *(u32x2*)(sO + fr * QK_STRIDE + (w * 32 + 16 + fg * 4) * 2) = (u32x2){pack2(o1[0], o1[1]), pack2(o1[2], o1[3])};
        }
        STAMP(6);
        BARRIER();
        STAMP(7);
        bf16x8 of[4];
#pragma unroll
        for (int ks = 0; ks < 4; ++ks) of[ks] = *(const bf16x8*)(ord_ + ks * 64);
        {
            f32x4 a[4];
#pragma unroll
            for (int p = 0; p <= 4; ++p) {
                if (p < 4) {
#pragma unroll
                    for (int u = 0; u < 2; ++u) {
                        const int t = 2 * p + u;
                        f32x4& ac = a[(p & 1) * 2 + u];
#pragma unroll
                        for (int ks = 0; ks < 4; ++ks) {
                            if (t < 4) { if (ks == 0) MFMA0_A(ac, wo[t * 4 + ks], of[ks]); else MFMA_A(ac, wo[t * 4 + ks], of[ks]); }
                            else {
                                bf16x8 f = (t < 6) ? wo[t * 4 + ks] : wf[(t - 6) * 4 + ks];
                                if (ks == 0) MFMA0_V(ac, f, of[ks]); else MFMA_V(ac, f, of[ks]);
                            }
                        }
                    }
                }
                if (p >= 1) {
                    const int q0 = ((p - 1) & 1) * 2;
                    if (p == 4) asm volatile("s_nop 7" : "+v"(a[q0]), "+v"(a[q0 + 1]));
                    u32x4 o;
#pragma unroll
                    for (int q = 0; q < 4; ++q) o[q] = __float_as_uint(fr < 8 ? a[q0][q] : a[q0 + 1][q]) | 1u;
                    bst16_sc1(rW, stA_vo, ((unsigned)(((w * 8 + 2 * (p - 1)) * NH + h) * 512) + (unsigned)s * EXA_STEP) + (unsigned)OFF_exA, o);
                }
            }
        }
        STAMP(8);
                if (s > 0 && tid == 192) __builtin_amdgcn_raw_buffer_store_b32(1u, rW, 0, (unsigned)OFF_flagMr + (unsigned)(((s - 1) * 8 + (h >> 2)) * 32 + (h & 3)) * 4u, 16);
        {
#pragma unroll 1
            for (int d = 0; d < DELAY_A; ++d) __builtin_amdgcn_s_sleep(1);
            float tot[4];
            for (unsigned spin = 0;; ++spin) {
                if (spin > (1u << 20)) break;
                asm volatile("" ::: "memory");
                u32x4 v[4];
#pragma unroll
                for (int i = 0; i < 4; ++i) v[i] = bld16_sc1(rW, rdA_vo, ((unsigned)(h * NH * 512 + i * 4096 + w * 128) + (unsigned)s * EXA_STEP) + (unsigned)OFF_exA);
                bool ok = true;
#pragma unroll
                for (int q = 0; q < 4; ++q) {
                    ok &= ((v[0][q] & v[1][q] & v[2][q] & v[3][q] & 1u) != 0);
                    tot[q] = (__uint_as_float(v[0][q]) + __uint_as_float(v[1][q])) + (__uint_as_float(v[2][q]) + __uint_as_float(v[3][q]));
                }
                if (__all(ok)) break;
            }
#pragma unroll
            for (int q = 0; q < 4; ++q) {
                tot[q] = xsum8(tot[q]);
                tot[q] = xsum16(tot[q]);
                tot[q] = xsum32(tot[q]);
            }
            STAMP(9);
            if (lane < 8) {
                u32x4 o;
#pragma unroll
                for (int q = 0; q < 4; ++q) o[q] = ((unsigned)f2bf(tot[q]) << 16) | 1u;
                bst16_sc1(rW, l16, ((unsigned)(h * 512 + w * 128) + (unsigned)(s + 1) * EXB_STEP) + (unsigned)OFF_exB, o);
            }
        }
    }
    asm volatile("s_waitcnt vmcnt(0)" ::: "memory");
    __syncthreads();
    if (tid == 0) __builtin_amdgcn_raw_buffer_store_b32(1u, rW, 0, (unsigned)OFF_flagMr + (unsigned)(((NSTEPS - 1) * 8 + (h >> 2)) * 32 + (h & 3)) * 4u, 16);
}

enum { EPI_QKV = 0, EPI_H = 1, EPI_G = 2 };
__device__ __forceinline__ float gelu_tanh(float v) {
    float u = 0.7978845608028654f * (v + 0.044715f * v * v * v);
    return 0.5f * v * (1.f + tanhf(u));
}
__device__ __forceinline__ void wait_flag_ge(const unsigned* f, unsigned want) {
    for (unsigned spin = 0; spin < (1u << 22); ++spin) {
        unsigned v = __hip_atomic_load(f, __ATOMIC_RELAXED, __HIP_MEMORY_SCOPE_AGENT);
        if (__all(v >= want)) break;
        __builtin_amdgcn_s_sleep(64);
    }
}
__device__ __forceinline__ void acquire_all() {
    __builtin_amdgcn_fence(__ATOMIC_ACQUIRE, "agent");
    asm volatile("s_waitcnt vmcnt(0)" ::: "memory");
    __syncthreads();
}
__device__ __forceinline__ void publish_done() {
    asm volatile("s_waitcnt vmcnt(0)" ::: "memory");
    __syncthreads();
}

template <int EPI, int NKS>
__device__ __forceinline__ void gemm_tile(unsigned offA, int tb, int cb, const WS& ws, const u16* __restrict__ bias, const int tidx) {
    const int lane = tidx & 63, w = __builtin_amdgcn_readfirstlane(tidx >> 6);
    const int wr = w >> 1, wc = w & 1;
    const int fr = lane & 15, fg = lane >> 4;
    const rsrc_t rW = mk_rsrc(ws.base);
    const unsigned l16 = lane * 16;
    const bool vtile = (EPI == EPI_QKV) && (cb >= 64);
    unsigned ao[4], bo[4];
#pragma unroll
    for (int i = 0; i < 4; ++i) {
        ao[i] = offA + (unsigned)((tb * 8 + wr * 4 + i) * NKS) * 1024u;
        int ct = cb * 8 + wc * 4 + i;
        if (EPI == EPI_QKV) {
            int m = ct >> 8, r = ct & 255;
            bo[i] = (unsigned)OFF_WqkvF + (unsigned)((((r >> 3) * 4 + ((r >> 1) & 3)) * 6 + m * 2 + (r & 1)) * 16) * 1024u;
        } else if (EPI == EPI_H) bo[i] = (unsigned)OFF_W1F + (unsigned)(ct * 16) * 1024u;
        else bo[i] = (unsigned)OFF_W2F + (unsigned)(ct * 44) * 1024u;
    }
    f32x4 acc[4][4];
#pragma unroll
    for (int i = 0; i < 4; ++i)
#pragma unroll
        for (int j = 0; j < 4; ++j) acc[i][j] = (f32x4){0.f, 0.f, 0.f, 0.f};
    constexpr int D = 4;
    u32x4 fa[D][4], fb[D][4];
#pragma unroll
    for (int d = 0; d < D; ++d)
#pragma unroll
        for (int i = 0; i < 4; ++i) { fa[d][i] = bld16(rW, l16, ao[i] + d * 1024u); fb[d][i] = bld16(rW, l16, bo[i] + d * 1024u); }
    for (int k0 = 0; k0 < NKS; k0 += D) {
#pragma unroll
        for (int d = 0; d < D; ++d) {
            u32x4 a[4], bq[4];
#pragma unroll
            for (int i = 0; i < 4; ++i) { a[i] = fa[d][i]; bq[i] = fb[d][i]; }
            const int kn = k0 + d + D;
            if (kn < NKS) {
#pragma unroll
                for (int i = 0; i < 4; ++i) { fa[d][i] = bld16(rW, l16, ao[i] + (unsigned)kn * 1024u); fb[d][i] = bld16(rW, l16, bo[i] + (unsigned)kn * 1024u); }
            }
            if (vtile) {
#pragma unroll
                for (int i = 0; i < 4; ++i)
#pragma unroll
                    for (int j = 0; j < 4; ++j) acc[i][j] = __builtin_amdgcn_mfma_f32_16x16x32_bf16(__builtin_bit_cast(bf16x8, a[i]), __builtin_bit_cast(bf16x8, bq[j]), acc[i][j], 0, 0, 0);
            } else {
#pragma unroll
                for (int i = 0; i < 4; ++i)
#pragma unroll
                    for (int j = 0; j < 4; ++j) acc[i][j] = __builtin_amdgcn_mfma_f32_16x16x32_bf16(__builtin_bit_cast(bf16x8, bq[j]), __builtin_bit_cast(bf16x8, a[i]), acc[i][j], 0, 0, 0);
            }
        }
    }
    const int brow = tb * 128, bcol = cb * 128;
#pragma unroll
    for (int i = 0; i < 4; ++i)
#pragma unroll
        for (int j = 0; j < 4; ++j) {
            f32x4 a = acc[i][j];
            if (vtile) {
                int col = bcol + wc * 64 + j * 16 + fr - 2 * HD;
                int tok = brow + wr * 64 + i * 16 + fg * 4;
                int seg = tok >> 5, tt = (tok >> 4) & 1, hh = col >> 7, et = (col >> 4) & 7;
                u32x2 o = {pack2(a[0], a[1]), pack2(a[2], a[3])};
                bst8_sc1(rW, (unsigned)((((seg * NH + hh) * 8 + et) * 512 + (fg * 16 + fr) * 8 + tt * 4) * 2), (unsigned)OFF_VT, o);
            } else {
                int tok = brow + wr * 64 + i * 16 + fr;
                int col = bcol + wc * 64 + j * 16 + fg * 4;
                if (EPI == EPI_QKV) {
                    int cc = col & (HD - 1);
                    int seg = tok >> 5, tt = (tok >> 4) & 1, hh = cc >> 7, e = cc & 127;
                    u32x2 o = {pack2(a[0], a[1]), pack2(a[2], a[3])};
                    unsigned off = (unsigned)((((seg * NH + hh) * 8 + tt * 4 + (e >> 5)) * 512 + (((e >> 3) & 3) * 16 + (tok & 15)) * 8 + (e & 4)) * 2);
                    bst8_sc1(rW, off, (col >= HD) ? (unsigned)OFF_K : (unsigned)OFF_Q, o);
                } else if (EPI == EPI_H) {
                    float r[4];
#pragma unroll
                    for (int q = 0; q < 4; ++q) {
                        float b = (col + q < FF) ? bf2f(bias[col + q]) : 0.f;
                        float v = rbf(rbf(a[q]) + b);
                        r[q] = (col + q < FF) ? gelu_tanh(v) : 0.f;
                    }
                    u32x2 o = {pack2(r[0], r[1]), pack2(r[2], r[3])};
                    unsigned off = (unsigned)((((tok >> 4) * 44 + (col >> 5)) * 64 + ((col >> 3) & 3) * 16 + (tok & 15)) * 16 + (col & 4) * 2);
                    bst8_sc1(rW, off, (unsigned)OFF_hmidF, o);
                } else {
                    float r[4];
#pragma unroll
                    for (int q = 0; q < 4; ++q) {
                        float v = rbf(rbf(a[q]) + bf2f(bias[col + q]));
                        r[q] = v / (1.f + __expf(-v));
                    }
                    u32x2 o = {pack2(r[0], r[1]), pack2(r[2], r[3])};
                    bst8_sc1(rW, (unsigned)((tok * DM + col) * 2), (unsigned)OFF_g, o);
                }
            }
        }
}

__device__ __forceinline__ u32x2 bld8_sc1(rsrc_t r, unsigned vo, unsigned so) { return __builtin_amdgcn_raw_buffer_load_b64(r, vo, so, 16); }
__device__ __forceinline__ void attn_task(int s, int hg, const WS& ws, char* lds, const int tidx) {
    const int lane = tidx & 63, w = __builtin_amdgcn_readfirstlane(tidx >> 6);
    const int h = hg * 4 + w;
    const int fr = lane & 15, fg = lane >> 4, fr8 = lane & 7;
    char* sO = lds;
    const rsrc_t rW = mk_rsrc(ws.base);
    const unsigned l16 = lane * 16;
    const unsigned sh = (unsigned)(s * NH + h) * 8192u;
    char* ring = lds + 33280 + w * 24576;
    const unsigned wbase = (unsigned)OFF_WoF + (unsigned)((w * 8 * 32 + hg * 4) * 4) * 1024u;
    auto issue = [&](int ks) {
#pragma unroll
        for (int t = 0; t < 8; ++t)
            __builtin_amdgcn_raw_ptr_buffer_load_lds(rW, (LAS void*)(ring + ((ks % 3) * 8 + t) * 1024), 16, l16, wbase + (unsigned)(t * 128 + ks) * 1024u, 0, 0);
    };
    issue(0); issue(1); issue(2);
    u32x4 ax[8];
    f32x4 sx0[2], sx1[2];
    u32x4 qf[2][4];
    {
        u32x4 kx[2][4];
#pragma unroll
        for (int ks = 0; ks < 4; ++ks)
#pragma unroll
            for (int t = 0; t < 2; ++t) { kx[t][ks] = bld16(rW, l16, (unsigned)OFF_K + sh + (unsigned)(t * 4 + ks) * 1024u); qf[t][ks] = bld16(rW, l16, (unsigned)OFF_Q + sh + (unsigned)(t * 4 + ks) * 1024u); }
#pragma unroll
        for (int et = 0; et < 8; ++et) ax[et] = bld16(rW, l16, (unsigned)OFF_VT + sh + (unsigned)et * 1024u);
#pragma unroll
        for (int qt = 0; qt < 2; ++qt) {
            sx0[qt] = (f32x4){0.f, 0.f, 0.f, 0.f}; sx1[qt] = sx0[qt];
#pragma unroll
            for (int ks = 0; ks < 4; ++ks) {
                sx0[qt] = __builtin_amdgcn_mfma_f32_16x16x32_bf16(__builtin_bit_cast(bf16x8, kx[0][ks]), __builtin_bit_cast(bf16x8, qf[qt][ks]), sx0[qt], 0, 0, 0);
                sx1[qt] = __builtin_amdgcn_mfma_f32_16x16x32_bf16(__builtin_bit_cast(bf16x8, kx[1][ks]), __builtin_bit_cast(bf16x8, qf[qt][ks]), sx1[qt], 0, 0, 0);
            }
        }
    }
    if (w == 0) {
        const unsigned* f = ws.flagM_() + (s * 8 + hg) * 32 + (lane & 3);
        for (unsigned spin = 0; spin < (1u << 22); ++spin) {
            unsigned v = __hip_atomic_load(f, __ATOMIC_RELAXED, __HIP_MEMORY_SCOPE_AGENT);
            if (__all(v != 0)) break;
            __builtin_amdgcn_s_sleep(16);
        }
    }
    asm volatile("" ::: "memory");
    __builtin_amdgcn_s_barrier();
    asm volatile("" ::: "memory");
    u32x4 kmf[4];
    u32x2 vm[8];
#pragma unroll
    for (int ks = 0; ks < 4; ++ks) kmf[ks] = bld16_sc1(rW, (unsigned)fr8 * 256 + fg * 16, (unsigned)OFF_km + (unsigned)(s * NH + h) * 2048u + ks * 64u);
#pragma unroll
    for (int et = 0; et < 8; ++et) {
        vm[et] = (u32x2){0u, 0u};
        if (fg < 2) vm[et] = bld8_sc1(rW, (unsigned)fr * 16 + fg * 8, (unsigned)OFF_vmT + (unsigned)(s * NH + h) * 2048u + et * 256u);
    }
    u32x4 px[2], pm[2];
#pragma unroll
    for (int qt = 0; qt < 2; ++qt) {
        f32x4 sm = {0.f, 0.f, 0.f, 0.f};
#pragma unroll
        for (int ks = 0; ks < 4; ++ks) sm = __builtin_amdgcn_mfma_f32_16x16x32_bf16(__builtin_bit_cast(bf16x8, kmf[ks]), __builtin_bit_cast(bf16x8, qf[qt][ks]), sm, 0, 0, 0);
        const int qi = qt * 16 + fr;
        float sv[12];
#pragma unroll
        for (int q = 0; q < 4; ++q) {
            sv[q] = (fg < 2) ? rbf(rbf(sm[q]) * SCALE) : -3.0e38f;
            int k0 = fg * 4 + q, k1 = 16 + fg * 4 + q;
            sv[4 + q] = (k0 <= qi) ? rbf(rbf(sx0[qt][q]) * SCALE) : -3.0e38f;
            sv[8 + q] = (k1 <= qi) ? rbf(rbf(sx1[qt][q]) * SCALE) : -3.0e38f;
        }
        float mx = sv[0];
#pragma unroll
        for (int q = 1; q < 12; ++q) mx = fmaxf(mx, sv[q]);
        mx = xmax16(mx); mx = xmax32(mx);
        float sum = 0.f;
#pragma unroll
        for (int q = 0; q < 12; ++q) { sv[q] = __expf(sv[q] - mx); sum += sv[q]; }
        sum = xsum16(sum); sum = xsum32(sum);
        float inv = 1.f / sum;
        px[qt] = (u32x4){pack2(sv[4] * inv, sv[5] * inv), pack2(sv[6] * inv, sv[7] * inv), pack2(sv[8] * inv, sv[9] * inv), pack2(sv[10] * inv, sv[11] * inv)};
        pm[qt] = (u32x4){pack2(sv[0] * inv, sv[1] * inv), pack2(sv[2] * inv, sv[3] * inv), 0u, 0u};
    }
#pragma unroll
    for (int et = 0; et < 8; ++et) {
        u32x4 am = {vm[et][0], vm[et][1], 0u, 0u};
#pragma unroll
        for (int qt = 0; qt < 2; ++qt) {
            f32x4 o = {0.f, 0.f, 0.f, 0.f};
            o = __builtin_amdgcn_mfma_f32_16x16x32_bf16(__builtin_bit_cast(bf16x8, ax[et]), __builtin_bit_cast(bf16x8, px[qt]), o, 0, 0, 0);
            o = __builtin_amdgcn_mfma_f32_16x16x32_bf16(__builtin_bit_cast(bf16x8, am), __builtin_bit_cast(bf16x8, pm[qt]), o, 0, 0, 0);
            u32x2 ov = {pack2(o[0], o[1]), pack2(o[2], o[3])};
            *(u32x2*)(sO + (qt * 16 + fr) * 1040 + (w * DH + et * 16 + fg * 4) * 2) = ov;
        }
    }
    asm volatile("s_waitcnt lgkmcnt(0)\n\ts_barrier" ::: "memory");
    f32x4 acc[8][2];
#pragma unroll
    for (int t = 0; t < 8; ++t) { acc[t][0] = (f32x4){0.f, 0.f, 0.f, 0.f}; acc[t][1] = acc[t][0]; }
    {
#pragma unroll
        for (int ks = 0; ks < 16; ++ks) {
            if (ks <= 13) asm volatile("s_waitcnt vmcnt(16)" ::: "memory");
            else if (ks == 14) asm volatile("s_waitcnt vmcnt(8)" ::: "memory");
            else asm volatile("s_waitcnt vmcnt(0)" ::: "memory");
            bf16x8 b0 = *(const bf16x8*)(sO + fr * 1040 + (ks * 32 + fg * 8) * 2);
            bf16x8 b1 = *(const bf16x8*)(sO + (16 + fr) * 1040 + (ks * 32 + fg * 8) * 2);
            bf16x8 a[8];
#pragma unroll
            for (int t = 0; t < 8; ++t) a[t] = *(const bf16x8*)(ring + ((ks % 3) * 8 + t) * 1024 + lane * 16);
            if (ks + 3 < 16) {
                asm volatile("s_waitcnt lgkmcnt(0)" ::: "memory");
                issue(ks + 3);
            }
#pragma unroll
            for (int t = 0; t < 8; ++t) {
                acc[t][0] = __builtin_amdgcn_mfma_f32_16x16x32_bf16(a[t], b0, acc[t][0], 0, 0, 0);
                acc[t][1] = __builtin_amdgcn_mfma_f32_16x16x32_bf16(a[t], b1, acc[t][1], 0, 0, 0);
            }
        }
    }
#pragma unroll
    for (int t = 0; t < 8; ++t)
#pragma unroll
        for (int qt = 0; qt < 2; ++qt) {
            u32x4 o = {__float_as_uint(acc[t][qt][0]), __float_as_uint(acc[t][qt][1]), __float_as_uint(acc[t][qt][2]), __float_as_uint(acc[t][qt][3])};
            unsigned off = (unsigned)((((s * 8 + hg) * 32 + qt * 16 + fr) * 512 + w * 128 + t * 16 + fg * 4) * 4);
            bst16_sc1(rW, off, (unsigned)OFF_part, o);
        }
}

__device__ __forceinline__ void reduce_task(int s, int r0, const u16* __restrict__ x, u16* __restrict__ out, const WS& ws, const int tidx) {
    const int i = tidx;
    {
        int row = r0 + (i >> 7), c = (i & 127) * 4;
        f32x4 t = {0.f, 0.f, 0.f, 0.f};
        f32x4 pv[8];
#pragma unroll
        for (int hg = 0; hg < 8; ++hg) pv[hg] = *(const f32x4*)(ws.part_() + ((size_t)(s * 8 + hg) * 32 + row) * 512 + c);
#pragma unroll
        for (int hg = 0; hg < 8; ++hg) t += pv[hg];
        int tok = s * SEG + row;
        u32x2 xv = *(const u32x2*)(x + (size_t)tok * DM + c);
        u32x2 gv = *(const u32x2*)(ws.g_() + (size_t)tok * DM + c);
        float xs[4] = {bf2f(xv[0] & 0xffff), bf2f(xv[0] >> 16), bf2f(xv[1] & 0xffff), bf2f(xv[1] >> 16)};
        float gs[4] = {bf2f(gv[0] & 0xffff), bf2f(gv[0] >> 16), bf2f(gv[1] & 0xffff), bf2f(gv[1] >> 16)};
        float r[4];
#pragma unroll
        for (int q = 0; q < 4; ++q) r[q] = xs[q] + rbf(rbf(t[q]) * gs[q]);
        u32x2 o = {pack2(r[0], r[1]), pack2(r[2], r[3])};
        *(u32x2*)(out + (size_t)tok * DM + c) = o;
    }
}

#define NWORK 224
#define T_KV0 64
#define T_Z 64
#define T_KV 960
#define T_H 176
#define T_Q 512
#define T_G 64
#define T_A 512
#define T_TOTAL (T_KV0 + T_Z + T_KV + T_H + T_Q + T_G + T_A)
#define LDS_BYTES 162816
#define NG 128

__global__ void __launch_bounds__(256, 1) mega_k(const u16* __restrict__ x, const u16* __restrict__ mem0, const u16* __restrict__ b1, const u16* __restrict__ b2,
                                                 u16* __restrict__ out, WS ws) {
    extern __shared__ __attribute__((aligned(16))) char smem[];
    if (blockIdx.x < NH) {
        chain_body(mem0, ws, smem, blockIdx.x);
        return;
    }
    const int tid = threadIdx.x, lane = tid & 63, w = tid >> 6;
    const int wj = blockIdx.x - NH;
    constexpr int T_GEMM = T_KV0 + T_Z + T_KV + T_H + T_Q + T_G;
    for (int it = 0;; ++it) {
        int task;
        if (wj < NG) { task = it == 0 ? wj : NWORK + wj + (it - 1) * NG; if (task >= T_GEMM) break; }
        else { task = it == 0 ? wj : T_GEMM + (wj - NG) + (it - 1) * (NWORK - NG); if (task >= T_TOTAL) break; }
        int t = task;
        int tidx = threadIdx.x;
        asm volatile("" : "+v"(tidx));
        if (t < T_KV0 + T_Z + T_KV) {
            if (t >= T_KV0 && t < T_KV0 + T_Z) {
                const int z = t - T_KV0;
                const rsrc_t rW = mk_rsrc(ws.base);
                u32x4 zz = {0u, 0u, 0u, 0u};
#pragma unroll 8
                for (int i = 0; i < 128; ++i) bst16_sc1(rW, (unsigned)tidx * 16, (unsigned)OFF_exA + (unsigned)z * 524288u + (unsigned)i * 4096u, zz);
                publish_done();
                if (tid == 0) __hip_atomic_store(ws.flagZ_() + z, 1u, __ATOMIC_RELAXED, __HIP_MEMORY_SCOPE_AGENT);
                continue;
            }
            if (t >= T_KV0) t -= T_Z;
            int tb = t >> 6, cb = t & 63;
            gemm_tile<EPI_QKV, 16>((unsigned)OFF_xnF, tb, 32 + cb, ws, nullptr, tidx);
            publish_done();
            if (tid == 0) __hip_atomic_store(ws.flagKV_() + tb * 64 + cb, 1u, __ATOMIC_RELAXED, __HIP_MEMORY_SCOPE_AGENT);
            continue;
        }
        t -= T_KV0 + T_Z + T_KV;
        if (t < T_H) {
            int tb = t / 11, cb = t % 11;
            gemm_tile<EPI_H, 16>((unsigned)OFF_yF, tb, cb, ws, b1, tidx);
            publish_done();
            if (tid == 0) __hip_atomic_fetch_add(ws.cntH_() + tb, 1u, __ATOMIC_RELAXED, __HIP_MEMORY_SCOPE_AGENT);
            continue;
        }
        t -= T_H;
        if (t < T_Q) {
            int tb = t >> 5, cb = t & 31;
            gemm_tile<EPI_QKV, 16>((unsigned)OFF_xnF, tb, cb, ws, nullptr, tidx);
            publish_done();
            if (tid == 0) __hip_atomic_store(ws.flagQ_() + tb * 32 + cb, 1u, __ATOMIC_RELAXED, __HIP_MEMORY_SCOPE_AGENT);
            continue;
        }
        t -= T_Q;
        if (t < T_G) {
            int tb = t >> 2, cb = t & 3;
            if (w == 0) wait_flag_ge(ws.cntH_() + tb, 11u);
            acquire_all();
            gemm_tile<EPI_G, 44>((unsigned)OFF_hmidF, tb, cb, ws, b2, tidx);
            publish_done();
            if (tid == 0) __hip_atomic_store(ws.flagG_() + tb * 4 + cb, 1u, __ATOMIC_RELAXED, __HIP_MEMORY_SCOPE_AGENT);
            continue;
        }
        t -= T_G;
        {
            int s = t >> 3, hg = t & 7, tb = s >> 2;
            if (w == 0) {
                const unsigned* f;
                int l = lane & 15, hh = hg * 4 + (l & 3);
                if (l < 4) f = ws.flagKV_() + tb * 64 + hh;
                else if (l < 8) f = ws.flagKV_() + tb * 64 + 32 + hh;
                else f = ws.flagQ_() + tb * 32 + hh;
                wait_flag_ge(f, 1u);
            }
            acquire_all();
            attn_task(s, hg, ws, smem, tidx);
            publish_done();
            if (tid == 0) __hip_atomic_fetch_add(ws.cntO_() + s, 1u, __ATOMIC_RELAXED, __HIP_MEMORY_SCOPE_AGENT);
            if (w == 0) { wait_flag_ge(lane < 4 ? ws.flagG_() + tb * 4 + lane : ws.cntO_() + s, lane < 4 ? 1u : 8u); }
            acquire_all();
            reduce_task(s, hg * 4, x, out, ws, tidx);
            reduce_task(s, hg * 4 + 2, x, out, ws, tidx);
            __syncthreads();
        }
    }
}

extern "C" void kernel_launch(void* const* d_in, const int* in_sizes, int n_in, void* d_out, int out_size, void* d_ws, size_t ws_size,
                              hipStream_t stream) {
    const u16* x = (const u16*)d_in[0];
    const u16* mem0 = (const u16*)d_in[1];
    const u16* Wq = (const u16*)d_in[2];
    const u16* Wk = (const u16*)d_in[3];
    const u16* Wv = (const u16*)d_in[4];
    const u16* Wo = (const u16*)d_in[5];
    const u16* w1 = (const u16*)d_in[8];
    const u16* b1 = (const u16*)d_in[9];
    const u16* w2 = (const u16*)d_in[10];
    const u16* b2 = (const u16*)d_in[11];
    u16* out = (u16*)d_out;

    WS ws; ws.base = (char*)d_ws;

    const int nprep = PREP_LN + PREP_TQKV + PREP_TWO + PREP_TW1 + PREP_TW2 + PREP_ZERO;
    prep_k<<<nprep, 256, 0, stream>>>(x, Wq, Wk, Wv, Wo, w1, w2, ws);
    (void)hipFuncSetAttribute((const void*)mega_k, hipFuncAttributeMaxDynamicSharedMemorySize, LDS_BYTES);
    mega_k<<<256, 256, LDS_BYTES, stream>>>(x, mem0, b1, b2, out, ws);
}
```

```cpp
#include <hip/hip_runtime.h>
#include <hip/hip_bf16.h>
#include <stdint.h>

typedef __attribute__((ext_vector_type(8))) short bf16x8;
typedef __attribute__((ext_vector_type(4))) float f32x4;
typedef __attribute__((ext_vector_type(4))) unsigned u32x4;
typedef __attribute__((ext_vector_type(2))) unsigned u32x2;
typedef unsigned short u16;

#define S_TOK 2048
#define DM 512
#define NH 32
#define DH 128
#define HD 4096
#define NMEM 8
#define SEG 32
#define NSEG 64
#define NSTEPS 64
#define FF 1376
#define FFP 1408

__device__ __forceinline__ u16 f2bf(float f) { return __builtin_bit_cast(u16, (__bf16)f); }
__device__ __forceinline__ float bf2f(u16 h) { return __builtin_bit_cast(float, (unsigned)h << 16); }
__device__ __forceinline__ float rbf(float f) { return bf2f(f2bf(f)); }
typedef __attribute__((ext_vector_type(2))) float f32x2;
typedef __attribute__((ext_vector_type(2))) __bf16 bf16x2v;
__device__ __forceinline__ unsigned pack2(float a, float b) { f32x2 v = {a, b}; return __builtin_bit_cast(unsigned, __builtin_convertvector(v, bf16x2v)); }
__device__ __forceinline__ f32x2 rbf2(f32x2 v) { unsigned u = __builtin_bit_cast(unsigned, __builtin_convertvector(v, bf16x2v)); f32x2 r = {__uint_as_float(u << 16), __uint_as_float(u & 0xffff0000u)}; return r; }

constexpr size_t OFF_flags = 0;
constexpr size_t OFF_flagMr = OFF_flags + 65536;
constexpr size_t OFF_exB = OFF_flagMr + 65536;
constexpr size_t OFF_exA = OFF_exB + (((size_t)68 * 16384) + 255) / 256 * 256;
constexpr size_t OFF_xnF = OFF_exA + (((size_t)64 * 524288) + 255) / 256 * 256;
constexpr size_t OFF_yF = OFF_xnF + (((size_t)S_TOK * DM * 2) + 255) / 256 * 256;
constexpr size_t OFF_WqkvF = OFF_yF + (((size_t)S_TOK * DM * 2) + 255) / 256 * 256;
constexpr size_t OFF_WoF = OFF_WqkvF + (((size_t)3 * HD * DM * 2) + 255) / 256 * 256;
constexpr size_t OFF_W1F = OFF_WoF + (((size_t)DM * HD * 2) + 255) / 256 * 256;
constexpr size_t OFF_W2F = OFF_W1F + (((size_t)FFP * DM * 2) + 255) / 256 * 256;
constexpr size_t OFF_Q = OFF_W2F + (((size_t)DM * FFP * 2) + 255) / 256 * 256;
constexpr size_t OFF_K = OFF_Q + (((size_t)S_TOK * HD * 2) + 255) / 256 * 256;
constexpr size_t OFF_VT = OFF_K + (((size_t)S_TOK * HD * 2) + 255) / 256 * 256;
constexpr size_t OFF_hmidF = OFF_VT + (((size_t)S_TOK * HD * 2) + 255) / 256 * 256;
constexpr size_t OFF_g = OFF_hmidF + (((size_t)S_TOK * FFP * 2) + 255) / 256 * 256;
constexpr size_t OFF_km = OFF_g + (((size_t)S_TOK * DM * 2) + 255) / 256 * 256;
constexpr size_t OFF_vmT = OFF_km + (((size_t)NSEG * NH * 8 * DH * 2) + 255) / 256 * 256;
constexpr size_t OFF_part = OFF_vmT + (((size_t)NSEG * NH * 8 * DH * 2) + 255) / 256 * 256;
constexpr size_t OFF_dbg = (size_t)240 * 1024 * 1024;
struct WS {
    char* base;
    __host__ __device__ unsigned* flags_() const { return (unsigned*)(base + OFF_flags); }
    __host__ __device__ unsigned* exB_() const { return (unsigned*)(base + OFF_exB); }
    __host__ __device__ unsigned* exA_() const { return (unsigned*)(base + OFF_exA); }
    __host__ __device__ u16* xnF_() const { return (u16*)(base + OFF_xnF); }
    __host__ __device__ u16* yF_() const { return (u16*)(base + OFF_yF); }
    __host__ __device__ u16* WqkvF_() const { return (u16*)(base + OFF_WqkvF); }
    __host__ __device__ u16* WoF_() const { return (u16*)(base + OFF_WoF); }
    __host__ __device__ u16* W1F_() const { return (u16*)(base + OFF_W1F); }
    __host__ __device__ u16* W2F_() const { return (u16*)(base + OFF_W2F); }
    __host__ __device__ u16* Q_() const { return (u16*)(base + OFF_Q); }
    __host__ __device__ u16* K_() const { return (u16*)(base + OFF_K); }
    __host__ __device__ u16* VT_() const { return (u16*)(base + OFF_VT); }
    __host__ __device__ u16* hmidF_() const { return (u16*)(base + OFF_hmidF); }
    __host__ __device__ u16* g_() const { return (u16*)(base + OFF_g); }
    __host__ __device__ u16* km_() const { return (u16*)(base + OFF_km); }
    __host__ __device__ u16* vmT_() const { return (u16*)(base + OFF_vmT); }
    __host__ __device__ float* part_() const { return (float*)(base + OFF_part); }
    __host__ __device__ unsigned* flagKV_() const { return flags_(); }
    __host__ __device__ unsigned* flagQ_() const { return flags_() + 1024; }
    __host__ __device__ unsigned* cntH_() const { return flags_() + 1536; }
    __host__ __device__ unsigned* flagG_() const { return flags_() + 1600; }
    __host__ __device__ unsigned* flagZ_() const { return flags_() + 1792; }
    __host__ __device__ unsigned* flagM_() const { return (unsigned*)(base + OFF_flagMr); }
    __host__ __device__ unsigned* cntO_() const { return flags_() + 4096; }
    __host__ __device__ unsigned long long* dbg_() const { return (unsigned long long*)(base + OFF_dbg); }
};

__device__ __forceinline__ float wave_sum(float v) {
#pragma unroll
    for (int m = 32; m >= 1; m >>= 1) v += __shfl_xor(v, m);
    return v;
}
__device__ __forceinline__ void ln8(float (&v)[8]) {
    float s = 0.f;
#pragma unroll
    for (int i = 0; i < 8; ++i) s += v[i];
    s = wave_sum(s);
    float mean = s * (1.f / 512.f);
    float q = 0.f;
#pragma unroll
    for (int i = 0; i < 8; ++i) { float d = v[i] - mean; q += d * d; }
    q = wave_sum(q);
    float var = rbf(q * (1.f / 512.f));
    float mb = rbf(mean);
    float sd = rbf(sqrtf(rbf(var + 1e-5f)));
#pragma unroll
    for (int i = 0; i < 8; ++i) v[i] = rbf(rbf(v[i] - mb) / sd);
}
template <class F>
__device__ __forceinline__ void frag_tile(const u16* __restrict__ src, int R, int C, int bx, int by, u16 (*t)[72], F addr) {
    const int c0 = bx * 64, r0 = by * 64;
#pragma unroll
    for (int i = 0; i < 2; ++i) {
        int idx = threadIdx.x + i * 256, rr = idx >> 3, cc = (idx & 7) * 8;
        int r = r0 + rr, c = c0 + cc;
        u32x4 v = {0u, 0u, 0u, 0u};
        if (r < R && c < C) v = *(const u32x4*)(src + (size_t)r * C + c);
        *(u32x4*)(&t[rr][cc]) = v;
    }
    __syncthreads();
    int l = threadIdx.x & 63, q = threadIdx.x >> 6;
#pragma unroll
    for (int i = 0; i < 2; ++i) {
        int f = q * 2 + i, ksl = f >> 2, ct = f & 3;
        u16 v[8];
#pragma unroll
        for (int j = 0; j < 8; ++j) v[j] = t[ksl * 32 + (l >> 4) * 8 + j][ct * 16 + (l & 15)];
        u32x4 o = {(unsigned)v[0] | ((unsigned)v[1] << 16), (unsigned)v[2] | ((unsigned)v[3] << 16), (unsigned)v[4] | ((unsigned)v[5] << 16), (unsigned)v[6] | ((unsigned)v[7] << 16)};
        *(u32x4*)(addr(ksl, ct) + l * 8) = o;
    }
}
#define PREP_LN 512
#define PREP_TQKV 1536
#define PREP_TWO 512
#define PREP_TW1 176
#define PREP_TW2 176
#define ZERO_BYTES ((size_t)68 * 16384 + 2 * 65536)
#define PREP_ZERO ((int)(ZERO_BYTES / 65536))
__global__ void __launch_bounds__(256) prep_k(const u16* __restrict__ x, const u16* __restrict__ Wq, const u16* __restrict__ Wk, const u16* __restrict__ Wv,
                                              const u16* __restrict__ Wo, const u16* __restrict__ w1, const u16* __restrict__ w2, WS ws) {
    __shared__ __attribute__((aligned(16))) u16 t[64][72];
    int b = blockIdx.x;
    if (b < PREP_LN) {
        int row = b * 4 + (threadIdx.x >> 6);
        int lane = threadIdx.x & 63;
        u32x4 r = *(const u32x4*)(x + (size_t)row * DM + lane * 8);
        float v[8];
#pragma unroll
        for (int i = 0; i < 4; ++i) { v[2 * i] = bf2f(r[i] & 0xffff); v[2 * i + 1] = bf2f(r[i] >> 16); }
        ln8(v);
        u32x4 o;
#pragma unroll
        for (int i = 0; i < 4; ++i) o[i] = pack2(v[2 * i], v[2 * i + 1]);
        const size_t fo = ((size_t)((row >> 4) * 16 + (lane >> 2)) * 64 + (lane & 3) * 16 + (row & 15)) * 8;
        *(u32x4*)(ws.xnF_() + fo) = o;
        ln8(v);
#pragma unroll
        for (int i = 0; i < 4; ++i) o[i] = pack2(v[2 * i], v[2 * i + 1]);
        *(u32x4*)(ws.yF_() + fo) = o;
        return;
    }
    b -= PREP_LN;
    if (b < PREP_TQKV) {
        int m = b / 512, r = b % 512;
        const u16* src = m == 0 ? Wq : (m == 1 ? Wk : Wv);
        const int bx = r % 64, by = r / 64;
        frag_tile(src, DM, HD, bx, by, t, [&](int ksl, int ct) {
            int n = bx * 64 + ct * 16, ks = by * 2 + ksl;
            int hh = n >> 7, ww = (n >> 5) & 3, tt = (n >> 4) & 1;
            return ws.WqkvF_() + ((size_t)(((hh * 4 + ww) * 6 + m * 2 + tt) * 16 + ks)) * 512;
        });
        return;
    }
    b -= PREP_TQKV;
    if (b < PREP_TWO) {
        const int bx = b % 8, by = b / 8;
        frag_tile(Wo, HD, DM, bx, by, t, [&](int ksl, int ct) {
            int tile = bx * 4 + ct, k = by * 64 + ksl * 32;
            return ws.WoF_() + ((size_t)((tile * 32 + (k >> 7)) * 4 + ((k >> 5) & 3))) * 512;
        });
        return;
    }
    b -= PREP_TWO;
    if (b < PREP_TW1) {
        const int bx = b % 22, by = b / 22;
        frag_tile(w1, DM, FF, bx, by, t, [&](int ksl, int ct) { return ws.W1F_() + ((size_t)((bx * 4 + ct) * 16 + by * 2 + ksl)) * 512; });
        return;
    }
    b -= PREP_TW1;
    if (b < PREP_TW2) {
        const int bx = b % 8, by = b / 8;
        frag_tile(w2, FF, DM, bx, by, t, [&](int ksl, int ct) { return ws.W2F_() + ((size_t)((bx * 4 + ct) * 44 + by * 2 + ksl)) * 512; });
        return;
    }
    b -= PREP_TW2;
    {
        u32x4 z = {0u, 0u, 0u, 0u};
        u32x4* p = (u32x4*)(ws.base + (size_t)b * 65536) + threadIdx.x;
#pragma unroll
        for (int i = 0; i < 16; ++i) p[i * 256] = z;
    }
}

#define SCALE 0.08838834764831845f
#define MEM_STRIDE 1056
#define QK_STRIDE 288

typedef __amdgpu_buffer_rsrc_t rsrc_t;
__device__ __forceinline__ rsrc_t mk_rsrc(const void* p) { return __builtin_amdgcn_make_buffer_rsrc((void*)p, 0, 0x7fffffff, 0x00020000); }
__device__ __forceinline__ u32x4 bld16(rsrc_t r, unsigned vo, unsigned so) { return __builtin_amdgcn_raw_buffer_load_b128(r, vo, so, 0); }
__device__ __forceinline__ u32x4 bld16_sc1(rsrc_t r, unsigned vo, unsigned so) { return __builtin_amdgcn_raw_buffer_load_b128(r, vo, so, 16); }
__device__ __forceinline__ u32x2 bld8(rsrc_t r, unsigned vo, unsigned so) { return __builtin_amdgcn_raw_buffer_load_b64(r, vo, so, 0); }
__device__ __forceinline__ void bst16_sc1(rsrc_t r, unsigned vo, unsigned so, u32x4 v) { __builtin_amdgcn_raw_buffer_store_b128(v, r, vo, so, 16); }
__device__ __forceinline__ void bst8(rsrc_t r, unsigned vo, unsigned so, u32x2 v) { __builtin_amdgcn_raw_buffer_store_b64(v, r, vo, so, 0); }
__device__ __forceinline__ void bst8_sc1(rsrc_t r, unsigned vo, unsigned so, u32x2 v) { __builtin_amdgcn_raw_buffer_store_b64(v, r, vo, so, 16); }

#define STAMP(i)
#define MFMA0_A(acc, a, b) asm volatile("v_mfma_f32_16x16x32_bf16 %0, %1, %2, 0" : "=&v"(acc) : "a"(a), "v"(b))
#define MFMA_A(acc, a, b) asm volatile("v_mfma_f32_16x16x32_bf16 %0, %1, %2, %0" : "+v"(acc) : "a"(a), "v"(b))
#define MFMA0_V(acc, a, b) asm volatile("v_mfma_f32_16x16x32_bf16 %0, %1, %2, 0" : "=&v"(acc) : "v"(a), "v"(b))
#define MFMA_V(acc, a, b) asm volatile("v_mfma_f32_16x16x32_bf16 %0, %1, %2, %0" : "+v"(acc) : "v"(a), "v"(b))
#define BARRIER() asm volatile("s_waitcnt lgkmcnt(0)\n\ts_barrier" ::: "memory")
#define LAS __attribute__((address_space(3)))

__device__ __forceinline__ float xmax16(float x) {
    unsigned u = __float_as_uint(x);
    auto r = __builtin_amdgcn_permlane16_swap(u, u, false, false);
    return fmaxf(__uint_as_float(r[0]), __uint_as_float(r[1]));
}
__device__ __forceinline__ float xmax32(float x) {
    unsigned u = __float_as_uint(x);
    auto r = __builtin_amdgcn_permlane32_swap(u, u, false, false);
    return fmaxf(__uint_as_float(r[0]), __uint_as_float(r[1]));
}
__device__ __forceinline__ float xsum16(float x) {
    unsigned u = __float_as_uint(x);
    auto r = __builtin_amdgcn_permlane16_swap(u, u, false, false);
    return __uint_as_float(r[0]) + __uint_as_float(r[1]);
}
__device__ __forceinline__ float xsum32(float x) {
    unsigned u = __float_as_uint(x);
    auto r = __builtin_amdgcn_permlane32_swap(u, u, false, false);
    return __uint_as_float(r[0]) + __uint_as_float(r[1]);
}
__device__ __forceinline__ unsigned ror8u(unsigned x) { return __builtin_amdgcn_update_dpp(0u, x, 0x128, 0xf, 0xf, false); }
__device__ __forceinline__ float ror8(float x) { return __uint_as_float(ror8u(__float_as_uint(x))); }
__device__ __forceinline__ float xsum8(float x) {
    return x + __uint_as_float(__builtin_amdgcn_update_dpp(0u, __float_as_uint(x), 0x128, 0xf, 0xf, false));
}

#define DELAY_A 10
#define DELAY_B 10
#define EXA_STEP 524288u
#define EXB_STEP 16384u
__device__ __forceinline__ void chain_body(const u16* __restrict__ mem0, const WS& ws, char* smem, const int h) {
    char* sW = smem;
    char* sKV = smem + 131072;
    char* sMem = smem + 147456;
    char* sQK = sMem + 8 * MEM_STRIDE;
    char* sO = sQK + 16 * QK_STRIDE;
    const int tid = threadIdx.x, lane = tid & 63;
    const int w = __builtin_amdgcn_readfirstlane(tid >> 6);
    const int fr = lane & 15, fg = lane >> 4, fr8 = lane & 7;
    const unsigned l16 = lane * 16;
    char* lw = sW + w * 32768 + l16;

    bf16x8 wqk[3][16], wv[24], wo[24];
#pragma unroll
    for (int t = 0; t < 6; ++t) {
        const u16* p = ws.WqkvF_() + ((size_t)((h * 4 + w) * 6 + t) * 16) * 512 + lane * 8;
#pragma unroll
        for (int ks = 0; ks < 16; ++ks) {
            bf16x8 v = *(const bf16x8*)(p + ks * 512);
            if (t < 3) wqk[t][ks] = v;
            else if (t == 3) *(bf16x8*)(lw + ks * 1024) = v;
            else if (t == 4) wv[ks] = v;
            else if (ks < 8) wv[16 + ks] = v;
            else *(bf16x8*)(lw + (16 + ks - 8) * 1024) = v;
        }
    }
#pragma unroll
    for (int t = 0; t < 8; ++t)
#pragma unroll
        for (int ks = 0; ks < 4; ++ks) {
            bf16x8 v = *(const bf16x8*)(ws.WoF_() + ((size_t)(((w * 8 + t) * 32 + h) * 4 + ks)) * 512 + lane * 8);
            if (t < 6) wo[t * 4 + ks] = v;
            else *(bf16x8*)(lw + (24 + (t - 6) * 4 + ks) * 1024) = v;
        }
    for (int i = tid; i < 8 * 64; i += 256) {
        int row = i >> 6, c = (i & 63) * 8;
        *(u32x4*)(sMem + row * MEM_STRIDE + c * 2) = *(const u32x4*)(mem0 + row * DM + c);
    }
    const rsrc_t rW = mk_rsrc(ws.base);
    const unsigned stA_vo = (unsigned)fg * 128 + (fr & 7) * 16 + (fr >> 3) * 16384;
    const unsigned rdA_vo = (unsigned)(lane >> 3) * 512 + (lane & 7) * 16;
    const char* memrd = sMem + fr8 * MEM_STRIDE + fg * 16;
    const char* qrd = sQK + fr8 * QK_STRIDE + fg * 16;
    const char* ord_ = sO + fr8 * QK_STRIDE + fg * 16;
    char* memwr = sMem + (lane & 7) * MEM_STRIDE + ((w * 2 + (lane >> 5)) * 16 + ((lane >> 3) & 3) * 4) * 2;

    for (int s = 0; s < NSTEPS; ++s) {
        STAMP(0);
        if ((s & 3) == 0) {
            const unsigned* f = (lane & 4) ? (ws.flagZ_() + s + (lane & 3)) : (ws.flagKV_() + (s >> 2) * 64 + (lane & 1) * 32 + h);
            for (unsigned spin = 0; spin < (1u << 22); ++spin) {
                unsigned v = __hip_atomic_load(f, __ATOMIC_RELAXED, __HIP_MEMORY_SCOPE_AGENT);
                if (__all(v != 0)) break;
                __builtin_amdgcn_s_sleep(2);
            }
        }
        {
            const u16* gk = ws.K_() + (size_t)(s * NH + h) * 4096 + w * 1024 + lane * 8;
            const u16* gv = ws.VT_() + (size_t)(s * NH + h) * 4096 + w * 1024 + lane * 8;
            __builtin_amdgcn_global_load_lds((const unsigned*)gk, (LAS unsigned*)(sKV + w * 2048), 16, 0, 16);
            __builtin_amdgcn_global_load_lds((const unsigned*)(gk + 512), (LAS unsigned*)(sKV + w * 2048 + 1024), 16, 0, 16);
            __builtin_amdgcn_global_load_lds((const unsigned*)gv, (LAS unsigned*)(sKV + 8192 + w * 2048), 16, 0, 16);
            __builtin_amdgcn_global_load_lds((const unsigned*)(gv + 512), (LAS unsigned*)(sKV + 8192 + w * 2048 + 1024), 16, 0, 16);
        }
        if (s == 0) {
            asm volatile("s_waitcnt vmcnt(0)" ::: "memory");
        } else {
#pragma unroll 1
            for (int d = 0; d < DELAY_B; ++d) __builtin_amdgcn_s_sleep(1);
            u32x4 vv[4];
            for (unsigned spin = 0;; ++spin) {
                if (spin > (1u << 20)) break;
                asm volatile("" ::: "memory");
#pragma unroll
                for (int i = 0; i < 4; ++i) vv[i] = bld16_sc1(rW, l16, ((unsigned)(i * 4096 + w * 1024) + (unsigned)s * EXB_STEP) + (unsigned)OFF_exB);
                unsigned am_ = 1u;
#pragma unroll
                for (int i = 0; i < 4; ++i) am_ &= (vv[i][0] & vv[i][1]) & (vv[i][2] & vv[i][3]);
                if (__all(am_ != 0)) break;
            }
#pragma unroll
            for (int i = 0; i < 4; ++i) {
                u32x2 o = {(vv[i][0] >> 16) | (vv[i][1] & 0xffff0000u), (vv[i][2] >> 16) | (vv[i][3] & 0xffff0000u)};
                *(u32x2*)(memwr + i * 256) = o;
            }
        }
        bf16x8 kw[2], vw[2];
#pragma unroll
        for (int i = 0; i < 2; ++i) kw[i] = *(const bf16x8*)(lw + i * 1024);
        vw[0] = *(const bf16x8*)(lw + 16 * 1024); vw[1] = *(const bf16x8*)(lw + 17 * 1024);
        STAMP(1);
        BARRIER();
        STAMP(2);

        f32x4 acc[4], av[2];
        {
            bf16x8 mb[3];
#pragma unroll
            for (int i = 0; i < 3; ++i) mb[i] = *(const bf16x8*)(memrd + i * 64);
#pragma unroll
            for (int ks = 0; ks < 16; ++ks) {
                bf16x8 m = mb[ks % 3];
                bf16x8 k3 = kw[ks & 1];
                bf16x8 v5 = (ks < 8) ? wv[16 + ks] : vw[ks & 1];
                if (ks == 0) {
                    MFMA0_A(acc[0], wqk[0][ks], m); MFMA0_A(acc[1], wqk[1][ks], m); MFMA0_A(acc[2], wqk[2][ks], m); MFMA0_V(acc[3], k3, m);
                    MFMA0_V(av[0], m, wv[ks]); MFMA0_V(av[1], m, v5);
                } else {
                    MFMA_A(acc[0], wqk[0][ks], m); MFMA_A(acc[1], wqk[1][ks], m); MFMA_A(acc[2], wqk[2][ks], m); MFMA_V(acc[3], k3, m);
                    MFMA_V(av[0], m, wv[ks]); MFMA_V(av[1], m, v5);
                }
                if (ks + 3 < 16) mb[ks % 3] = *(const bf16x8*)(memrd + (ks + 3) * 64);
                if (ks + 2 < 16) kw[ks & 1] = *(const bf16x8*)(lw + (ks + 2) * 1024);
                if (ks >= 8 && ks + 2 < 16) vw[ks & 1] = *(const bf16x8*)(lw + (16 + ks + 2 - 8) * 1024);
            }
        }
        STAMP(3);
        asm volatile("s_nop 7" : "+v"(acc[0]), "+v"(acc[1]), "+v"(acc[2]), "+v"(acc[3]), "+v"(av[0]), "+v"(av[1]));
        u32x2 o[4];
        {
#pragma unroll
            for (int t = 0; t < 4; ++t) o[t] = (u32x2){pack2(acc[t][0], acc[t][1]), pack2(acc[t][2], acc[t][3])};
            if (fr < 8) {
#pragma unroll
                for (int t = 0; t < 4; ++t) {
                    int row = (t >> 1) * 8 + fr;
                    int c = w * 32 + (t & 1) * 16 + fg * 4;
                    *(u32x2*)(sQK + row * QK_STRIDE + c * 2) = o[t];
                }
            }
        }
        u32x4 am[2], ax[2];
#pragma unroll
        for (int t = 0; t < 2; ++t) {
            am[t] = (u32x4){pack2(av[t][0], av[t][1]), pack2(av[t][2], av[t][3]), 0u, 0u};
            if (fg >= 2) { am[t][0] = 0u; am[t][1] = 0u; }
        }
        bf16x8 kx[2][4];
#pragma unroll
        for (int ks = 0; ks < 4; ++ks) {
            kx[0][ks] = *(const bf16x8*)(sKV + ks * 1024 + l16);
            kx[1][ks] = *(const bf16x8*)(sKV + 4096 + ks * 1024 + l16);
        }
        ax[0] = *(const u32x4*)(sKV + 8192 + (w * 2) * 1024 + l16);
        ax[1] = *(const u32x4*)(sKV + 8192 + (w * 2 + 1) * 1024 + l16);
        STAMP(4);
        BARRIER();
        STAMP(5);
        f32x4 sm, sx0, sx1;
        {
            bf16x8 qf[4], kf[4];
#pragma unroll
            for (int ks = 0; ks < 4; ++ks) {
                qf[ks] = *(const bf16x8*)(qrd + ks * 64);
                kf[ks] = *(const bf16x8*)(qrd + 8 * QK_STRIDE + ks * 64);
            }
            MFMA0_V(sx0, kx[0][0], qf[0]); MFMA0_V(sx1, kx[1][0], qf[0]); MFMA0_V(sm, kf[0], qf[0]);
#pragma unroll
            for (int ks = 1; ks < 4; ++ks) { MFMA_V(sx0, kx[0][ks], qf[ks]); MFMA_V(sx1, kx[1][ks], qf[ks]); MFMA_V(sm, kf[ks], qf[ks]); }
        }
        STAMP(10);
        if (fr < 8) {
                bst8_sc1(rW, (unsigned)fr * 256 + fg * 8, ((unsigned)((s * NH + h) * 2048 + (w * 32) * 2)) + (unsigned)OFF_km, o[2]);
                bst8_sc1(rW, (unsigned)fr * 256 + fg * 8, ((unsigned)((s * NH + h) * 2048 + (w * 32 + 16) * 2)) + (unsigned)OFF_km, o[3]);
        }
        if (fg < 2) {
            bst8_sc1(rW, (unsigned)fr * 16 + fg * 8, ((unsigned)((s * NH + h) * 2048 + (w * 32) * 16)) + (unsigned)OFF_vmT, (u32x2){am[0][0], am[0][1]});
            bst8_sc1(rW, (unsigned)fr * 16 + fg * 8, ((unsigned)((s * NH + h) * 2048 + (w * 32 + 16) * 16)) + (unsigned)OFF_vmT, (u32x2){am[1][0], am[1][1]});
        }
        bf16x8 wf[8];
#pragma unroll
        for (int i = 0; i < 8; ++i) wf[i] = *(const bf16x8*)(lw + (24 + i) * 1024);
        asm volatile("s_nop 7" : "+v"(sm), "+v"(sx0), "+v"(sx1));
        const bool hi = fr >= 8;
        const f32x2 sc2 = {SCALE, SCALE};
        f32x2 p0 = {hi ? sx1[0] : sx0[0], hi ? sx1[1] : sx0[1]};
        f32x2 p1 = {hi ? sx1[2] : sx0[2], hi ? sx1[3] : sx0[3]};
        f32x2 p2 = {hi ? sm[2] : sm[0], hi ? sm[3] : sm[1]};
        p0 = rbf2(rbf2(p0) * sc2);
        p1 = rbf2(rbf2(p1) * sc2);
        p2 = rbf2(rbf2(p2) * sc2);
        if (fg >= 2) p2 = (f32x2){-3.0e38f, -3.0e38f};
        float mx = fmaxf(fmaxf(fmaxf(p0[0], p0[1]), fmaxf(p1[0], p1[1])), fmaxf(p2[0], p2[1]));
        mx = fmaxf(mx, ror8(mx));
        mx = xmax16(mx);
        mx = xmax32(mx);
        const float L2E = 1.4426950408889634f;
        const f32x2 l2 = {L2E, L2E};
        const f32x2 mxl = {mx, mx};
        p0 = (p0 - mxl) * l2; p1 = (p1 - mxl) * l2; p2 = (p2 - mxl) * l2;
        p0 = (f32x2){__builtin_amdgcn_exp2f(p0[0]), __builtin_amdgcn_exp2f(p0[1])};
        p1 = (f32x2){__builtin_amdgcn_exp2f(p1[0]), __builtin_amdgcn_exp2f(p1[1])};
        p2 = (f32x2){__builtin_amdgcn_exp2f(p2[0]), __builtin_amdgcn_exp2f(p2[1])};
        f32x2 s2 = (p0 + p1) + p2;
        float sum = s2[0] + s2[1];
        sum = sum + ror8(sum);
        sum = xsum16(sum);
        sum = xsum32(sum);
        float inv = __builtin_amdgcn_rcpf(sum);
        const f32x2 iv = {inv, inv};
        p0 *= iv; p1 *= iv; p2 *= iv;
        unsigned d0 = pack2(p0[0], p0[1]), d1 = pack2(p1[0], p1[1]), d2 = pack2(p2[0], p2[1]);
        unsigned e0 = ror8u(d0), e1 = ror8u(d1), e2 = ror8u(d2);
        u32x4 px = {d0, d1, e0, e1};
        u32x4 pm = {d2, e2, 0u, 0u};
        STAMP(12);
        f32x4 o0, o1;
        asm volatile("s_nop 1" : "+v"(px), "+v"(pm), "+v"(am[0]), "+v"(am[1]), "+v"(ax[0]), "+v"(ax[1]));
        MFMA0_V(o0, ax[0], px); MFMA0_V(o1, ax[1], px);
        MFMA_V(o0, am[0], pm); MFMA_V(o1, am[1], pm);
        asm volatile("s_nop 7" : "+v"(o0), "+v"(o1));
        if (fr < 8) {
            *(u32x2*)(sO + fr * QK_STRIDE + (w * 32 + fg * 4) * 2) = (u32x2){pack2(o0[0], o0[1]), pack2(o0[2], o0[3])};
            *(u32x2*)(sO + fr * QK_STRIDE + (w * 32 + 16 + fg * 4) * 2) = (u32x2){pack2(o1[0], o1[1]), pack2(o1[2], o1[3])};
        }
        STAMP(6);
        BARRIER();
        STAMP(7);
        bf16x8 of[4];
#pragma unroll
        for (int ks = 0; ks < 4; ++ks) of[ks] = *(const bf16x8*)(ord_ + ks * 64);
        {
            f32x4 a[4];
#pragma unroll
            for (int p = 0; p <= 4; ++p) {
                if (p < 4) {
#pragma unroll
                    for (int u = 0; u < 2; ++u) {
                        const int t = 2 * p + u;
                        f32x4& ac = a[(p & 1) * 2 + u];
#pragma unroll
                        for (int ks = 0; ks < 4; ++ks) {
                            if (t < 4) { if (ks == 0) MFMA0_A(ac, wo[t * 4 + ks], of[ks]); else MFMA_A(ac, wo[t * 4 + ks], of[ks]); }
                            else {
                                bf16x8 f = (t < 6) ? wo[t * 4 + ks] : wf[(t - 6) * 4 + ks];
                                if (ks == 0) MFMA0_V(ac, f, of[ks]); else MFMA_V(ac, f, of[ks]);
                            }
                        }
                    }
                }
                if (p >= 1) {
                    const int q0 = ((p - 1) & 1) * 2;
                    if (p == 4) asm volatile("s_nop 7" : "+v"(a[q0]), "+v"(a[q0 + 1]));
                    u32x4 o;
#pragma unroll
                    for (int q = 0; q < 4; ++q) o[q] = __float_as_uint(fr < 8 ? a[q0][q] : a[q0 + 1][q]) | 1u;
                    bst16_sc1(rW, stA_vo, ((unsigned)(((w * 8 + 2 * (p - 1)) * NH + h) * 512) + (unsigned)s * EXA_STEP) + (unsigned)OFF_exA, o);
                }
            }
        }
        STAMP(8);
                if (s > 0 && tid == 192) __builtin_amdgcn_raw_buffer_store_b32(1u, rW, 0, (unsigned)OFF_flagMr + (unsigned)(((s - 1) * 8 + (h >> 2)) * 32 + (h & 3)) * 4u, 16);
        {
#pragma unroll 1
            for (int d = 0; d < DELAY_A; ++d) __builtin_amdgcn_s_sleep(1);
            float tot[4];
            for (unsigned spin = 0;; ++spin) {
                if (spin > (1u << 20)) break;
                asm volatile("" ::: "memory");
                u32x4 v[4];
#pragma unroll
                for (int i = 0; i < 4; ++i) v[i] = bld16_sc1(rW, rdA_vo, ((unsigned)(h * NH * 512 + i * 4096 + w * 128) + (unsigned)s * EXA_STEP) + (unsigned)OFF_exA);
                bool ok = true;
#pragma unroll
                for (int q = 0; q < 4; ++q) {
                    ok &= ((v[0][q] & v[1][q] & v[2][q] & v[3][q] & 1u) != 0);
                    tot[q] = (__uint_as_float(v[0][q]) + __uint_as_float(v[1][q])) + (__uint_as_float(v[2][q]) + __uint_as_float(v[3][q]));
                }
                if (__all(ok)) break;
            }
#pragma unroll
            for (int q = 0; q < 4; ++q) {
                tot[q] = xsum8(tot[q]);
                tot[q] = xsum16(tot[q]);
                tot[q] = xsum32(tot[q]);
            }
            STAMP(9);
            if (lane < 8) {
                u32x4 o;
#pragma unroll
                for (int q = 0; q < 4; ++q) o[q] = ((unsigned)f2bf(tot[q]) << 16) | 1u;
                bst16_sc1(rW, l16, ((unsigned)(h * 512 + w * 128) + (unsigned)(s + 1) * EXB_STEP) + (unsigned)OFF_exB, o);
            }
        }
    }
    asm volatile("s_waitcnt vmcnt(0)" ::: "memory");
    __syncthreads();
    if (tid == 0) __builtin_amdgcn_raw_buffer_store_b32(1u, rW, 0, (unsigned)OFF_flagMr + (unsigned)(((NSTEPS - 1) * 8 + (h >> 2)) * 32 + (h & 3)) * 4u, 16);
}

enum { EPI_QKV = 0, EPI_H = 1, EPI_G = 2 };
__device__ __forceinline__ float gelu_tanh(float v) {
    float u = 0.7978845608028654f * (v + 0.044715f * v * v * v);
    return 0.5f * v * (1.f + tanhf(u));
}
__device__ __forceinline__ void wait_flag_ge(const unsigned* f, unsigned want) {
    for (unsigned spin = 0; spin < (1u << 22); ++spin) {
        unsigned v = __hip_atomic_load(f, __ATOMIC_RELAXED, __HIP_MEMORY_SCOPE_AGENT);
        if (__all(v >= want)) break;
        __builtin_amdgcn_s_sleep(64);
    }
}
__device__ __forceinline__ void acquire_all() {
    __builtin_amdgcn_fence(__ATOMIC_ACQUIRE, "agent");
    asm volatile("s_waitcnt vmcnt(0)" ::: "memory");
    __syncthreads();
}
__device__ __forceinline__ void publish_done() {
    asm volatile("s_waitcnt vmcnt(0)" ::: "memory");
    __syncthreads();
}

template <int EPI, int NKS>
__device__ __forceinline__ void gemm_tile(unsigned offA, int tb, int cb, const WS& ws, const u16* __restrict__ bias, const int tidx) {
    const int lane = tidx & 63, w = __builtin_amdgcn_readfirstlane(tidx >> 6);
    const int wr = w >> 1, wc = w & 1;
    const int fr = lane & 15, fg = lane >> 4;
    const rsrc_t rW = mk_rsrc(ws.base);
    const unsigned l16 = lane * 16;
    const bool vtile = (EPI == EPI_QKV) && (cb >= 64);
    unsigned ao[4], bo[4];
#pragma unroll
    for (int i = 0; i < 4; ++i) {
        ao[i] = offA + (unsigned)((tb * 8 + wr * 4 + i) * NKS) * 1024u;
        int ct = cb * 8 + wc * 4 + i;
        if (EPI == EPI_QKV) {
            int m = ct >> 8, r = ct & 255;
            bo[i] = (unsigned)OFF_WqkvF + (unsigned)((((r >> 3) * 4 + ((r >> 1) & 3)) * 6 + m * 2 + (r & 1)) * 16) * 1024u;
        } else if (EPI == EPI_H) bo[i] = (unsigned)OFF_W1F + (unsigned)(ct * 16) * 1024u;
        else bo[i] = (unsigned)OFF_W2F + (unsigned)(ct * 44) * 1024u;
    }
    f32x4 acc[4][4];
#pragma unroll
    for (int i = 0; i < 4; ++i)
#pragma unroll
        for (int j = 0; j < 4; ++j) acc[i][j] = (f32x4){0.f, 0.f, 0.f, 0.f};
    constexpr int D = 4;
    u32x4 fa[D][4], fb[D][4];
#pragma unroll
    for (int d = 0; d < D; ++d)
#pragma unroll
        for (int i = 0; i < 4; ++i) { fa[d][i] = bld16(rW, l16, ao[i] + d * 1024u); fb[d][i] = bld16(rW, l16, bo[i] + d * 1024u); }
    for (int k0 = 0; k0 < NKS; k0 += D) {
#pragma unroll
        for (int d = 0; d < D; ++d) {
            u32x4 a[4], bq[4];
#pragma unroll
            for (int i = 0; i < 4; ++i) { a[i] = fa[d][i]; bq[i] = fb[d][i]; }
            const int kn = k0 + d + D;
            if (kn < NKS) {
#pragma unroll
                for (int i = 0; i < 4; ++i) { fa[d][i] = bld16(rW, l16, ao[i] + (unsigned)kn * 1024u); fb[d][i] = bld16(rW, l16, bo[i] + (unsigned)kn * 1024u); }
            }
            if (vtile) {
#pragma unroll
                for (int i = 0; i < 4; ++i)
#pragma unroll
                    for (int j = 0; j < 4; ++j) acc[i][j] = __builtin_amdgcn_mfma_f32_16x16x32_bf16(__builtin_bit_cast(bf16x8, a[i]), __builtin_bit_cast(bf16x8, bq[j]), acc[i][j], 0, 0, 0);
            } else {
#pragma unroll
                for (int i = 0; i < 4; ++i)
#pragma unroll
                    for (int j = 0; j < 4; ++j) acc[i][j] = __builtin_amdgcn_mfma_f32_16x16x32_bf16(__builtin_bit_cast(bf16x8, bq[j]), __builtin_bit_cast(bf16x8, a[i]), acc[i][j], 0, 0, 0);
            }
        }
    }
    const int brow = tb * 128, bcol = cb * 128;
#pragma unroll
    for (int i = 0; i < 4; ++i)
#pragma unroll
        for (int j = 0; j < 4; ++j) {
            f32x4 a = acc[i][j];
            if (vtile) {
                int col = bcol + wc * 64 + j * 16 + fr - 2 * HD;
                int tok = brow + wr * 64 + i * 16 + fg * 4;
                int seg = tok >> 5, tt = (tok >> 4) & 1, hh = col >> 7, et = (col >> 4) & 7;
                u32x2 o = {pack2(a[0], a[1]), pack2(a[2], a[3])};
                bst8_sc1(rW, (unsigned)((((seg * NH + hh) * 8 + et) * 512 + (fg * 16 + fr) * 8 + tt * 4) * 2), (unsigned)OFF_VT, o);
            } else {
                int tok = brow + wr * 64 + i * 16 + fr;
                int col = bcol + wc * 64 + j * 16 + fg * 4;
                if (EPI == EPI_QKV) {
                    int cc = col & (HD - 1);
                    int seg = tok >> 5, tt = (tok >> 4) & 1, hh = cc >> 7, e = cc & 127;
                    u32x2 o = {pack2(a[0], a[1]), pack2(a[2], a[3])};
                    unsigned off = (unsigned)((((seg * NH + hh) * 8 + tt * 4 + (e >> 5)) * 512 + (((e >> 3) & 3) * 16 + (tok & 15)) * 8 + (e & 4)) * 2);
                    bst8_sc1(rW, off, (col >= HD) ? (unsigned)OFF_K : (unsigned)OFF_Q, o);
                } else if (EPI == EPI_H) {
                    float r[4];
#pragma unroll
                    for (int q = 0; q < 4; ++q) {
                        float b = (col + q < FF) ? bf2f(bias[col + q]) : 0.f;
                        float v = rbf(rbf(a[q]) + b);
                        r[q] = (col + q < FF) ? gelu_tanh(v) : 0.f;
                    }
                    u32x2 o = {pack2(r[0], r[1]), pack2(r[2], r[3])};
                    unsigned off = (unsigned)((((tok >> 4) * 44 + (col >> 5)) * 64 + ((col >> 3) & 3) * 16 + (tok & 15)) * 16 + (col & 4) * 2);
                    bst8_sc1(rW, off, (unsigned)OFF_hmidF, o);
                } else {
                    float r[4];
#pragma unroll
                    for (int q = 0; q < 4; ++q) {
                        float v = rbf(rbf(a[q]) + bf2f(bias[col + q]));
                        r[q] = v / (1.f + __expf(-v));
                    }
                    u32x2 o = {pack2(r[0], r[1]), pack2(r[2], r[3])};
                    bst8_sc1(rW, (unsigned)((tok * DM + col) * 2), (unsigned)OFF_g, o);
                }
            }
        }
}

__device__ __forceinline__ u32x2 bld8_sc1(rsrc_t r, unsigned vo, unsigned so) { return __builtin_amdgcn_raw_buffer_load_b64(r, vo, so, 16); }
__device__ __forceinline__ void attn_task(int s, int hg, const WS& ws, char* lds, const int tidx) {
    const int lane = tidx & 63, w = __builtin_amdgcn_readfirstlane(tidx >> 6);
    const int h = hg * 4 + w;
    const int fr = lane & 15, fg = lane >> 4, fr8 = lane & 7;
    char* sO = lds;
    const rsrc_t rW = mk_rsrc(ws.base);
    const unsigned l16 = lane * 16;
    const unsigned sh = (unsigned)(s * NH + h) * 8192u;
    char* ring = lds + 33280 + w * 24576;
    const unsigned wbase = (unsigned)OFF_WoF + (unsigned)((w * 8 * 32 + hg * 4) * 4) * 1024u;
    auto issue = [&](int ks) {
#pragma unroll
        for (int t = 0; t < 8; ++t)
            __builtin_amdgcn_raw_ptr_buffer_load_lds(rW, (LAS void*)(ring + ((ks % 3) * 8 + t) * 1024), 16, l16, wbase + (unsigned)(t * 128 + ks) * 1024u, 0, 0);
    };
    issue(0); issue(1); issue(2);
    u32x4 ax[8];
    f32x4 sx0[2], sx1[2];
    u32x4 qf[2][4];
    {
        u32x4 kx[2][4];
#pragma unroll
        for (int ks = 0; ks < 4; ++ks)
#pragma unroll
            for (int t = 0; t < 2; ++t) { kx[t][ks] = bld16(rW, l16, (unsigned)OFF_K + sh + (unsigned)(t * 4 + ks) * 1024u); qf[t][ks] = bld16(rW, l16, (unsigned)OFF_Q + sh + (unsigned)(t * 4 + ks) * 1024u); }
#pragma unroll
        for (int et = 0; et < 8; ++et) ax[et] = bld16(rW, l16, (unsigned)OFF_VT + sh + (unsigned)et * 1024u);
#pragma unroll
        for (int qt = 0; qt < 2; ++qt) {
            sx0[qt] = (f32x4){0.f, 0.f, 0.f, 0.f}; sx1[qt] = sx0[qt];
#pragma unroll
            for (int ks = 0; ks < 4; ++ks) {
                sx0[qt] = __builtin_amdgcn_mfma_f32_16x16x32_bf16(__builtin_bit_cast(bf16x8, kx[0][ks]), __builtin_bit_cast(bf16x8, qf[qt][ks]), sx0[qt], 0, 0, 0);
                sx1[qt] = __builtin_amdgcn_mfma_f32_16x16x32_bf16(__builtin_bit_cast(bf16x8, kx[1][ks]), __builtin_bit_cast(bf16x8, qf[qt][ks]), sx1[qt], 0, 0, 0);
            }
        }
    }
    if (w == 0) {
        const unsigned* f = ws.flagM_() + (s * 8 + hg) * 32 + (lane & 3);
        for (unsigned spin = 0; spin < (1u << 22); ++spin) {
            unsigned v = __hip_atomic_load(f, __ATOMIC_RELAXED, __HIP_MEMORY_SCOPE_AGENT);
            if (__all(v != 0)) break;
            __builtin_amdgcn_s_sleep(16);
        }
    }
    asm volatile("" ::: "memory");
    __builtin_amdgcn_s_barrier();
    asm volatile("" ::: "memory");
    u32x4 kmf[4];
    u32x2 vm[8];
#pragma unroll
    for (int ks = 0; ks < 4; ++ks) kmf[ks] = bld16_sc1(rW, (unsigned)fr8 * 256 + fg * 16, (unsigned)OFF_km + (unsigned)(s * NH + h) * 2048u + ks * 64u);
#pragma unroll
    for (int et = 0; et < 8; ++et) {
        vm[et] = (u32x2){0u, 0u};
        if (fg < 2) vm[et] = bld8_sc1(rW, (unsigned)fr * 16 + fg * 8, (unsigned)OFF_vmT + (unsigned)(s * NH + h) * 2048u + et * 256u);
    }
    u32x4 px[2], pm[2];
#pragma unroll
    for (int qt = 0; qt < 2; ++qt) {
        f32x4 sm = {0.f, 0.f, 0.f, 0.f};
#pragma unroll
        for (int ks = 0; ks < 4; ++ks) sm = __builtin_amdgcn_mfma_f32_16x16x32_bf16(__builtin_bit_cast(bf16x8, kmf[ks]), __builtin_bit_cast(bf16x8, qf[qt][ks]), sm, 0, 0, 0);
        const int qi = qt * 16 + fr;
        float sv[12];
#pragma unroll
        for (int q = 0; q < 4; ++q) {
            sv[q] = (fg < 2) ? rbf(rbf(sm[q]) * SCALE) : -3.0e38f;
            int k0 = fg * 4 + q, k1 = 16 + fg * 4 + q;
            sv[4 + q] = (k0 <= qi) ? rbf(rbf(sx0[qt][q]) * SCALE) : -3.0e38f;
            sv[8 + q] = (k1 <= qi) ? rbf(rbf(sx1[qt][q]) * SCALE) : -3.0e38f;
        }
        float mx = sv[0];
#pragma unroll
        for (int q = 1; q < 12; ++q) mx = fmaxf(mx, sv[q]);
        mx = xmax16(mx); mx = xmax32(mx);
        float sum = 0.f;
#pragma unroll
        for (int q = 0; q < 12; ++q) { sv[q] = __expf(sv[q] - mx); sum += sv[q]; }
        sum = xsum16(sum); sum = xsum32(sum);
        float inv = 1.f / sum;
        px[qt] = (u32x4){pack2(sv[4] * inv, sv[5] * inv), pack2(sv[6] * inv, sv[7] * inv), pack2(sv[8] * inv, sv[9] * inv), pack2(sv[10] * inv, sv[11] * inv)};
        pm[qt] = (u32x4){pack2(sv[0] * inv, sv[1] * inv), pack2(sv[2] * inv, sv[3] * inv), 0u, 0u};
    }
#pragma unroll
    for (int et = 0; et < 8; ++et) {
        u32x4 am = {vm[et][0], vm[et][1], 0u, 0u};
#pragma unroll
        for (int qt = 0; qt < 2; ++qt) {
            f32x4 o = {0.f, 0.f, 0.f, 0.f};
            o = __builtin_amdgcn_mfma_f32_16x16x32_bf16(__builtin_bit_cast(bf16x8, ax[et]), __builtin_bit_cast(bf16x8, px[qt]), o, 0, 0, 0);
            o = __builtin_amdgcn_mfma_f32_16x16x32_bf16(__builtin_bit_cast(bf16x8, am), __builtin_bit_cast(bf16x8, pm[qt]), o, 0, 0, 0);
            u32x2 ov = {pack2(o[0], o[1]), pack2(o[2], o[3])};
            *(u32x2*)(sO + (qt * 16 + fr) * 1040 + (w * DH + et * 16 + fg * 4) * 2) = ov;
        }
    }
    asm volatile("s_waitcnt lgkmcnt(0)\n\ts_barrier" ::: "memory");
    f32x4 acc[8][2];
#pragma unroll
    for (int t = 0; t < 8; ++t) { acc[t][0] = (f32x4){0.f, 0.f, 0.f, 0.f}; acc[t][1] = acc[t][0]; }
    {
#pragma unroll
        for (int ks = 0; ks < 16; ++ks) {
            if (ks <= 13) asm volatile("s_waitcnt vmcnt(16)" ::: "memory");
            else if (ks == 14) asm volatile("s_waitcnt vmcnt(8)" ::: "memory");
            else asm volatile("s_waitcnt vmcnt(0)" ::: "memory");
            bf16x8 b0 = *(const bf16x8*)(sO + fr * 1040 + (ks * 32 + fg * 8) * 2);
            bf16x8 b1 = *(const bf16x8*)(sO + (16 + fr) * 1040 + (ks * 32 + fg * 8) * 2);
            bf16x8 a[8];
#pragma unroll
            for (int t = 0; t < 8; ++t) a[t] = *(const bf16x8*)(ring + ((ks % 3) * 8 + t) * 1024 + lane * 16);
            if (ks + 3 < 16) {
                asm volatile("s_waitcnt lgkmcnt(0)" ::: "memory");
                issue(ks + 3);
            }
#pragma unroll
            for (int t = 0; t < 8; ++t) {
                acc[t][0] = __builtin_amdgcn_mfma_f32_16x16x32_bf16(a[t], b0, acc[t][0], 0, 0, 0);
                acc[t][1] = __builtin_amdgcn_mfma_f32_16x16x32_bf16(a[t], b1, acc[t][1], 0, 0, 0);
            }
        }
    }
#pragma unroll
    for (int t = 0; t < 8; ++t)
#pragma unroll
        for (int qt = 0; qt < 2; ++qt) {
            u32x4 o = {__float_as_uint(acc[t][qt][0]), __float_as_uint(acc[t][qt][1]), __float_as_uint(acc[t][qt][2]), __float_as_uint(acc[t][qt][3])};
            unsigned off = (unsigned)((((s * 8 + hg) * 32 + qt * 16 + fr) * 512 + w * 128 + t * 16 + fg * 4) * 4);
            bst16_sc1(rW, off, (unsigned)OFF_part, o);
        }
}

__device__ __forceinline__ void reduce_task(int s, int r0, const u16* __restrict__ x, u16* __restrict__ out, const WS& ws, const int tidx) {
    const int i = tidx;
    {
        int row = r0 + (i >> 7), c = (i & 127) * 4;
        f32x4 t = {0.f, 0.f, 0.f, 0.f};
        f32x4 pv[8];
#pragma unroll
        for (int hg = 0; hg < 8; ++hg) pv[hg] = *(const f32x4*)(ws.part_() + ((size_t)(s * 8 + hg) * 32 + row) * 512 + c);
#pragma unroll
        for (int hg = 0; hg < 8; ++hg) t += pv[hg];
        int tok = s * SEG + row;
        u32x2 xv = *(const u32x2*)(x + (size_t)tok * DM + c);
        u32x2 gv = *(const u32x2*)(ws.g_() + (size_t)tok * DM + c);
        float xs[4] = {bf2f(xv[0] & 0xffff), bf2f(xv[0] >> 16), bf2f(xv[1] & 0xffff), bf2f(xv[1] >> 16)};
        float gs[4] = {bf2f(gv[0] & 0xffff), bf2f(gv[0] >> 16), bf2f(gv[1] & 0xffff), bf2f(gv[1] >> 16)};
        float r[4];
#pragma unroll
        for (int q = 0; q < 4; ++q) r[q] = xs[q] + rbf(rbf(t[q]) * gs[q]);
        u32x2 o = {pack2(r[0], r[1]), pack2(r[2], r[3])};
        *(u32x2*)(out + (size_t)tok * DM + c) = o;
    }
}

#define NWORK 224
#define T_KV0 64
#define T_Z 64
#define T_KV 960
#define T_H 176
#define T_Q 512
#define T_G 64
#define T_A 512
#define T_TOTAL (T_KV0 + T_Z + T_KV + T_H + T_Q + T_G + T_A)
#define LDS_BYTES 162816

__global__ void __launch_bounds__(256, 1) mega_k(const u16* __restrict__ x, const u16* __restrict__ mem0, const u16* __restrict__ b1, const u16* __restrict__ b2,
                                                 u16* __restrict__ out, WS ws) {
    extern __shared__ __attribute__((aligned(16))) char smem[];
    if (blockIdx.x < NH) {
        chain_body(mem0, ws, smem, blockIdx.x);
        return;
    }
    const int tid = threadIdx.x, lane = tid & 63, w = tid >> 6;
    for (int task = blockIdx.x - NH; task < T_TOTAL; task += NWORK) {
        int t = task;
        int tidx = threadIdx.x;
        asm volatile("" : "+v"(tidx));
        if (t < T_KV0 + T_Z + T_KV) {
            if (t >= T_KV0 && t < T_KV0 + T_Z) {
                const int z = t - T_KV0;
                const rsrc_t rW = mk_rsrc(ws.base);
                u32x4 zz = {0u, 0u, 0u, 0u};
#pragma unroll 8
                for (int i = 0; i < 128; ++i) bst16_sc1(rW, (unsigned)tidx * 16, (unsigned)OFF_exA + (unsigned)z * 524288u + (unsigned)i * 4096u, zz);
                publish_done();
                if (tid == 0) __hip_atomic_store(ws.flagZ_() + z, 1u, __ATOMIC_RELAXED, __HIP_MEMORY_SCOPE_AGENT);
                continue;
            }
            if (t >= T_KV0) t -= T_Z;
            int tb = t >> 6, cb = t & 63;
            gemm_tile<EPI_QKV, 16>((unsigned)OFF_xnF, tb, 32 + cb, ws, nullptr, tidx);
            publish_done();
            if (tid == 0) __hip_atomic_store(ws.flagKV_() + tb * 64 + cb, 1u, __ATOMIC_RELAXED, __HIP_MEMORY_SCOPE_AGENT);
            continue;
        }
        t -= T_KV0 + T_Z + T_KV;
        if (t < T_H) {
            int tb = t / 11, cb = t % 11;
            gemm_tile<EPI_H, 16>((unsigned)OFF_yF, tb, cb, ws, b1, tidx);
            publish_done();
            if (tid == 0) __hip_atomic_fetch_add(ws.cntH_() + tb, 1u, __ATOMIC_RELAXED, __HIP_MEMORY_SCOPE_AGENT);
            continue;
        }
        t -= T_H;
        if (t < T_Q) {
            int tb = t >> 5, cb = t & 31;
            gemm_tile<EPI_QKV, 16>((unsigned)OFF_xnF, tb, cb, ws, nullptr, tidx);
            publish_done();
            if (tid == 0) __hip_atomic_store(ws.flagQ_() + tb * 32 + cb, 1u, __ATOMIC_RELAXED, __HIP_MEMORY_SCOPE_AGENT);
            continue;
        }
        t -= T_Q;
        if (t < T_G) {
            int tb = t >> 2, cb = t & 3;
            if (w == 0) wait_flag_ge(ws.cntH_() + tb, 11u);
            acquire_all();
            gemm_tile<EPI_G, 44>((unsigned)OFF_hmidF, tb, cb, ws, b2, tidx);
            publish_done();
            if (tid == 0) __hip_atomic_store(ws.flagG_() + tb * 4 + cb, 1u, __ATOMIC_RELAXED, __HIP_MEMORY_SCOPE_AGENT);
            continue;
        }
        t -= T_G;
        {
            int s = t >> 3, hg = t & 7, tb = s >> 2;
            if (w == 0) {
                const unsigned* f;
                int l = lane & 15, hh = hg * 4 + (l & 3);
                if (l < 4) f = ws.flagKV_() + tb * 64 + hh;
                else if (l < 8) f = ws.flagKV_() + tb * 64 + 32 + hh;
                else f = ws.flagQ_() + tb * 32 + hh;
                wait_flag_ge(f, 1u);
            }
            acquire_all();
            attn_task(s, hg, ws, smem, tidx);
            publish_done();
            if (tid == 0) __hip_atomic_fetch_add(ws.cntO_() + s, 1u, __ATOMIC_RELAXED, __HIP_MEMORY_SCOPE_AGENT);
            if (w == 0) { wait_flag_ge(lane < 4 ? ws.flagG_() + tb * 4 + lane : ws.cntO_() + s, lane < 4 ? 1u : 8u); }
            acquire_all();
            reduce_task(s, hg * 4, x, out, ws, tidx);
            reduce_task(s, hg * 4 + 2, x, out, ws, tidx);
            __syncthreads();
        }
    }
}

extern "C" void kernel_launch(void* const* d_in, const int* in_sizes, int n_in, void* d_out, int out_size, void* d_ws, size_t ws_size,
                              hipStream_t stream) {
    const u16* x = (const u16*)d_in[0];
    const u16* mem0 = (const u16*)d_in[1];
    const u16* Wq = (const u16*)d_in[2];
    const u16* Wk = (const u16*)d_in[3];
    const u16* Wv = (const u16*)d_in[4];
    const u16* Wo = (const u16*)d_in[5];
    const u16* w1 = (const u16*)d_in[8];
    const u16* b1 = (const u16*)d_in[9];
    const u16* w2 = (const u16*)d_in[10];
    const u16* b2 = (const u16*)d_in[11];
    u16* out = (u16*)d_out;

    WS ws; ws.base = (char*)d_ws;

    const int nprep = PREP_LN + PREP_TQKV + PREP_TWO + PREP_TW1 + PREP_TW2 + PREP_ZERO;
    prep_k<<<nprep, 256, 0, stream>>>(x, Wq, Wk, Wv, Wo, w1, w2, ws);
    (void)hipFuncSetAttribute((const void*)mega_k, hipFuncAttributeMaxDynamicSharedMemorySize, LDS_BYTES);
    mega_k<<<256, 256, LDS_BYTES, stream>>>(x, mem0, b1, b2, out, ws);
}
```
